```python
import torch

def _softmax(x, axis=-1):
    return torch.softmax(x, dim=axis)
def _gelu(x):
    return torch.nn.functional.gelu(x, approximate="tanh")
def _silu(x):
    return torch.nn.functional.silu(x)
def _sigmoid(x):
    return torch.sigmoid(x)
def _layernorm(x, axis=-1, eps=1e-5):
    m = x.mean(dim=axis, keepdim=True)
    v = x.var(dim=axis, keepdim=True, unbiased=False)
    return (x - m) / torch.sqrt(v + eps)
def _scatter_add(tgt, idx, src, axis=0):
    return tgt.clone().index_add_(axis, idx, src)
def _segment_sum(src, seg, n):
    out = torch.zeros((n,) + src.shape[1:], dtype=src.dtype)
    return out.index_add_(0, seg, src)
def _pad(x, pw):
    flat = tuple(v for pair in reversed(pw) for v in pair)
    return torch.nn.functional.pad(x, flat)

kernel_name = 'sequential_preLN__sliding_block__mixer_s_mlp__prompt'

def setup_inputs():

    _w = (lambda a: (lambda t: t.to(torch.bfloat16) if t.is_floating_point() else t)(torch.as_tensor(a)))
    def _A_setup():
        B, S, D = 1, 2048, 640
        return {
            "x": _w((lambda *s: torch.randn(*s, dtype=torch.bfloat16) * 0.05)(B, S, D)),
            "wqkv": _w((lambda *s: torch.randn(*s, dtype=torch.bfloat16) * 0.05)(D, 3 * D)),
            "wo": _w((lambda *s: torch.randn(*s, dtype=torch.bfloat16) * 0.05)(D, D)),
        }
    _A_in = _A_setup()

    def _B_setup():
        B, S, D, F = 1, 2048, 640, 1536
        return {
            "x":  _w((lambda *s: torch.randn(*s, dtype=torch.bfloat16) * 0.05)(B, S, D)),
            "w1": _w((lambda *s: torch.randn(*s, dtype=torch.bfloat16) * 3.2)(D, D) * (D ** -0.5)),
            "w2": _w((lambda *s: torch.randn(*s, dtype=torch.bfloat16) * 3.2)(D, D) * (D ** -0.5)),
            "w3": _w((lambda *s: torch.randn(*s, dtype=torch.bfloat16) * 3.2)(D, F) * (D ** -0.5)),
            "w4": _w((lambda *s: torch.randn(*s, dtype=torch.bfloat16) * 3.2)(F, D) * (F ** -0.5)),
        }
    _B_in = _B_setup()

    return {
        "A_x": _A_in["x"],
        "A_wqkv": _A_in["wqkv"],
        "A_wo": _A_in["wo"],
        "B_x": _B_in["x"],
        "B_w1": _B_in["w1"],
        "B_w2": _B_in["w2"],
        "B_w3": _B_in["w3"],
        "B_w4": _B_in["w4"],
    }

def reference(A_x, A_wqkv, A_wo, B_x, B_w1, B_w2, B_w3, B_w4):
    def _A_ref(x, wqkv, wo):
        B, S, D = x.shape
        H = 32; Dh = D // H; W = 256
        qkv = (lambda x, *d: x.permute(*d))((x @ wqkv).reshape(B, S, 3, H, Dh), 2, 0, 3, 1, 4)
        q, k, v = qkv[0], qkv[1], qkv[2]
        scores = torch.einsum("bhqd,bhkd->bhqk", q, k) * (Dh ** -0.5)
        idx = torch.arange(S)
        mask = (idx[:, None] - idx[None, :] >= 0) & (idx[:, None] - idx[None, :] < W)
        scores = torch.where(mask[None, None], scores, -1e9)
        p = _softmax(scores, axis=-1)
        o = torch.einsum("bhqk,bhkd->bhqd", p, v)
        return (lambda x, *d: x.permute(*d))(o, 0, 2, 1, 3).reshape(B, S, D) @ wo

    def _B_ref(x, w1, w2, w3, w4):
        B, S, D = x.shape
        h = _gelu(_layernorm(x, axis=-1) @ w1)
        c = D // 4
        g0 = _pad(h[:, :, 0*c:1*c], ((0, 0), (1, 0), (0, 0)))[:, :S, :]
        g1 = _pad(h[:, :, 1*c:2*c], ((0, 0), (0, 1), (0, 0)))[:, 1:, :]
        g2 = _pad(h[:, :, 2*c:3*c], ((0, 0), (2, 0), (0, 0)))[:, :S, :]
        g3 = _pad(h[:, :, 3*c:   ], ((0, 0), (0, 2), (0, 0)))[:, 2:, :]
        hs = torch.concatenate([g0, g1, g2, g3], axis=-1) @ w2
        x1 = x + hs
        h2 = _gelu(_layernorm(x1, axis=-1) @ w3) @ w4
        return x1 + h2

    x = A_x
    h = x + _A_ref(_layernorm(x, axis=-1), A_wqkv, A_wo)
    h2 = h + _B_ref(_layernorm(h, axis=-1), B_w1, B_w2, B_w3, B_w4)
    return h2
```

```cpp
#include <hip/hip_runtime.h>
#include <hip/hip_bf16.h>
#include <stdint.h>
#include <type_traits>

typedef __attribute__((ext_vector_type(8))) __bf16 bf16x8;
typedef __attribute__((ext_vector_type(4))) __bf16 bf16x4;
typedef __attribute__((ext_vector_type(2))) __bf16 bf16x2;
typedef __attribute__((ext_vector_type(4))) short s16x4;
typedef __attribute__((ext_vector_type(4))) float f32x4;
typedef __attribute__((ext_vector_type(2))) unsigned u32x2;
typedef __attribute__((ext_vector_type(4))) unsigned u32x4;
#define LAS __attribute__((address_space(3)))
#define DEV __device__ __forceinline__

constexpr int S_ = 2048, D_ = 640, F_ = 1536, H_ = 32;

constexpr size_t QK_HEAD = 128 * 768;
constexpr size_t V_HEAD  = 64 * 1280;
constexpr size_t WS_Q  = 0;
constexpr size_t WS_K  = WS_Q + H_ * QK_HEAD;
constexpr size_t WS_V  = WS_K + H_ * QK_HEAD;
constexpr size_t WS_O  = WS_V + H_ * V_HEAD;
constexpr size_t WS_H  = WS_O + (size_t)S_ * D_ * 2;
constexpr size_t WS_G  = WS_H + (size_t)S_ * D_ * 2;
constexpr size_t WS_X1 = WS_G + (size_t)S_ * D_ * 2;
constexpr size_t WS_U  = WS_X1 + (size_t)S_ * D_ * 2;
constexpr size_t WS_ST = WS_U + (size_t)S_ * F_ * 2;
constexpr size_t WS_CS = WS_ST + (size_t)S_ * 8;

template <int I, int N, class Fn> DEV void static_for(Fn&& f) {
  if constexpr (I < N) { f(std::integral_constant<int, I>{}); static_for<I + 1, N>(f); }
}
template <int N> DEV void wait_vm() { asm volatile("s_waitcnt vmcnt(%0) lgkmcnt(0)" ::"n"(N) : "memory"); }
DEV void cfence() { asm volatile("" ::: "memory"); }
DEV float bfr(float x) { return (float)(__bf16)x; }
DEV unsigned pk2(float a, float b) { bf16x2 v = {(__bf16)a, (__bf16)b}; return __builtin_bit_cast(unsigned, v); }
DEV float bf_lo(unsigned u) { return __builtin_bit_cast(float, u << 16); }
DEV float bf_hi(unsigned u) { return __builtin_bit_cast(float, u & 0xffff0000u); }
DEV float gelu_f(float x) {
  const float c0 = -2.0f * 0.7978845608028654f * 1.4426950408889634f;
  const float c1 = c0 * 0.044715f;
  float t = x * x;
  float e = __builtin_amdgcn_exp2f(x * fmaf(t, c1, c0));
  return x * __builtin_amdgcn_rcpf(1.0f + e);
}
DEV f32x4 mfma16(bf16x8 a, bf16x8 b, f32x4 c) { return __builtin_amdgcn_mfma_f32_16x16x32_bf16(a, b, c, 0, 0, 0); }
DEV unsigned ft_off(int row, int k, int KS) {
  return (unsigned)(((row >> 4) * KS + (k >> 5)) * 1024 + ((row & 15) + 16 * ((k >> 3) & 3)) * 16 + (k & 7) * 2);
}

template <int MT, int NT, int WM, int WN, int KSTEPS, int G, int NG, bool A_RM, bool SWAP, bool STATS, bool CSUM>
struct Gemm {
  static constexpr int NW = WM * WN, TM = MT / WM, TN = NT / WN;
  static constexpr int PPK = MT + NT, PPG = G * PPK, PW = PPG / NW, NGRP = KSTEPS / G, SB = PPG * 1024;
  static constexpr int CG = 2;
  static_assert(PPG % NW == 0 && KSTEPS % G == 0 && G % CG == 0, "geometry");
  f32x4 acc[TM][TN];
  f32x4 cs[TN];
  float rs[TM], rq[TM];

  DEV void run(char* smem, const char* Ab, const char* Wb, int ldw_bytes) {
    const int tid = threadIdx.x, l = tid & 63;
    const int wave = __builtin_amdgcn_readfirstlane(tid >> 6);
    const int wm = wave / WN, wn = wave % WN;
    const int g = l >> 4;
    unsigned off[PW];
    bool isA[PW];
#pragma unroll
    for (int i = 0; i < PW; ++i) {
      const int q = wave + NW * i, ksl = q / PPK, r = q - ksl * PPK;
      isA[i] = r < MT;
      if (r < MT) {
        if constexpr (A_RM) off[i] = (unsigned)((16 * r + (l & 15)) * (KSTEPS * 64) + ksl * 64 + g * 16);
        else off[i] = (unsigned)((r * KSTEPS + ksl) * 1024 + l * 16);
      } else {
        const int tp = r - MT, p = 8 * tp + (l >> 3), kq2 = p / (2 * NT), idx = p ^ (kq2 & 1);
        const int kq = idx / NT, t = idx - kq * NT;
        off[i] = (unsigned)((ksl * 32 + 4 * kq + ((l & 7) >> 1)) * ldw_bytes + t * 32 + (l & 1) * 16);
      }
    }
    const long strA = A_RM ? G * 64 : G * 1024, strW = (long)G * 32 * ldw_bytes;
    auto issue = [&](int grp) {
      char* dst = smem + (grp % NG) * SB + wave * 1024;
      const char* a = Ab + grp * strA;
      const char* w = Wb + grp * strW;
#pragma unroll
      for (int i = 0; i < PW; ++i) {
        const char* src = (isA[i] ? a : w) + off[i];
        __builtin_amdgcn_global_load_lds((const __attribute__((address_space(1))) void*)src,
                                         (LAS void*)(dst + i * NW * 1024), 16, 0, 0);
      }
    };
#pragma unroll
    for (int i = 0; i < TM; ++i) {
      rs[i] = 0.f; rq[i] = 0.f;
#pragma unroll
      for (int j = 0; j < TN; ++j) acc[i][j] = f32x4{0.f, 0.f, 0.f, 0.f};
    }
#pragma unroll
    for (int j = 0; j < TN; ++j) cs[j] = f32x4{0.f, 0.f, 0.f, 0.f};
    constexpr int PRE = NG < NGRP ? NG : NGRP;
    static_for<0, PRE>([&](auto gi) { issue(gi.value); });
    const unsigned a_lane = (unsigned)(wm * TM * 1024 + l * 16);
    const unsigned w_base = (unsigned)(MT * 1024 + g * 2 * NT * 128 + ((l & 15) >> 2) * 32 + (l & 3) * 8 + wn * TN * 128);
    const unsigned w_ev = w_base + ((g & 1) ? 128u : 0u), w_od = w_base - ((g & 1) ? 128u : 0u);
    static_assert(WN == 1 || (TN % 2) == 0, "parity");
    const bf16x2 one2 = {(__bf16)1.0f, (__bf16)1.0f};
    const bf16x8 ones = {(__bf16)1.0f, (__bf16)1.0f, (__bf16)1.0f, (__bf16)1.0f, (__bf16)1.0f, (__bf16)1.0f, (__bf16)1.0f, (__bf16)1.0f};
    static_for<0, KSTEPS / CG>([&](auto ci) {
      constexpr int c = ci.value, ks0 = c * CG, grp = ks0 / G, ksl0 = ks0 % G;
      constexpr int issued = (grp + NG - (ksl0 == 0 && grp > 0 ? 1 : 0)) < NGRP ? (grp + NG - (ksl0 == 0 && grp > 0 ? 1 : 0)) : NGRP;
      constexpr int need = ((ksl0 + CG) * PPK + NW - 1) / NW;
      wait_vm<(PW - need) + PW * (issued - grp - 1)>();
      __builtin_amdgcn_s_barrier();
      cfence();
      if constexpr (ksl0 == 0 && grp > 0 && grp + NG - 1 < NGRP) issue(grp + NG - 1);
      char* sb = smem + (grp % NG) * SB;
#pragma unroll
      for (int kk = 0; kk < CG; ++kk) {
        char* kb = sb + (ksl0 + kk) * PPK * 1024;
        bf16x8 af[TM], wf[TN];
#pragma unroll
        for (int i = 0; i < TM; ++i) af[i] = *(const bf16x8*)(kb + a_lane + i * 1024);
#pragma unroll
        for (int j = 0; j < TN; ++j) {
          s16x4 lo = __builtin_amdgcn_ds_read_tr16_b64_v4i16((LAS s16x4*)(kb + ((j & 1) ? w_od : w_ev) + j * 128));
          s16x4 hi = __builtin_amdgcn_ds_read_tr16_b64_v4i16((LAS s16x4*)(kb + (((NT + j) & 1) ? w_od : w_ev) + (NT + j) * 128));
          typedef __attribute__((ext_vector_type(8))) short s16x8;
          s16x8 v = __builtin_shufflevector(lo, hi, 0, 1, 2, 3, 4, 5, 6, 7);
          wf[j] = __builtin_bit_cast(bf16x8, v);
        }
#pragma unroll
        for (int i = 0; i < TM; ++i)
#pragma unroll
          for (int j = 0; j < TN; ++j)
            acc[i][j] = SWAP ? mfma16(wf[j], af[i], acc[i][j]) : mfma16(af[i], wf[j], acc[i][j]);
        if constexpr (CSUM) {
#pragma unroll
          for (int j = 0; j < TN; ++j) cs[j] = SWAP ? mfma16(wf[j], ones, cs[j]) : mfma16(ones, wf[j], cs[j]);
        }
        if constexpr (STATS) {
#pragma unroll
          for (int i = 0; i < TM; ++i)
#pragma unroll
            for (int e = 0; e < 4; ++e) {
              bf16x2 v = {af[i][2 * e], af[i][2 * e + 1]};
              rs[i] = __builtin_amdgcn_fdot2_f32_bf16(v, one2, rs[i], false);
              rq[i] = __builtin_amdgcn_fdot2_f32_bf16(v, v, rq[i], false);
            }
        }
      }
      cfence();
    });
    if constexpr (STATS) {
#pragma unroll
      for (int i = 0; i < TM; ++i) {
        rs[i] += __shfl_xor(rs[i], 16); rs[i] += __shfl_xor(rs[i], 32);
        rq[i] += __shfl_xor(rq[i], 16); rq[i] += __shfl_xor(rq[i], 32);
      }
    }
  }
};

DEV void ln_stats(float sum, float sq, float& mean, float& var, float& m, float& s) {
  mean = sum * (1.0f / D_);
  var = fmaxf(sq * (1.0f / D_) - mean * mean, 0.f);
  m = bfr(mean);
  s = bfr(sqrtf(bfr(bfr(var) + 1e-5f)));
}

template <bool ISV>
DEV void qkv_body(char* smem, const char* x, const char* wqkv, char* ws, int rt, int ct) {
  using GM = Gemm<8, 8, 2, 2, 20, 2, 5, true, !ISV, true, true>;
  GM gm;
  const int row0 = rt * 128, n0 = ct * 128;
  gm.run(smem, x + (size_t)row0 * (D_ * 2), wqkv + n0 * 2, 3 * D_ * 2);
  const int tid = threadIdx.x, l = tid & 63, wave = tid >> 6, wm = wave >> 1, wn = wave & 1, g = l >> 4;
#pragma unroll
  for (int i = 0; i < 4; ++i) {
    float mean, var, m, s;
    ln_stats(gm.rs[i], gm.rq[i], mean, var, m, s);
    const float rinv = 1.0f / s;
    const int rbase = row0 + wm * 64 + i * 16;
    if constexpr (!ISV) {
      const int row = rbase + (l & 15);
      char* dstb = ws + (ct < 5 ? WS_Q : WS_K) + (size_t)(row >> 4) * 768 + (row & 15) * 16;
#pragma unroll
      for (int j = 0; j < 4; ++j) {
        const int c = (n0 + wn * 64 + j * 16 + 4 * g) % D_, head = c / 20, d0 = c - head * 20;
        float y[4];
#pragma unroll
        for (int r = 0; r < 4; ++r) y[r] = (gm.acc[i][j][r] - m * gm.cs[j][r]) * rinv;
        char* p = dstb + (size_t)head * QK_HEAD + (d0 >> 3) * 256 + (d0 & 7) * 2;
        u32x2 v = {pk2(y[0], y[1]), pk2(y[2], y[3])};
        *(u32x2*)p = v;
        if (d0 == 16) *(u32x2*)(p + 8) = u32x2{0u, 0u};
      }
    } else {
      float mr[4], rr[4];
#pragma unroll
      for (int r = 0; r < 4; ++r) { mr[r] = __shfl(m, 4 * g + r); rr[r] = __shfl(rinv, 4 * g + r); }
      const int key = rbase + 4 * g;
      const size_t kboff = (size_t)(key >> 5) * 1280 + ((key >> 4) & 1) * 8;
#pragma unroll
      for (int j = 0; j < 4; ++j) {
        const int c = n0 - 2 * D_ + wn * 64 + j * 16 + (l & 15), head = c / 20, d = c - head * 20;
        float y[4];
#pragma unroll
        for (int r = 0; r < 4; ++r) y[r] = (gm.acc[i][j][r] - mr[r] * gm.cs[j][0]) * rr[r];
        char* p = ws + WS_V + (size_t)head * V_HEAD + kboff + (d < 16 ? (g * 16 + d) * 16 : 1024 + (g * 4 + d - 16) * 16);
        *(u32x2*)p = u32x2{pk2(y[0], y[1]), pk2(y[2], y[3])};
      }
    }
  }
}

DEV void colsum_job(char* smem, const char* w1, const char* w3, float* cs, int blk) {
  const int tid = threadIdx.x, c = tid % 17, rg = tid / 17;
  const int gc = blk * 17 + c;
  const char* base = gc < 80 ? w1 + gc * 16 : w3 + (gc - 80) * 16;
  const int ld = gc < 80 ? D_ * 2 : F_ * 2;
  float a[8] = {0.f, 0.f, 0.f, 0.f, 0.f, 0.f, 0.f, 0.f};
  if (rg < 15) {
#pragma unroll 8
    for (int r = rg; r < D_; r += 15) {
      u32x4 v = *(const u32x4*)(base + (size_t)r * ld);
#pragma unroll
      for (int e = 0; e < 4; ++e) { a[2 * e] += bf_lo(v[e]); a[2 * e + 1] += bf_hi(v[e]); }
    }
  }
  float* sm = (float*)smem;
  if (rg < 15) {
#pragma unroll
    for (int e = 0; e < 8; ++e) sm[rg * 136 + c * 8 + e] = a[e];
  }
  __syncthreads();
  if (tid < 136) {
    float t = 0.f;
#pragma unroll
    for (int r = 0; r < 15; ++r) t += sm[r * 136 + tid];
    cs[blk * 136 + tid] = t;
  }
}

__global__ __launch_bounds__(256) void k1_qkv(const char* x, const char* wqkv, const char* w1, const char* w3, char* ws) {
  extern __shared__ __attribute__((aligned(16))) char smem[];
  const int b = blockIdx.x, xcd = b & 7, j = b >> 3;
  if (j >= 30) { colsum_job(smem, w1, w3, (float*)(ws + WS_CS), xcd * 2 + (j - 30)); return; }
  const int rt = xcd * 2 + j / 15, ct = j % 15;
  if (ct >= 10) qkv_body<true>(smem, x, wqkv, ws, rt, ct);
  else qkv_body<false>(smem, x, wqkv, ws, rt, ct);
}

__global__ __launch_bounds__(256) void k2_attn(char* ws) {
  __shared__ __attribute__((aligned(16))) char smem[24 * 768 + 12 * 1280];
  const int b = blockIdx.x, xcd = b & 7, j = b >> 3;
  const int qb = xcd * 2 + (j >> 5), head = j & 31;
  const int tid = threadIdx.x, l = tid & 63, g = l >> 4, li = l & 15;
  const int w = __builtin_amdgcn_readfirstlane(tid >> 6);
  const char* Kh = ws + WS_K + (size_t)head * QK_HEAD;
  const char* Vh = ws + WS_V + (size_t)head * V_HEAD;
  const char* Qh = ws + WS_Q + (size_t)head * QK_HEAD;
  bf16x8 qf[2];
#pragma unroll
  for (int i = 0; i < 2; ++i) {
    u32x4 v = {0u, 0u, 0u, 0u};
    if (g < 3) v = *(const u32x4*)(Qh + (size_t)(qb * 8 + 2 * w + i) * 768 + g * 256 + li * 16);
    qf[i] = __builtin_bit_cast(bf16x8, v);
  }
  {
    const int koff0 = (qb * 8 - 16) * 768, voff0 = (qb * 4 - 8) * 1280;
#pragma unroll
    for (int i = 0; i < 9; ++i) {
      const int p = w + 4 * i;
      if (p < 33) {
        const char* src;
        if (p < 18) { int o = koff0 + p * 1024 + l * 16; src = Kh + (o < 0 ? l * 16 : o); }
        else { int o = voff0 + (p - 18) * 1024 + l * 16; src = Vh + (o < 0 ? l * 16 : o); }
        __builtin_amdgcn_global_load_lds((const __attribute__((address_space(1))) void*)src, (LAS void*)(smem + p * 1024), 16, 0, 0);
      }
    }
  }
  wait_vm<0>();
  __syncthreads();
  const char* Ks = smem + 2 * w * 768 + (g < 3 ? g : 2) * 256 + li * 16;
  const int kbase = qb * 128 + 32 * w - 256;
  f32x4 s0[17], s1[17];
  const f32x4 z4 = {0.f, 0.f, 0.f, 0.f};
#pragma unroll
  for (int c = 0; c < 18; ++c) {
    bf16x8 kf = *(const bf16x8*)(Ks + c * 768);
    if (c < 17) s0[c] = mfma16(kf, qf[0], z4);
    if (c > 0) s1[c - 1] = mfma16(kf, qf[1], z4);
  }
  const float NEG = -__builtin_inff();
#pragma unroll
  for (int r = 0; r < 4; ++r) {
    const int kj = 4 * g + r;
    if (kj <= li) { s0[0][r] = NEG; s1[0][r] = NEG; }
    if (kj > li) { s0[16][r] = NEG; s1[16][r] = NEG; }
  }
  if (kbase < 0) {
#pragma unroll
    for (int c = 0; c < 17; ++c) {
      if (kbase + 16 * c < 0) s0[c] = f32x4{NEG, NEG, NEG, NEG};
      if (kbase + 16 * (c + 1) < 0) s1[c] = f32x4{NEG, NEG, NEG, NEG};
    }
  }
  float m0 = NEG, m1 = NEG;
#pragma unroll
  for (int c = 0; c < 17; ++c)
#pragma unroll
    for (int r = 0; r < 4; ++r) { m0 = fmaxf(m0, s0[c][r]); m1 = fmaxf(m1, s1[c][r]); }
  m0 = fmaxf(m0, __shfl_xor(m0, 16)); m0 = fmaxf(m0, __shfl_xor(m0, 32));
  m1 = fmaxf(m1, __shfl_xor(m1, 16)); m1 = fmaxf(m1, __shfl_xor(m1, 32));
  const float C2 = 0.22360679774997896f * 1.4426950408889634f;
  const float b0 = -m0 * C2, b1 = -m1 * C2;
  u32x2 p0[18], p1[18];
#pragma unroll
  for (int c = 0; c < 17; ++c) {
    float e[4], f[4];
#pragma unroll
    for (int r = 0; r < 4; ++r) {
      e[r] = __builtin_amdgcn_exp2f(fmaf(s0[c][r], C2, b0));
      f[r] = __builtin_amdgcn_exp2f(fmaf(s1[c][r], C2, b1));
    }
    p0[c] = u32x2{pk2(e[0], e[1]), pk2(e[2], e[3])};
    p1[c + 1] = u32x2{pk2(f[0], f[1]), pk2(f[2], f[3])};
  }
  p0[17] = u32x2{0u, 0u}; p1[0] = u32x2{0u, 0u};
  const char* Vs0 = smem + 24 * 768 + w * 1280 + (g * 16 + li) * 16;
  const char* Vs1 = smem + 24 * 768 + w * 1280 + 1024 + (g * 4 + (li & 3)) * 16;
  f32x4 o00 = z4, o01 = z4, o10 = z4, o11 = z4;
#pragma unroll
  for (int s = 0; s < 9; ++s) {
    bf16x8 v0 = *(const bf16x8*)(Vs0 + s * 1280);
    u32x4 v1u = *(const u32x4*)(Vs1 + s * 1280);
    if (li == 4) v1u = u32x4{0x3F803F80u, 0x3F803F80u, 0x3F803F80u, 0x3F803F80u};
    bf16x8 v1 = __builtin_bit_cast(bf16x8, v1u);
    u32x4 pa = {p0[2 * s][0], p0[2 * s][1], p0[2 * s + 1][0], p0[2 * s + 1][1]};
    u32x4 pb = {p1[2 * s][0], p1[2 * s][1], p1[2 * s + 1][0], p1[2 * s + 1][1]};
    bf16x8 pfa = __builtin_bit_cast(bf16x8, pa), pfb = __builtin_bit_cast(bf16x8, pb);
    o00 = mfma16(v0, pfa, o00); o01 = mfma16(v1, pfa, o01);
    o10 = mfma16(v0, pfb, o10); o11 = mfma16(v1, pfb, o11);
  }
  const float r0 = 1.0f / __shfl(o01[0], 16 + li), r1 = 1.0f / __shfl(o11[0], 16 + li);
  char* Of = ws + WS_O;
  const int q0 = qb * 128 + 32 * w + li, col = head * 20 + 4 * g;
  *(u32x2*)(Of + ft_off(q0, col, 20)) = u32x2{pk2(o00[0] * r0, o00[1] * r0), pk2(o00[2] * r0, o00[3] * r0)};
  *(u32x2*)(Of + ft_off(q0 + 16, col, 20)) = u32x2{pk2(o10[0] * r1, o10[1] * r1), pk2(o10[2] * r1, o10[3] * r1)};
  if (g == 0) {
    *(u32x2*)(Of + ft_off(q0, col + 16, 20)) = u32x2{pk2(o01[0] * r0, o01[1] * r0), pk2(o01[2] * r0, o01[3] * r0)};
    *(u32x2*)(Of + ft_off(q0 + 16, col + 16, 20)) = u32x2{pk2(o11[0] * r1, o11[1] * r1), pk2(o11[2] * r1, o11[3] * r1)};
  }
}

using G80 = Gemm<4, 5, 4, 1, 20, 4, 4, false, true, false, false>;
using G80s = Gemm<4, 5, 4, 1, 20, 4, 4, false, true, true, false>;
using G80L = Gemm<4, 5, 4, 1, 48, 4, 4, false, true, false, false>;
using G192 = Gemm<4, 12, 2, 2, 20, 2, 5, false, true, true, false>;

__global__ __launch_bounds__(256) void k3_wo(const char* x, const char* wo, char* ws) {
  extern __shared__ __attribute__((aligned(16))) char smem[];
  const int b = blockIdx.x, xcd = b & 7, jj = b >> 3, rt = xcd * 4 + (jj >> 3), ct = jj & 7;
  const int tid = threadIdx.x, l = tid & 63, wave = tid >> 6, g = l >> 4;
  const int row = rt * 64 + wave * 16 + (l & 15), n0 = ct * 80;
  u32x2 xr[5];
#pragma unroll
  for (int j = 0; j < 5; ++j) xr[j] = *(const u32x2*)(x + (size_t)row * (D_ * 2) + (n0 + j * 16 + 4 * g) * 2);
  G80 gm;
  gm.run(smem, ws + WS_O + (size_t)rt * 4 * 20 * 1024, wo + n0 * 2, D_ * 2);
#pragma unroll
  for (int j = 0; j < 5; ++j) {
    const f32x4 a = gm.acc[0][j];
    u32x2 v = {pk2(bf_lo(xr[j][0]) + bfr(a[0]), bf_hi(xr[j][0]) + bfr(a[1])), pk2(bf_lo(xr[j][1]) + bfr(a[2]), bf_hi(xr[j][1]) + bfr(a[3]))};
    *(u32x2*)(ws + WS_H + ft_off(row, n0 + j * 16 + 4 * g, 20)) = v;
  }
}

__global__ __launch_bounds__(256) void k4_w1(const char* w1, char* ws) {
  extern __shared__ __attribute__((aligned(16))) char smem[];
  const int b = blockIdx.x, xcd = b & 7, jj = b >> 3, rt = xcd * 4 + (jj >> 3), ct = jj & 7;
  const int tid = threadIdx.x, l = tid & 63, wave = tid >> 6, g = l >> 4;
  const int row = rt * 64 + wave * 16 + (l & 15), n0 = ct * 80;
  f32x4 cs[5];
#pragma unroll
  for (int j = 0; j < 5; ++j) cs[j] = *(const f32x4*)(ws + WS_CS + (n0 + j * 16 + 4 * g) * 4);
  G80s gm;
  gm.run(smem, ws + WS_H + (size_t)rt * 4 * 20 * 1024, w1 + n0 * 2, D_ * 2);
  float mean, var, m1, s1;
  ln_stats(gm.rs[0], gm.rq[0], mean, var, m1, s1);
  const float r1 = 1.0f / s1;
  const float m2 = bfr((mean - m1) * r1), v2 = bfr(var * r1 * r1);
  const float s2 = bfr(sqrtf(bfr(v2 + 1e-5f)));
  const float mm = m1 + m2 * s1, rr = r1 / s2;
  if (ct == 0 && g == 0) *(float2*)(ws + WS_ST + row * 8) = float2{m1, r1};
  const int grp = ct >> 1;
  const int sh = (grp & 2) ? ((grp & 1) ? -2 : 2) : ((grp & 1) ? -1 : 1);
  const int drow = row + sh;
  const int zrow = sh > 0 ? row : row;
  const bool zfill = sh > 0 ? (row < sh) : (row >= S_ + sh);
#pragma unroll
  for (int j = 0; j < 5; ++j) {
    const f32x4 a = gm.acc[0][j];
    float y[4];
#pragma unroll
    for (int r = 0; r < 4; ++r) y[r] = gelu_f(bfr((a[r] - mm * cs[j][r]) * rr));
    const int col = n0 + j * 16 + 4 * g;
    if (drow >= 0 && drow < S_) *(u32x2*)(ws + WS_G + ft_off(drow, col, 20)) = u32x2{pk2(y[0], y[1]), pk2(y[2], y[3])};
    if (zfill) *(u32x2*)(ws + WS_G + ft_off(zrow, col, 20)) = u32x2{0u, 0u};
  }
}

__global__ __launch_bounds__(256) void k5_w2(const char* w2, char* ws) {
  extern __shared__ __attribute__((aligned(16))) char smem[];
  const int b = blockIdx.x, xcd = b & 7, jj = b >> 3, rt = xcd * 4 + (jj >> 3), ct = jj & 7;
  const int tid = threadIdx.x, l = tid & 63, wave = tid >> 6, g = l >> 4;
  const int row = rt * 64 + wave * 16 + (l & 15), n0 = ct * 80;
  u32x2 hr[5];
#pragma unroll
  for (int j = 0; j < 5; ++j) hr[j] = *(const u32x2*)(ws + WS_H + ft_off(row, n0 + j * 16 + 4 * g, 20));
  const float2 st = *(const float2*)(ws + WS_ST + row * 8);
  G80 gm;
  gm.run(smem, ws + WS_G + (size_t)rt * 4 * 20 * 1024, w2 + n0 * 2, D_ * 2);
#pragma unroll
  for (int j = 0; j < 5; ++j) {
    const f32x4 a = gm.acc[0][j];
    float hv[4] = {bf_lo(hr[j][0]), bf_hi(hr[j][0]), bf_lo(hr[j][1]), bf_hi(hr[j][1])};
    float y[4];
#pragma unroll
    for (int r = 0; r < 4; ++r) y[r] = bfr(bfr(hv[r] - st.x) * st.y) + bfr(a[r]);
    *(u32x2*)(ws + WS_X1 + ft_off(row, n0 + j * 16 + 4 * g, 20)) = u32x2{pk2(y[0], y[1]), pk2(y[2], y[3])};
  }
}

__global__ __launch_bounds__(256) void k6_w3(const char* w3, char* ws) {
  extern __shared__ __attribute__((aligned(16))) char smem[];
  const int b = blockIdx.x, xcd = b & 7, jj = b >> 3, rt = xcd * 4 + (jj >> 3), ct = jj & 7;
  const int tid = threadIdx.x, l = tid & 63, wave = tid >> 6, wm = wave >> 1, wn = wave & 1, g = l >> 4;
  const int n0 = ct * 192 + wn * 96;
  f32x4 cs[6];
#pragma unroll
  for (int j = 0; j < 6; ++j) cs[j] = *(const f32x4*)(ws + WS_CS + (D_ + n0 + j * 16 + 4 * g) * 4);
  G192 gm;
  gm.run(smem, ws + WS_X1 + (size_t)rt * 4 * 20 * 1024, w3 + ct * 192 * 2, F_ * 2);
#pragma unroll
  for (int i = 0; i < 2; ++i) {
    const int row = rt * 64 + wm * 32 + i * 16 + (l & 15);
    float mean, var, m, s;
    ln_stats(gm.rs[i], gm.rq[i], mean, var, m, s);
    const float rinv = 1.0f / s;
#pragma unroll
    for (int j = 0; j < 6; ++j) {
      const f32x4 a = gm.acc[i][j];
      float y[4];
#pragma unroll
      for (int r = 0; r < 4; ++r) y[r] = gelu_f(bfr((a[r] - m * cs[j][r]) * rinv));
      *(u32x2*)(ws + WS_U + ft_off(row, n0 + j * 16 + 4 * g, 48)) = u32x2{pk2(y[0], y[1]), pk2(y[2], y[3])};
    }
  }
}

__global__ __launch_bounds__(256) void k7_w4(const char* w4, const char* ws, char* out) {
  extern __shared__ __attribute__((aligned(16))) char smem[];
  const int b = blockIdx.x, xcd = b & 7, jj = b >> 3, rt = xcd * 4 + (jj >> 3), ct = jj & 7;
  const int tid = threadIdx.x, l = tid & 63, wave = tid >> 6, g = l >> 4;
  const int row = rt * 64 + wave * 16 + (l & 15), n0 = ct * 80;
  u32x2 hr[5], xr[5];
#pragma unroll
  for (int j = 0; j < 5; ++j) {
    hr[j] = *(const u32x2*)(ws + WS_H + ft_off(row, n0 + j * 16 + 4 * g, 20));
    xr[j] = *(const u32x2*)(ws + WS_X1 + ft_off(row, n0 + j * 16 + 4 * g, 20));
  }
  G80L gm;
  gm.run(smem, ws + WS_U + (size_t)rt * 4 * 48 * 1024, w4 + n0 * 2, D_ * 2);
#pragma unroll
  for (int j = 0; j < 5; ++j) {
    const f32x4 a = gm.acc[0][j];
    float hv[4] = {bf_lo(hr[j][0]), bf_hi(hr[j][0]), bf_lo(hr[j][1]), bf_hi(hr[j][1])};
    float xv[4] = {bf_lo(xr[j][0]), bf_hi(xr[j][0]), bf_lo(xr[j][1]), bf_hi(xr[j][1])};
    float y[4];
#pragma unroll
    for (int r = 0; r < 4; ++r) y[r] = hv[r] + bfr(xv[r] + bfr(a[r]));
    *(u32x2*)(out + (size_t)row * (D_ * 2) + (n0 + j * 16 + 4 * g) * 2) = u32x2{pk2(y[0], y[1]), pk2(y[2], y[3])};
  }
}

extern "C" void kernel_launch(void* const* d_in, const int* in_sizes, int n_in, void* d_out, int out_size,
                              void* d_ws, size_t ws_size, hipStream_t stream) {
  const char* x = (const char*)d_in[0];
  const char* wqkv = (const char*)d_in[1];
  const char* wo = (const char*)d_in[2];
  const char* w1 = (const char*)d_in[4];
  const char* w2 = (const char*)d_in[5];
  const char* w3 = (const char*)d_in[6];
  const char* w4 = (const char*)d_in[7];
  char* ws = (char*)d_ws;
  if (ws_size < WS_CS + (size_t)(D_ + F_) * 4) return;
  constexpr int L160 = 163840, L144 = 147456;
  (void)hipFuncSetAttribute((const void*)k1_qkv, hipFuncAttributeMaxDynamicSharedMemorySize, L160);
  (void)hipFuncSetAttribute((const void*)k3_wo, hipFuncAttributeMaxDynamicSharedMemorySize, L144);
  (void)hipFuncSetAttribute((const void*)k4_w1, hipFuncAttributeMaxDynamicSharedMemorySize, L144);
  (void)hipFuncSetAttribute((const void*)k5_w2, hipFuncAttributeMaxDynamicSharedMemorySize, L144);
  (void)hipFuncSetAttribute((const void*)k6_w3, hipFuncAttributeMaxDynamicSharedMemorySize, L160);
  (void)hipFuncSetAttribute((const void*)k7_w4, hipFuncAttributeMaxDynamicSharedMemorySize, L144);
  k1_qkv<<<256, 256, L160, stream>>>(x, wqkv, w1, w3, ws);
  k2_attn<<<512, 256, 0, stream>>>(ws);
  k3_wo<<<256, 256, L144, stream>>>(x, wo, ws);
  k4_w1<<<256, 256, L144, stream>>>(w1, ws);
  k5_w2<<<256, 256, L144, stream>>>(w2, ws);
  k6_w3<<<256, 256, L160, stream>>>(w3, ws);
  k7_w4<<<256, 256, L144, stream>>>(w4, ws, (char*)d_out);
}
```

```cpp
#include <hip/hip_runtime.h>
#include <hip/hip_bf16.h>
#include <stdint.h>
#include <type_traits>

typedef __attribute__((ext_vector_type(8))) __bf16 bf16x8;
typedef __attribute__((ext_vector_type(4))) __bf16 bf16x4;
typedef __attribute__((ext_vector_type(2))) __bf16 bf16x2;
typedef __attribute__((ext_vector_type(4))) short s16x4;
typedef __attribute__((ext_vector_type(4))) float f32x4;
typedef __attribute__((ext_vector_type(2))) unsigned u32x2;
typedef __attribute__((ext_vector_type(4))) unsigned u32x4;
#define LAS __attribute__((address_space(3)))
#define DEV __device__ __forceinline__

constexpr int S_ = 2048, D_ = 640, F_ = 1536, H_ = 32;

constexpr size_t QK_HEAD = 128 * 768;
constexpr size_t V_HEAD  = 64 * 1280;
constexpr size_t WS_Q  = 0;
constexpr size_t WS_K  = WS_Q + H_ * QK_HEAD;
constexpr size_t WS_V  = WS_K + H_ * QK_HEAD;
constexpr size_t WS_O  = WS_V + H_ * V_HEAD;
constexpr size_t WS_H  = WS_O + (size_t)S_ * D_ * 2;
constexpr size_t WS_G  = WS_H + (size_t)S_ * D_ * 2;
constexpr size_t WS_X1 = WS_G + (size_t)S_ * D_ * 2;
constexpr size_t WS_U  = WS_X1 + (size_t)S_ * D_ * 2;
constexpr size_t WS_ST = WS_U + (size_t)S_ * F_ * 2;
constexpr size_t WS_WO = WS_ST + (size_t)S_ * 8;
constexpr size_t WS_W1 = WS_WO + (size_t)D_ * D_ * 2;
constexpr size_t WS_W2 = WS_W1 + (size_t)D_ * D_ * 2;
constexpr size_t WS_W3 = WS_W2 + (size_t)D_ * D_ * 2;
constexpr size_t WS_W4 = WS_W3 + (size_t)D_ * F_ * 2;
constexpr size_t WS_END = WS_W4 + (size_t)D_ * F_ * 2;

template <int I, int N, class Fn> DEV void static_for(Fn&& f) {
  if constexpr (I < N) { f(std::integral_constant<int, I>{}); static_for<I + 1, N>(f); }
}
template <int N> DEV void wait_vm() { asm volatile("s_waitcnt vmcnt(%0) lgkmcnt(0)" ::"n"(N) : "memory"); }
DEV void glds16(const char* sbase, unsigned voff, unsigned lds) {
  asm volatile("s_mov_b32 m0, %0\n\ts_nop 0\n\tglobal_load_lds_dwordx4 %1, %2" ::"s"(lds), "v"(voff), "s"(sbase) : "memory");
}
DEV unsigned lds_addr(const void* p) { return (unsigned)(uintptr_t)(LAS const void*)p; }
DEV void cfence() { asm volatile("" ::: "memory"); }
DEV float bfr(float x) { return (float)(__bf16)x; }
DEV unsigned pk2(float a, float b) { bf16x2 v = {(__bf16)a, (__bf16)b}; return __builtin_bit_cast(unsigned, v); }
DEV float bf_lo(unsigned u) { return __builtin_bit_cast(float, u << 16); }
DEV float bf_hi(unsigned u) { return __builtin_bit_cast(float, u & 0xffff0000u); }
DEV float gelu_f(float x) {
  const float c0 = -2.0f * 0.7978845608028654f * 1.4426950408889634f;
  const float c1 = c0 * 0.044715f;
  float t = x * x;
  float e = __builtin_amdgcn_exp2f(x * fmaf(t, c1, c0));
  return x * __builtin_amdgcn_rcpf(1.0f + e);
}
DEV f32x4 mfma16(bf16x8 a, bf16x8 b, f32x4 c) { return __builtin_amdgcn_mfma_f32_16x16x32_bf16(a, b, c, 0, 0, 0); }
DEV unsigned ft_off(int row, int k, int KS) {
  return (unsigned)(((row >> 4) * KS + (k >> 5)) * 1024 + ((row & 15) + 16 * ((k >> 3) & 3)) * 16 + (k & 7) * 2);
}

DEV void dot_sum(float& s, bf16x8 f) {
  const bf16x2 one2 = {(__bf16)1.0f, (__bf16)1.0f};
#pragma unroll
  for (int e = 0; e < 4; ++e) { bf16x2 v = {f[2 * e], f[2 * e + 1]}; s = __builtin_amdgcn_fdot2_f32_bf16(v, one2, s, false); }
}
DEV void dot_sq(float& s, bf16x8 f) {
#pragma unroll
  for (int e = 0; e < 4; ++e) { bf16x2 v = {f[2 * e], f[2 * e + 1]}; s = __builtin_amdgcn_fdot2_f32_bf16(v, v, s, false); }
}
DEV float red_g(float v) { v += __shfl_xor(v, 16); v += __shfl_xor(v, 32); return v; }

template <int MT, int NT, int WM, int WN, int KS, int G, int NG, bool STATS>
struct Gemm {
  static constexpr int NW = WM * WN, TM = MT / WM, TN = NT / WN;
  static constexpr int PPK = MT + NT, WPK = NW / G, PW = PPK / WPK, NGRP = KS / G, SB = G * PPK * 1024;
  static_assert(NW % G == 0 && PPK % WPK == 0 && KS % G == 0, "geometry");
  f32x4 acc[TM][TN];
  f32x4 cs[TN];
  float rs[TM], rq[TM];

  DEV void run(char* smem, const char* Ab, const char* Wb) {
    const int tid = threadIdx.x, l = tid & 63;
    const int wave = __builtin_amdgcn_readfirstlane(tid >> 6);
    const int wm = wave / WN, wn = wave % WN, g = l >> 4;
    const int ksl = wave / WPK, r0 = (wave % WPK) * PW;
    const char* sb[PW];
#pragma unroll
    for (int i = 0; i < PW; ++i) {
      const int r = r0 + i;
      sb[i] = (r < MT ? Ab + r * (KS * 1024) : Wb + (r - MT) * (KS * 1024)) + ksl * 1024;
    }
    const unsigned dst0 = lds_addr(smem) + (ksl * PPK + r0) * 1024;
    auto issue = [&](int grp) {
      const unsigned voff = l * 16 + grp * (G * 1024);
#pragma unroll
      for (int i = 0; i < PW; ++i) glds16(sb[i], voff, dst0 + (grp % NG) * SB + i * 1024);
    };
    constexpr int PRE = NG < NGRP ? NG : NGRP;
    static_for<0, PRE>([&](auto gi) { issue(gi.value); });
    float cd[TN];
#pragma unroll
    for (int i = 0; i < TM; ++i) {
      rs[i] = 0.f; rq[i] = 0.f;
#pragma unroll
      for (int j = 0; j < TN; ++j) acc[i][j] = f32x4{0.f, 0.f, 0.f, 0.f};
    }
#pragma unroll
    for (int j = 0; j < TN; ++j) cd[j] = 0.f;
    const char* a_lane = smem + wm * TM * 1024 + l * 16;
    const char* w_lane = smem + (MT + wn * TN) * 1024 + l * 16;
    static_for<0, NGRP>([&](auto gi) {
      constexpr int grp = gi.value;
      constexpr int iss0 = grp == 0 ? NG : grp + NG - 1;
      constexpr int issued = iss0 < NGRP ? iss0 : NGRP;
      wait_vm<PW * (issued - grp - 1)>();
      __builtin_amdgcn_s_barrier();
      cfence();
      if constexpr (grp > 0 && grp + NG - 1 < NGRP) issue(grp + NG - 1);
#pragma unroll
      for (int kk = 0; kk < G; ++kk) {
        constexpr int so = (grp % NG) * SB;
        bf16x8 af[TM], wf[TN];
#pragma unroll
        for (int i = 0; i < TM; ++i) af[i] = *(const bf16x8*)(a_lane + so + kk * PPK * 1024 + i * 1024);
#pragma unroll
        for (int j = 0; j < TN; ++j) wf[j] = *(const bf16x8*)(w_lane + so + kk * PPK * 1024 + j * 1024);
#pragma unroll
        for (int i = 0; i < TM; ++i)
#pragma unroll
          for (int j = 0; j < TN; ++j) acc[i][j] = mfma16(wf[j], af[i], acc[i][j]);
        if constexpr (STATS) {
#pragma unroll
          for (int i = 0; i < TM; ++i) { dot_sum(rs[i], af[i]); dot_sq(rq[i], af[i]); }
#pragma unroll
          for (int j = 0; j < TN; ++j) dot_sum(cd[j], wf[j]);
        }
      }
      cfence();
    });
    if constexpr (STATS) {
#pragma unroll
      for (int i = 0; i < TM; ++i) { rs[i] = red_g(rs[i]); rq[i] = red_g(rq[i]); }
#pragma unroll
      for (int j = 0; j < TN; ++j) {
        const float c = red_g(cd[j]);
#pragma unroll
        for (int r = 0; r < 4; ++r) cs[j][r] = __shfl(c, 4 * g + r);
      }
    }
  }
};

DEV unsigned wrm_voff(int l, int q, int ldw) {
  return (unsigned)((l >> 4) * ldw + ((((l & 15) >> 1) ^ ((l >> 4) | (q << 2))) * 32) + (l & 1) * 16);
}
DEV bf16x8 wrm_frag(const char* p) {
  s16x4 lo = __builtin_amdgcn_ds_read_tr16_b64_v4i16((LAS s16x4*)(p));
  s16x4 hi = __builtin_amdgcn_ds_read_tr16_b64_v4i16((LAS s16x4*)(p + 1024));
  typedef __attribute__((ext_vector_type(8))) short s16x8;
  s16x8 v = __builtin_shufflevector(lo, hi, 0, 1, 2, 3, 4, 5, 6, 7);
  return __builtin_bit_cast(bf16x8, v);
}
DEV unsigned wrm_lane(int l, int jhi, int jj) {
  const int g = l >> 4, i = l & 15;
  return (unsigned)((8 * g + (i >> 2)) * 256 + ((((jhi ^ (g & 1)) << 2) | (jj ^ (i >> 2))) * 32) + (i & 3) * 8);
}

template <bool SWAP>
struct GemmQ {
  f32x4 acc[4][4];
  float cd[4];
  float rs[4], rq[4];
  DEV void run(char* smem, const char* Ab, const char* Wb) {
    constexpr int LDA = D_ * 2, LDW = 3 * D_ * 2, NG = 5, NGRP = 10, SB = 32768;
    const int tid = threadIdx.x, l = tid & 63;
    const int wave = __builtin_amdgcn_readfirstlane(tid >> 6);
    const int wm = wave >> 1, wn = wave & 1, g = l >> 4;
    unsigned va[2], vw[2];
#pragma unroll
    for (int q = 0; q < 2; ++q) {
      va[q] = (unsigned)((l >> 3) * LDA + (((l & 7) ^ (q * 4 + (l >> 4))) * 16));
      vw[q] = wrm_voff(l, q, LDW);
    }
    const char* sa = Ab + (size_t)wave * 32 * LDA;
    const char* sw = Wb + (size_t)wave * 16 * LDW;
    const unsigned dst0 = lds_addr(smem) + wave * 4096;
    auto issue = [&](int grp) {
#pragma unroll
      for (int i = 0; i < 4; ++i) glds16(sa + grp * 128 + i * 8 * LDA, va[i & 1], dst0 + (grp % NG) * SB + i * 1024);
#pragma unroll
      for (int i = 0; i < 4; ++i) glds16(sw + (size_t)grp * 64 * LDW + i * 4 * LDW, vw[i >> 1], dst0 + (grp % NG) * SB + 16384 + i * 1024);
    };
    static_for<0, NG>([&](auto gi) { issue(gi.value); });
#pragma unroll
    for (int i = 0; i < 4; ++i) {
      rs[i] = 0.f; rq[i] = 0.f; cd[i] = 0.f;
#pragma unroll
      for (int j = 0; j < 4; ++j) acc[i][j] = f32x4{0.f, 0.f, 0.f, 0.f};
    }
    const unsigned a0 = (unsigned)((wm * 64 + (l & 15)) * 128 + ((g ^ ((l & 15) >> 1)) * 16));
    const char* al[2] = {smem + a0, smem + (a0 ^ 64u)};
    const char* wl[4];
#pragma unroll
    for (int j = 0; j < 4; ++j) wl[j] = smem + 16384 + wrm_lane(l, wn, j);
    static_for<0, NGRP>([&](auto gi) {
      constexpr int grp = gi.value;
      constexpr int iss0 = grp == 0 ? NG : grp + NG - 1;
      constexpr int issued = iss0 < NGRP ? iss0 : NGRP;
      wait_vm<8 * (issued - grp - 1)>();
      __builtin_amdgcn_s_barrier();
      cfence();
      if constexpr (grp > 0 && grp + NG - 1 < NGRP) issue(grp + NG - 1);
#pragma unroll
      for (int kk = 0; kk < 2; ++kk) {
        constexpr int so = (grp % NG) * SB;
        bf16x8 af[4], wf[4];
#pragma unroll
        for (int i = 0; i < 4; ++i) af[i] = *(const bf16x8*)(al[kk] + so + i * 2048);
#pragma unroll
        for (int j = 0; j < 4; ++j) wf[j] = wrm_frag(wl[j] + so + kk * 8192);
#pragma unroll
        for (int i = 0; i < 4; ++i)
#pragma unroll
          for (int j = 0; j < 4; ++j)
            acc[i][j] = SWAP ? mfma16(wf[j], af[i], acc[i][j]) : mfma16(af[i], wf[j], acc[i][j]);
#pragma unroll
        for (int i = 0; i < 4; ++i) { dot_sum(rs[i], af[i]); dot_sq(rq[i], af[i]); dot_sum(cd[i], wf[i]); }
      }
      cfence();
    });
#pragma unroll
    for (int i = 0; i < 4; ++i) { rs[i] = red_g(rs[i]); rq[i] = red_g(rq[i]); cd[i] = red_g(cd[i]); }
  }
};

DEV void ln_stats(float sum, float sq, float& mean, float& var, float& m, float& s) {
  mean = sum * (1.0f / D_);
  var = fmaxf(sq * (1.0f / D_) - mean * mean, 0.f);
  m = bfr(mean);
  s = bfr(sqrtf(bfr(bfr(var) + 1e-5f)));
}

template <bool ISV>
DEV void qkv_body(char* smem, const char* x, const char* wqkv, char* ws, int rt, int ct) {
  GemmQ<!ISV> gm;
  const int row0 = rt * 128, n0 = ct * 128;
  gm.run(smem, x + (size_t)row0 * (D_ * 2), wqkv + n0 * 2);
  const int tid = threadIdx.x, l = tid & 63, wave = tid >> 6, wm = wave >> 1, wn = wave & 1, g = l >> 4;
  float cs[4][4];
  if constexpr (!ISV) {
#pragma unroll
    for (int j = 0; j < 4; ++j)
#pragma unroll
      for (int r = 0; r < 4; ++r) cs[j][r] = __shfl(gm.cd[j], 4 * g + r);
  }
#pragma unroll
  for (int i = 0; i < 4; ++i) {
    float mean, var, m, s;
    ln_stats(gm.rs[i], gm.rq[i], mean, var, m, s);
    const float rinv = 1.0f / s;
    const int rbase = row0 + wm * 64 + i * 16;
    if constexpr (!ISV) {
      const int row = rbase + (l & 15);
      char* dstb = ws + (ct < 5 ? WS_Q : WS_K) + (size_t)(row >> 4) * 768 + (row & 15) * 16;
#pragma unroll
      for (int j = 0; j < 4; ++j) {
        const int c = (n0 + wn * 64 + j * 16 + 4 * g) % D_, head = c / 20, d0 = c - head * 20;
        float y[4];
#pragma unroll
        for (int r = 0; r < 4; ++r) y[r] = (gm.acc[i][j][r] - m * cs[j][r]) * rinv;
        char* p = dstb + (size_t)head * QK_HEAD + (d0 >> 3) * 256 + (d0 & 7) * 2;
        u32x2 v = {pk2(y[0], y[1]), pk2(y[2], y[3])};
        *(u32x2*)p = v;
        if (d0 == 16) *(u32x2*)(p + 8) = u32x2{0u, 0u};
      }
    } else {
      float mr[4], rr[4];
#pragma unroll
      for (int r = 0; r < 4; ++r) { mr[r] = __shfl(m, 4 * g + r); rr[r] = __shfl(rinv, 4 * g + r); }
      const int key = rbase + 4 * g;
      const size_t kboff = (size_t)(key >> 5) * 1280 + ((key >> 4) & 1) * 8;
#pragma unroll
      for (int j = 0; j < 4; ++j) {
        const int c = n0 - 2 * D_ + wn * 64 + j * 16 + (l & 15), head = c / 20, d = c - head * 20;
        float y[4];
#pragma unroll
        for (int r = 0; r < 4; ++r) y[r] = (gm.acc[i][j][r] - mr[r] * gm.cd[j]) * rr[r];
        char* p = ws + WS_V + (size_t)head * V_HEAD + kboff + (d < 16 ? (g * 16 + d) * 16 : 1024 + (g * 4 + d - 16) * 16);
        *(u32x2*)p = u32x2{pk2(y[0], y[1]), pk2(y[2], y[3])};
      }
    }
  }
}

__global__ __launch_bounds__(256) void k1_qkv(const char* x, const char* wqkv, char* ws) {
  extern __shared__ __attribute__((aligned(16))) char smem[];
  const int b = blockIdx.x, xcd = b & 7, j = b >> 3;
  const int rt = xcd * 2 + j / 15, ct = j % 15;
  if (ct >= 10) qkv_body<true>(smem, x, wqkv, ws, rt, ct);
  else qkv_body<false>(smem, x, wqkv, ws, rt, ct);
}

constexpr int AT_K = 0, AT_V = 24 * 768, AT_Q = AT_V + 12 * 1280, AT_W = AT_Q + 7 * 1024, AT_END = AT_W + 16384;
__global__ __launch_bounds__(256, 2) void k2_attn(const char* wo, const char* w1, const char* w2, const char* w3, const char* w4, char* ws) {
  __shared__ __attribute__((aligned(16))) char smem[AT_END];
  const int b = blockIdx.x, xcd = b & 7, j = b >> 3;
  const int qb = xcd * 2 + (j >> 5), head = j & 31;
  const int tid = threadIdx.x, l = tid & 63, g = l >> 4, li = l & 15;
  const int w = __builtin_amdgcn_readfirstlane(tid >> 6);
  const char* Kh = ws + WS_K + (size_t)head * QK_HEAD;
  const char* Vh = ws + WS_V + (size_t)head * V_HEAD;
  const char* Qh = ws + WS_Q + (size_t)head * QK_HEAD;
  const unsigned lb = lds_addr(smem);
  {
    const int koff0 = (qb * 8 - 16) * 768, voff0 = (qb * 4 - 8) * 1280;
#pragma unroll
    for (int i = 0; i < 10; ++i) {
      const int p = w + 4 * i;
      const char* src;
      if (p < 18) { const int o = koff0 + p * 1024; src = Kh + (o < 0 ? 0 : o); }
      else if (p < 33) { const int o = voff0 + (p - 18) * 1024; src = Vh + (o < 0 ? 0 : o); }
      else src = Qh + qb * 6144 + (p < 39 ? p - 33 : 0) * 1024;
      glds16(src, l * 16, lb + p * 1024);
    }
  }
  const bool prep = b < 390;
  char* wdst = nullptr;
  int wks = 20;
  if (prep) {
    const char* wsrc; int ldw = D_ * 2, kt, nt;
    if (b < 150) {
      const int mi = b / 50, u = b - mi * 50;
      wsrc = mi == 0 ? wo : (mi == 1 ? w1 : w2); wdst = ws + WS_WO + (size_t)mi * (D_ * D_ * 2);
      kt = u / 5; nt = u - kt * 5;
    } else if (b < 270) {
      const int u = b - 150; wsrc = w3; wdst = ws + WS_W3; ldw = F_ * 2; kt = u / 12; nt = u - kt * 12;
    } else {
      const int u = b - 270; wsrc = w4; wdst = ws + WS_W4; wks = 48; kt = u / 5; nt = u - kt * 5;
    }
    wsrc += (size_t)(kt * 64 + w * 16) * ldw + nt * 256;
    wdst += (size_t)((nt * 8 + (w & 1) * 4) * wks + kt * 2 + (w >> 1)) * 1024 + l * 16;
#pragma unroll
    for (int i = 0; i < 4; ++i) glds16(wsrc + (size_t)i * 4 * ldw, wrm_voff(l, i >> 1, ldw), lb + AT_W + w * 4096 + i * 1024);
    wait_vm<4>();
  } else {
    wait_vm<0>();
  }
  __builtin_amdgcn_s_barrier();
  cfence();
  bf16x8 qf[2];
#pragma unroll
  for (int i = 0; i < 2; ++i) {
    u32x4 v = *(const u32x4*)(smem + AT_Q + (2 * w + i) * 768 + (g < 3 ? g : 2) * 256 + li * 16);
    if (g == 3) v = u32x4{0u, 0u, 0u, 0u};
    qf[i] = __builtin_bit_cast(bf16x8, v);
  }
  const char* Ks = smem + 2 * w * 768 + (g < 3 ? g : 2) * 256 + li * 16;
  const int kbase = qb * 128 + 32 * w - 256;
  f32x4 s0[17], s1[17];
  const f32x4 z4 = {0.f, 0.f, 0.f, 0.f};
#pragma unroll
  for (int c = 0; c < 18; ++c) {
    bf16x8 kf = *(const bf16x8*)(Ks + c * 768);
    if (c < 17) s0[c] = mfma16(kf, qf[0], z4);
    if (c > 0) s1[c - 1] = mfma16(kf, qf[1], z4);
  }
  const float NEG = -__builtin_inff();
#pragma unroll
  for (int r = 0; r < 4; ++r) {
    const int kj = 4 * g + r;
    if (kj <= li) { s0[0][r] = NEG; s1[0][r] = NEG; }
    if (kj > li) { s0[16][r] = NEG; s1[16][r] = NEG; }
  }
  if (kbase < 0) {
#pragma unroll
    for (int c = 0; c < 17; ++c) {
      if (kbase + 16 * c < 0) s0[c] = f32x4{NEG, NEG, NEG, NEG};
      if (kbase + 16 * (c + 1) < 0) s1[c] = f32x4{NEG, NEG, NEG, NEG};
    }
  }
  float m0 = NEG, m1 = NEG;
#pragma unroll
  for (int c = 0; c < 17; ++c)
#pragma unroll
    for (int r = 0; r < 4; ++r) { m0 = fmaxf(m0, s0[c][r]); m1 = fmaxf(m1, s1[c][r]); }
  m0 = fmaxf(m0, __shfl_xor(m0, 16)); m0 = fmaxf(m0, __shfl_xor(m0, 32));
  m1 = fmaxf(m1, __shfl_xor(m1, 16)); m1 = fmaxf(m1, __shfl_xor(m1, 32));
  const float C2 = 0.22360679774997896f * 1.4426950408889634f;
  const float b0 = -m0 * C2, b1 = -m1 * C2;
  u32x2 p0[18], p1[18];
#pragma unroll
  for (int c = 0; c < 17; ++c) {
    float e[4], f[4];
#pragma unroll
    for (int r = 0; r < 4; ++r) {
      e[r] = __builtin_amdgcn_exp2f(fmaf(s0[c][r], C2, b0));
      f[r] = __builtin_amdgcn_exp2f(fmaf(s1[c][r], C2, b1));
    }
    p0[c] = u32x2{pk2(e[0], e[1]), pk2(e[2], e[3])};
    p1[c + 1] = u32x2{pk2(f[0], f[1]), pk2(f[2], f[3])};
  }
  p0[17] = u32x2{0u, 0u}; p1[0] = u32x2{0u, 0u};
  const char* Vs0 = smem + AT_V + w * 1280 + (g * 16 + li) * 16;
  const char* Vs1 = smem + AT_V + w * 1280 + 1024 + (g * 4 + (li & 3)) * 16;
  f32x4 o00 = z4, o01 = z4, o10 = z4, o11 = z4;
#pragma unroll
  for (int s = 0; s < 9; ++s) {
    bf16x8 v0 = *(const bf16x8*)(Vs0 + s * 1280);
    u32x4 v1u = *(const u32x4*)(Vs1 + s * 1280);
    if (li == 4) v1u = u32x4{0x3F803F80u, 0x3F803F80u, 0x3F803F80u, 0x3F803F80u};
    bf16x8 v1 = __builtin_bit_cast(bf16x8, v1u);
    u32x4 pa = {p0[2 * s][0], p0[2 * s][1], p0[2 * s + 1][0], p0[2 * s + 1][1]};
    u32x4 pb = {p1[2 * s][0], p1[2 * s][1], p1[2 * s + 1][0], p1[2 * s + 1][1]};
    bf16x8 pfa = __builtin_bit_cast(bf16x8, pa), pfb = __builtin_bit_cast(bf16x8, pb);
    o00 = mfma16(v0, pfa, o00); o01 = mfma16(v1, pfa, o01);
    o10 = mfma16(v0, pfb, o10); o11 = mfma16(v1, pfb, o11);
  }
  const float r0 = 1.0f / __shfl(o01[0], 16 + li), r1 = 1.0f / __shfl(o11[0], 16 + li);
  char* Of = ws + WS_O;
  const int q0 = qb * 128 + 32 * w + li, col = head * 20 + 4 * g;
  *(u32x2*)(Of + ft_off(q0, col, 20)) = u32x2{pk2(o00[0] * r0, o00[1] * r0), pk2(o00[2] * r0, o00[3] * r0)};
  *(u32x2*)(Of + ft_off(q0 + 16, col, 20)) = u32x2{pk2(o10[0] * r1, o10[1] * r1), pk2(o10[2] * r1, o10[3] * r1)};
  if (g == 0) {
    *(u32x2*)(Of + ft_off(q0, col + 16, 20)) = u32x2{pk2(o01[0] * r0, o01[1] * r0), pk2(o01[2] * r0, o01[3] * r0)};
    *(u32x2*)(Of + ft_off(q0 + 16, col + 16, 20)) = u32x2{pk2(o11[0] * r1, o11[1] * r1), pk2(o11[2] * r1, o11[3] * r1)};
  }
  if (prep) {
    wait_vm<0>();
    __builtin_amdgcn_s_barrier();
    cfence();
#pragma unroll
    for (int jj = 0; jj < 4; ++jj) {
      bf16x8 f = wrm_frag(smem + AT_W + (w >> 1) * 8192 + wrm_lane(l, w & 1, jj));
      *(bf16x8*)(wdst + (size_t)jj * wks * 1024) = f;
    }
  }
}

using G80 = Gemm<4, 5, 4, 1, 20, 4, 4, false>;
using G80s = Gemm<4, 5, 4, 1, 20, 4, 4, true>;
using G80L = Gemm<4, 5, 4, 1, 48, 4, 4, false>;
using G96 = Gemm<8, 6, 2, 2, 20, 2, 5, true>;

__global__ __launch_bounds__(256) void k3_wo(const char* x, char* ws) {
  extern __shared__ __attribute__((aligned(16))) char smem[];
  const int b = blockIdx.x, xcd = b & 7, jj = b >> 3, rt = xcd * 4 + (jj >> 3), ct = jj & 7;
  const int tid = threadIdx.x, l = tid & 63, wave = tid >> 6, g = l >> 4;
  const int row = rt * 64 + wave * 16 + (l & 15), n0 = ct * 80;
  u32x2 xr[5];
#pragma unroll
  for (int j = 0; j < 5; ++j) xr[j] = *(const u32x2*)(x + (size_t)row * (D_ * 2) + (n0 + j * 16 + 4 * g) * 2);
  G80 gm;
  gm.run(smem, ws + WS_O + (size_t)rt * 4 * 20 * 1024, ws + WS_WO + (size_t)ct * 5 * 20 * 1024);
#pragma unroll
  for (int j = 0; j < 5; ++j) {
    const f32x4 a = gm.acc[0][j];
    u32x2 v = {pk2(bf_lo(xr[j][0]) + bfr(a[0]), bf_hi(xr[j][0]) + bfr(a[1])), pk2(bf_lo(xr[j][1]) + bfr(a[2]), bf_hi(xr[j][1]) + bfr(a[3]))};
    *(u32x2*)(ws + WS_H + ft_off(row, n0 + j * 16 + 4 * g, 20)) = v;
  }
}

__global__ __launch_bounds__(256) void k4_w1(char* ws) {
  extern __shared__ __attribute__((aligned(16))) char smem[];
  const int b = blockIdx.x, xcd = b & 7, jj = b >> 3, rt = xcd * 4 + (jj >> 3), ct = jj & 7;
  const int tid = threadIdx.x, l = tid & 63, wave = tid >> 6, g = l >> 4;
  const int row = rt * 64 + wave * 16 + (l & 15), n0 = ct * 80;
  G80s gm;
  gm.run(smem, ws + WS_H + (size_t)rt * 4 * 20 * 1024, ws + WS_W1 + (size_t)ct * 5 * 20 * 1024);
  float mean, var, m1, s1;
  ln_stats(gm.rs[0], gm.rq[0], mean, var, m1, s1);
  const float r1 = 1.0f / s1;
  const float m2 = bfr((mean - m1) * r1), v2 = bfr(var * r1 * r1);
  const float s2 = bfr(sqrtf(bfr(v2 + 1e-5f)));
  const float mm = m1 + m2 * s1, rr = r1 / s2;
  if (ct == 0 && g == 0) *(float2*)(ws + WS_ST + row * 8) = float2{m1, r1};
  const int grp = ct >> 1;
  const int sh = (grp & 2) ? ((grp & 1) ? -2 : 2) : ((grp & 1) ? -1 : 1);
  const int drow = row + sh;
  const int zrow = sh > 0 ? row : row;
  const bool zfill = sh > 0 ? (row < sh) : (row >= S_ + sh);
#pragma unroll
  for (int j = 0; j < 5; ++j) {
    const f32x4 a = gm.acc[0][j];
    float y[4];
#pragma unroll
    for (int r = 0; r < 4; ++r) y[r] = gelu_f(bfr((a[r] - mm * gm.cs[j][r]) * rr));
    const int col = n0 + j * 16 + 4 * g;
    if (drow >= 0 && drow < S_) *(u32x2*)(ws + WS_G + ft_off(drow, col, 20)) = u32x2{pk2(y[0], y[1]), pk2(y[2], y[3])};
    if (zfill) *(u32x2*)(ws + WS_G + ft_off(zrow, col, 20)) = u32x2{0u, 0u};
  }
}

__global__ __launch_bounds__(256) void k5_w2(char* ws) {
  extern __shared__ __attribute__((aligned(16))) char smem[];
  const int b = blockIdx.x, xcd = b & 7, jj = b >> 3, rt = xcd * 4 + (jj >> 3), ct = jj & 7;
  const int tid = threadIdx.x, l = tid & 63, wave = tid >> 6, g = l >> 4;
  const int row = rt * 64 + wave * 16 + (l & 15), n0 = ct * 80;
  u32x2 hr[5];
#pragma unroll
  for (int j = 0; j < 5; ++j) hr[j] = *(const u32x2*)(ws + WS_H + ft_off(row, n0 + j * 16 + 4 * g, 20));
  const float2 st = *(const float2*)(ws + WS_ST + row * 8);
  G80 gm;
  gm.run(smem, ws + WS_G + (size_t)rt * 4 * 20 * 1024, ws + WS_W2 + (size_t)ct * 5 * 20 * 1024);
#pragma unroll
  for (int j = 0; j < 5; ++j) {
    const f32x4 a = gm.acc[0][j];
    float hv[4] = {bf_lo(hr[j][0]), bf_hi(hr[j][0]), bf_lo(hr[j][1]), bf_hi(hr[j][1])};
    float y[4];
#pragma unroll
    for (int r = 0; r < 4; ++r) y[r] = bfr(bfr(hv[r] - st.x) * st.y) + bfr(a[r]);
    *(u32x2*)(ws + WS_X1 + ft_off(row, n0 + j * 16 + 4 * g, 20)) = u32x2{pk2(y[0], y[1]), pk2(y[2], y[3])};
  }
}

__global__ __launch_bounds__(256) void k6_w3(char* ws) {
  extern __shared__ __attribute__((aligned(16))) char smem[];
  const int b = blockIdx.x, xcd = b & 7, jj = b >> 3, rt = xcd * 2 + (jj >> 4), ct = jj & 15;
  const int tid = threadIdx.x, l = tid & 63, wave = tid >> 6, wm = wave >> 1, wn = wave & 1, g = l >> 4;
  const int n0 = ct * 96 + wn * 48;
  G96 gm;
  gm.run(smem, ws + WS_X1 + (size_t)rt * 8 * 20 * 1024, ws + WS_W3 + (size_t)ct * 6 * 20 * 1024);
#pragma unroll
  for (int i = 0; i < 4; ++i) {
    const int row = rt * 128 + wm * 64 + i * 16 + (l & 15);
    float mean, var, m, s;
    ln_stats(gm.rs[i], gm.rq[i], mean, var, m, s);
    const float rinv = 1.0f / s;
#pragma unroll
    for (int j = 0; j < 3; ++j) {
      const f32x4 a = gm.acc[i][j];
      float y[4];
#pragma unroll
      for (int r = 0; r < 4; ++r) y[r] = gelu_f(bfr((a[r] - m * gm.cs[j][r]) * rinv));
      *(u32x2*)(ws + WS_U + ft_off(row, n0 + j * 16 + 4 * g, 48)) = u32x2{pk2(y[0], y[1]), pk2(y[2], y[3])};
    }
  }
}

__global__ __launch_bounds__(256) void k7_w4(const char* ws, char* out) {
  extern __shared__ __attribute__((aligned(16))) char smem[];
  const int b = blockIdx.x, xcd = b & 7, jj = b >> 3, rt = xcd * 4 + (jj >> 3), ct = jj & 7;
  const int tid = threadIdx.x, l = tid & 63, wave = tid >> 6, g = l >> 4;
  const int row = rt * 64 + wave * 16 + (l & 15), n0 = ct * 80;
  u32x2 hr[5], xr[5];
#pragma unroll
  for (int j = 0; j < 5; ++j) {
    hr[j] = *(const u32x2*)(ws + WS_H + ft_off(row, n0 + j * 16 + 4 * g, 20));
    xr[j] = *(const u32x2*)(ws + WS_X1 + ft_off(row, n0 + j * 16 + 4 * g, 20));
  }
  G80L gm;
  gm.run(smem, ws + WS_U + (size_t)rt * 4 * 48 * 1024, ws + WS_W4 + (size_t)ct * 5 * 48 * 1024);
#pragma unroll
  for (int j = 0; j < 5; ++j) {
    const f32x4 a = gm.acc[0][j];
    float hv[4] = {bf_lo(hr[j][0]), bf_hi(hr[j][0]), bf_lo(hr[j][1]), bf_hi(hr[j][1])};
    float xv[4] = {bf_lo(xr[j][0]), bf_hi(xr[j][0]), bf_lo(xr[j][1]), bf_hi(xr[j][1])};
    float y[4];
#pragma unroll
    for (int r = 0; r < 4; ++r) y[r] = hv[r] + bfr(xv[r] + bfr(a[r]));
    *(u32x2*)(out + (size_t)row * (D_ * 2) + (n0 + j * 16 + 4 * g) * 2) = u32x2{pk2(y[0], y[1]), pk2(y[2], y[3])};
  }
}

extern "C" void kernel_launch(void* const* d_in, const int* in_sizes, int n_in, void* d_out, int out_size,
                              void* d_ws, size_t ws_size, hipStream_t stream) {
  const char* x = (const char*)d_in[0];
  const char* wqkv = (const char*)d_in[1];
  const char* wo = (const char*)d_in[2];
  const char* w1 = (const char*)d_in[4];
  const char* w2 = (const char*)d_in[5];
  const char* w3 = (const char*)d_in[6];
  const char* w4 = (const char*)d_in[7];
  char* ws = (char*)d_ws;
  if (ws_size < WS_END) return;
  constexpr int L160 = 163840, L144 = 147456, L140 = 143360;
  (void)hipFuncSetAttribute((const void*)k1_qkv, hipFuncAttributeMaxDynamicSharedMemorySize, L160);
  (void)hipFuncSetAttribute((const void*)k3_wo, hipFuncAttributeMaxDynamicSharedMemorySize, L144);
  (void)hipFuncSetAttribute((const void*)k4_w1, hipFuncAttributeMaxDynamicSharedMemorySize, L144);
  (void)hipFuncSetAttribute((const void*)k5_w2, hipFuncAttributeMaxDynamicSharedMemorySize, L144);
  (void)hipFuncSetAttribute((const void*)k6_w3, hipFuncAttributeMaxDynamicSharedMemorySize, L140);
  (void)hipFuncSetAttribute((const void*)k7_w4, hipFuncAttributeMaxDynamicSharedMemorySize, L144);
  k1_qkv<<<240, 256, L160, stream>>>(x, wqkv, ws);
  k2_attn<<<512, 256, 0, stream>>>(wo, w1, w2, w3, w4, ws);
  k3_wo<<<256, 256, L144, stream>>>(x, ws);
  k4_w1<<<256, 256, L144, stream>>>(ws);
  k5_w2<<<256, 256, L144, stream>>>(ws);
  k6_w3<<<256, 256, L140, stream>>>(ws);
  k7_w4<<<256, 256, L144, stream>>>(ws, (char*)d_out);
}
```

```cpp
#include <hip/hip_runtime.h>
#include <hip/hip_bf16.h>
#include <stdint.h>
#include <type_traits>

typedef __attribute__((ext_vector_type(8))) __bf16 bf16x8;
typedef __attribute__((ext_vector_type(4))) __bf16 bf16x4;
typedef __attribute__((ext_vector_type(2))) __bf16 bf16x2;
typedef __attribute__((ext_vector_type(4))) short s16x4;
typedef __attribute__((ext_vector_type(4))) float f32x4;
typedef __attribute__((ext_vector_type(2))) unsigned u32x2;
typedef __attribute__((ext_vector_type(4))) unsigned u32x4;
#define LAS __attribute__((address_space(3)))
#define DEV __device__ __forceinline__

constexpr int S_ = 2048, D_ = 640, F_ = 1536, H_ = 32;

constexpr size_t QK_HEAD = 128 * 768;
constexpr size_t V_HEAD  = 64 * 1280;
constexpr size_t WS_Q  = 0;
constexpr size_t WS_K  = WS_Q + H_ * QK_HEAD;
constexpr size_t WS_V  = WS_K + H_ * QK_HEAD;
constexpr size_t WS_O  = WS_V + H_ * V_HEAD;
constexpr size_t WS_H  = WS_O + (size_t)S_ * D_ * 2;
constexpr size_t WS_G  = WS_H + (size_t)S_ * D_ * 2;
constexpr size_t WS_X1 = WS_G + (size_t)S_ * D_ * 2;
constexpr size_t WS_U  = WS_X1 + (size_t)S_ * D_ * 2;
constexpr size_t WS_ST = WS_U + (size_t)S_ * F_ * 2;
constexpr size_t WS_ST2 = WS_ST + (size_t)S_ * 64;
constexpr size_t WS_CS = WS_ST2 + (size_t)S_ * 64;
constexpr size_t WS_WO = WS_CS + 16384;
constexpr size_t WS_W1 = WS_WO + (size_t)D_ * D_ * 2;
constexpr size_t WS_W2 = WS_W1 + (size_t)D_ * D_ * 2;
constexpr size_t WS_W3 = WS_W2 + (size_t)D_ * D_ * 2;
constexpr size_t WS_W4 = WS_W3 + (size_t)D_ * F_ * 2;
constexpr size_t WS_END = WS_W4 + (size_t)D_ * F_ * 2;

template <int I, int N, class Fn> DEV void static_for(Fn&& f) {
  if constexpr (I < N) { f(std::integral_constant<int, I>{}); static_for<I + 1, N>(f); }
}
template <int N> DEV void wait_vm() { asm volatile("s_waitcnt vmcnt(%0) lgkmcnt(0)" ::"n"(N) : "memory"); }
DEV void glds16(const char* sbase, unsigned voff, unsigned lds) {
  asm volatile("s_mov_b32 m0, %0\n\ts_nop 0\n\tglobal_load_lds_dwordx4 %1, %2" ::"s"(lds), "v"(voff), "s"(sbase) : "memory");
}
DEV unsigned lds_addr(const void* p) { return (unsigned)(uintptr_t)(LAS const void*)p; }
#define ST(i)
#define STR(i)
#define STDECL
#define STDUMP(o, ws, kid)
DEV void wait_lgkm0() { asm volatile("s_waitcnt lgkmcnt(0)" ::: "memory"); }
DEV void cfence() { asm volatile("" ::: "memory"); }
DEV float bfr(float x) { return (float)(__bf16)x; }
DEV unsigned pk2(float a, float b) { bf16x2 v = {(__bf16)a, (__bf16)b}; return __builtin_bit_cast(unsigned, v); }
DEV float bf_lo(unsigned u) { return __builtin_bit_cast(float, u << 16); }
DEV float bf_hi(unsigned u) { return __builtin_bit_cast(float, u & 0xffff0000u); }
DEV float gelu_f(float x) {
  const float c0 = -2.0f * 0.7978845608028654f * 1.4426950408889634f;
  const float c1 = c0 * 0.044715f;
  float t = x * x;
  float e = __builtin_amdgcn_exp2f(x * fmaf(t, c1, c0));
  return x * __builtin_amdgcn_rcpf(1.0f + e);
}
DEV f32x4 mfma16(bf16x8 a, bf16x8 b, f32x4 c) { return __builtin_amdgcn_mfma_f32_16x16x32_bf16(a, b, c, 0, 0, 0); }
DEV unsigned ft_off(int row, int k, int KS) {
  return (unsigned)(((row >> 4) * KS + (k >> 5)) * 1024 + ((row & 15) + 16 * ((k >> 3) & 3)) * 16 + (k & 7) * 2);
}

DEV void ln_stats(float sum, float sq, float& mean, float& var, float& m, float& s) {
  mean = sum * (1.0f / D_);
  var = fmaxf(sq * (1.0f / D_) - mean * mean, 0.f);
  m = bfr(mean);
  s = bfr(sqrtf(bfr(bfr(var) + 1e-5f)));
}

DEV void dot_sum(float& s, bf16x8 f) {
  const bf16x2 one2 = {(__bf16)1.0f, (__bf16)1.0f};
#pragma unroll
  for (int e = 0; e < 4; ++e) { bf16x2 v = {f[2 * e], f[2 * e + 1]}; s = __builtin_amdgcn_fdot2_f32_bf16(v, one2, s, false); }
}
DEV void dot_sq(float& s, bf16x8 f) {
#pragma unroll
  for (int e = 0; e < 4; ++e) { bf16x2 v = {f[2 * e], f[2 * e + 1]}; s = __builtin_amdgcn_fdot2_f32_bf16(v, v, s, false); }
}
DEV float red_g(float v) { v += __shfl_xor(v, 16); v += __shfl_xor(v, 32); return v; }

template <int TM, int NT, int KS, int WIN, int RG, int D>
struct Gemm {
  static constexpr int NGRP = KS / 2, SB = 2 * NT * 1024;
  static_assert((RG >= NGRP || D < RG) && D <= NGRP && NT * D <= 63 && WIN <= KS, "geometry");
  f32x4 acc[TM][NT];
  STDECL;

  DEV bool run(char* smem, const char* Ab, const char* Wb) {
    STR(0); ST(1);
    const int tid = threadIdx.x, l = tid & 63;
    const int wave = __builtin_amdgcn_readfirstlane(tid >> 6);
    if (wave >= 4) {
      const int pw = wave - 4;
      const char* sw = Wb + pw * 1024;
      const unsigned dst0 = lds_addr(smem) + pw * NT * 1024;
      auto issue = [&](int grp) {
        const unsigned voff = l * 16 + grp * 2048;
#pragma unroll
        for (int i = 0; i < NT; ++i) glds16(sw + i * (KS * 1024), voff, dst0 + (grp % RG) * SB + i * 1024);
      };
      static_for<0, D>([&](auto gi) { issue(gi.value); });
      static_for<0, NGRP>([&](auto gi) {
        constexpr int grp = gi.value;
        constexpr int issued = grp + D < NGRP ? grp + D : NGRP;
        wait_vm<NT * (issued - grp - 1)>();
        __builtin_amdgcn_s_barrier();
        cfence();
        if constexpr (grp + D < NGRP) issue(grp + D);
      });
      return false;
    }
#pragma unroll
    for (int i = 0; i < TM; ++i)
#pragma unroll
      for (int j = 0; j < NT; ++j) acc[i][j] = f32x4{0.f, 0.f, 0.f, 0.f};
    const char* a_lane = Ab + (size_t)wave * (TM * KS * 1024) + l * 16;
    const char* w_lane = smem + l * 16;
    bf16x8 af[WIN][TM], wf[2][NT];
#pragma unroll
    for (int t = 0; t < WIN; ++t)
#pragma unroll
      for (int i = 0; i < TM; ++i) af[t][i] = *(const bf16x8*)(a_lane + (i * KS + t) * 1024);
    __builtin_amdgcn_s_barrier();
    cfence();
    ST(3);
#pragma unroll
    for (int j = 0; j < NT; ++j) wf[0][j] = *(const bf16x8*)(w_lane + j * 1024);
    static_for<0, KS>([&](auto ti) {
      constexpr int t = ti.value;
      if constexpr (t + 1 < KS) {
        if constexpr ((t + 1) % 2 == 0) {
          wait_lgkm0();
          __builtin_amdgcn_s_barrier();
          cfence();
          ST(3 + ((t + 1) / 2 < 18 ? (t + 1) / 2 : 18));
        }
        constexpr int so = (((t + 1) / 2) % RG) * SB + ((t + 1) & 1) * NT * 1024;
#pragma unroll
        for (int j = 0; j < NT; ++j) wf[(t + 1) & 1][j] = *(const bf16x8*)(w_lane + so + j * 1024);
      }
      __builtin_amdgcn_sched_barrier(0);
#pragma unroll
      for (int i = 0; i < TM; ++i)
#pragma unroll
        for (int j = 0; j < NT; ++j) acc[i][j] = mfma16(wf[t & 1][j], af[t % WIN][i], acc[i][j]);
      if constexpr (t + WIN < KS) {
#pragma unroll
        for (int i = 0; i < TM; ++i) af[t % WIN][i] = *(const bf16x8*)(a_lane + (i * KS + t + WIN) * 1024);
      }
      __builtin_amdgcn_sched_barrier(0);
    });
    ST(21);
    return true;
  }
};

DEV unsigned wrm_voff(int l, int q, int ldw) {
  return (unsigned)((l >> 4) * ldw + ((((l & 15) >> 1) ^ ((l >> 4) | (q << 2))) * 32) + (l & 1) * 16);
}
DEV bf16x8 wrm_frag(const char* p) {
  s16x4 lo = __builtin_amdgcn_ds_read_tr16_b64_v4i16((LAS s16x4*)(p));
  s16x4 hi = __builtin_amdgcn_ds_read_tr16_b64_v4i16((LAS s16x4*)(p + 1024));
  typedef __attribute__((ext_vector_type(8))) short s16x8;
  s16x8 v = __builtin_shufflevector(lo, hi, 0, 1, 2, 3, 4, 5, 6, 7);
  return __builtin_bit_cast(bf16x8, v);
}
DEV unsigned wrm_lane(int l, int jhi, int jj) {
  const int g = l >> 4, i = l & 15;
  return (unsigned)((8 * g + (i >> 2)) * 256 + ((((jhi ^ (g & 1)) << 2) | (jj ^ (i >> 2))) * 32) + (i & 3) * 8);
}

template <bool SWAP>
struct GemmQ {
  f32x4 acc[4][4];
  STDECL;
  static constexpr int NG = 4, D = 3, NGRP = 10, SB = 32768, XCH = NG * SB;
  DEV bool run(char* smem, const char* Ab, const char* Wb) {
    STR(0); ST(1);
    constexpr int LDA = D_ * 2, LDW = 3 * D_ * 2;
    const int tid = threadIdx.x, l = tid & 63;
    const int wave = __builtin_amdgcn_readfirstlane(tid >> 6);
    if (wave >= 4) {
      const bool isw = wave == 5;
      unsigned vo[2];
#pragma unroll
      for (int q = 0; q < 2; ++q)
        vo[q] = isw ? wrm_voff(l, q, LDW) : (unsigned)((l >> 3) * LDA + (((l & 7) ^ (q * 4 + (l >> 4))) * 16));
      const unsigned dst0 = lds_addr(smem) + (isw ? 16384 : 0);
      auto issue = [&](int grp) {
        if (isw) {
#pragma unroll
          for (int i = 0; i < 16; ++i) glds16(Wb + (size_t)(grp * 64 + 4 * i) * LDW, vo[(i >> 1) & 1], dst0 + (grp % NG) * SB + i * 1024);
        } else {
#pragma unroll
          for (int i = 0; i < 16; ++i) glds16(Ab + grp * 128 + i * 8 * LDA, vo[i & 1], dst0 + (grp % NG) * SB + i * 1024);
        }
      };
      static_for<0, D>([&](auto gi) { issue(gi.value); });
      static_for<0, NGRP>([&](auto gi) {
        constexpr int grp = gi.value;
        constexpr int issued = grp + D < NGRP ? grp + D : NGRP;
        wait_vm<16 * (issued - grp - 1)>();
        __builtin_amdgcn_s_barrier();
        cfence();
        if constexpr (grp + D < NGRP) issue(grp + D);
      });
      return false;
    }
    const int wm = wave >> 1, wn = wave & 1, g = l >> 4;
    float rs[2] = {0.f, 0.f}, rq[2] = {0.f, 0.f}, cd[2] = {0.f, 0.f};
#pragma unroll
    for (int i = 0; i < 4; ++i)
#pragma unroll
      for (int j = 0; j < 4; ++j) acc[i][j] = f32x4{0.f, 0.f, 0.f, 0.f};
    const unsigned a0 = (unsigned)((wm * 64 + (l & 15)) * 128 + ((g ^ ((l & 15) >> 1)) * 16));
    const char* al[2][2] = {{smem + a0 + wn * 4096, smem + a0 + 4096 - wn * 4096},
                            {smem + (a0 ^ 64u) + wn * 4096, smem + (a0 ^ 64u) + 4096 - wn * 4096}};
    const char* wl[4];
#pragma unroll
    for (int j = 0; j < 4; ++j) wl[j] = smem + 16384 + wrm_lane(l, wn, j ^ (2 * wm));
    bf16x8 af[2][4], wf[2][4];
    auto ldfrag = [&](auto ti) {
      constexpr int t = ti.value, kk = t & 1, so = ((t >> 1) % NG) * SB;
#pragma unroll
      for (int i = 0; i < 4; ++i) af[kk][i] = *(const bf16x8*)(al[kk][i >> 1] + so + (i & 1) * 2048);
#pragma unroll
      for (int j = 0; j < 4; ++j) wf[kk][j] = wrm_frag(wl[j] + so + kk * 8192);
    };
    __builtin_amdgcn_s_barrier();
    cfence();
    ST(3);
    ldfrag(std::integral_constant<int, 0>{});
    static_for<0, 2 * NGRP>([&](auto ti) {
      constexpr int t = ti.value, kk = t & 1;
      if constexpr (t + 1 < 2 * NGRP) {
        if constexpr (kk == 1) {
          wait_lgkm0();
          __builtin_amdgcn_s_barrier();
          cfence();
          ST(3 + (t + 1) / 2);
        }
        ldfrag(std::integral_constant<int, t + 1>{});
      }
      __builtin_amdgcn_sched_barrier(0);
#pragma unroll
      for (int i = 0; i < 4; ++i)
#pragma unroll
        for (int j = 0; j < 4; ++j)
          acc[i][j] = SWAP ? mfma16(wf[kk][j], af[kk][i], acc[i][j]) : mfma16(af[kk][i], wf[kk][j], acc[i][j]);
#pragma unroll
      for (int i = 0; i < 2; ++i) { dot_sum(rs[i], af[kk][i]); dot_sq(rq[i], af[kk][i]); dot_sum(cd[i], wf[kk][i]); }
#pragma unroll
      for (int q = 0; q < 8; ++q) {
        __builtin_amdgcn_sched_group_barrier(0x8, 2, 0);
        __builtin_amdgcn_sched_group_barrier(0x2, 3, 0);
      }
      asm volatile("" : "+v"(rs[0]), "+v"(rs[1]), "+v"(rq[0]), "+v"(rq[1]), "+v"(cd[0]), "+v"(cd[1]));
      __builtin_amdgcn_sched_barrier(0);
    });
    ST(21);
    float2* xs = (float2*)(smem + XCH);
    float* xc = (float*)(smem + XCH + 1024);
#pragma unroll
    for (int i = 0; i < 2; ++i) {
      const float a = red_g(rs[i]), b = red_g(rq[i]), c = red_g(cd[i]);
      float mean, var, m, sd;
      ln_stats(a, b, mean, var, m, sd);
      const float rinv = 1.0f / sd;
      if (l < 16) { xs[(wm * 4 + wn * 2 + i) * 16 + l] = float2{rinv, -m * rinv}; xc[(wn * 4 + wm * 2 + i) * 16 + l] = c; }
    }
    wait_lgkm0();
    __builtin_amdgcn_s_barrier();
    cfence();
    return true;
  }
};

template <bool ISV>
DEV void qkv_body(char* smem, const char* x, const char* wqkv, char* ws, int rt, int ct) {
  using GQ = GemmQ<!ISV>;
  GQ gm;
  const int row0 = rt * 128, n0 = ct * 128;
  if (!gm.run(smem, x + (size_t)row0 * (D_ * 2), wqkv + n0 * 2)) return;
  const int tid = threadIdx.x, l = tid & 63, wave = tid >> 6, wm = wave >> 1, wn = wave & 1, g = l >> 4;
  const float2* xs = (const float2*)(smem + GQ::XCH);
  const float* xc = (const float*)(smem + GQ::XCH + 1024);
  if constexpr (!ISV) {
    f32x4 cs[4];
    unsigned po[4];
#pragma unroll
    for (int j = 0; j < 4; ++j) {
      const int ja = wn * 4 + (j ^ (2 * wm));
      cs[j] = *(const f32x4*)(xc + ja * 16 + 4 * g);
      const int c = (n0 + ja * 16 + 4 * g) % D_, head = c / 20, d0 = c - head * 20;
      po[j] = (unsigned)(head * (int)QK_HEAD + (d0 >> 3) * 256 + (d0 & 7) * 2) | (d0 == 16 ? 1u : 0u);
    }
    char* dstb = ws + (ct < 5 ? WS_Q : WS_K) + (size_t)(row0 >> 4) * 768 + (l & 15) * 16;
#pragma unroll
    for (int i = 0; i < 4; ++i) {
      const int ia = wm * 4 + (i ^ (2 * wn));
      const float2 st = xs[ia * 16 + (l & 15)];
#pragma unroll
      for (int j = 0; j < 4; ++j) {
        const f32x4 y = gm.acc[i][j] * st.x + cs[j] * st.y;
        char* p = dstb + ia * 768 + (po[j] & ~1u);
        if (po[j] & 1u) *(u32x4*)p = u32x4{pk2(y[0], y[1]), pk2(y[2], y[3]), 0u, 0u};
        else *(u32x2*)p = u32x2{pk2(y[0], y[1]), pk2(y[2], y[3])};
      }
    }
  } else {
    float cd[4];
    unsigned po[4];
#pragma unroll
    for (int j = 0; j < 4; ++j) {
      const int ja = wn * 4 + (j ^ (2 * wm));
      cd[j] = xc[ja * 16 + (l & 15)];
      const int c = n0 - 2 * D_ + ja * 16 + (l & 15), head = c / 20, d = c - head * 20;
      po[j] = (unsigned)(head * (int)V_HEAD + (d < 16 ? (g * 16 + d) * 16 : 1024 + (g * 4 + d - 16) * 16));
    }
    char* dstb = ws + WS_V + (size_t)(row0 >> 5) * 1280;
#pragma unroll
    for (int i = 0; i < 4; ++i) {
      const int ia = wm * 4 + (i ^ (2 * wn));
      const f32x4 s01 = *(const f32x4*)(xs + ia * 16 + 4 * g), s23 = *(const f32x4*)(xs + ia * 16 + 4 * g + 2);
      const f32x4 rr = {s01[0], s01[2], s23[0], s23[2]}, nm = {s01[1], s01[3], s23[1], s23[3]};
#pragma unroll
      for (int j = 0; j < 4; ++j) {
        const f32x4 y = gm.acc[i][j] * rr + nm * cd[j];
        *(u32x2*)(dstb + (ia >> 1) * 1280 + (i & 1) * 8 + po[j]) = u32x2{pk2(y[0], y[1]), pk2(y[2], y[3])};
      }
    }
  }
  STDUMP(gm, ws, 1);
}

DEV void colsum_job(char* smem, const char* w1, const char* w3, float* cs, int blk) {
  const int tid = threadIdx.x, c = tid % 17, rg = tid / 17;
  const int gc = blk * 17 + c;
  const char* base = gc < 80 ? w1 + gc * 16 : w3 + (gc - 80) * 16;
  const int ld = gc < 80 ? D_ * 2 : F_ * 2;
  float a[8] = {0.f, 0.f, 0.f, 0.f, 0.f, 0.f, 0.f, 0.f};
  if (rg < 20) {
#pragma unroll 16
    for (int r = rg; r < D_; r += 20) {
      u32x4 v = *(const u32x4*)(base + (size_t)r * ld);
#pragma unroll
      for (int e = 0; e < 4; ++e) { a[2 * e] += bf_lo(v[e]); a[2 * e + 1] += bf_hi(v[e]); }
    }
  }
  float* sm = (float*)smem;
  if (rg < 20) {
#pragma unroll
    for (int e = 0; e < 8; ++e) sm[rg * 136 + c * 8 + e] = a[e];
  }
  __syncthreads();
  if (tid < 136) {
    float t = 0.f;
#pragma unroll
    for (int r = 0; r < 20; ++r) t += sm[r * 136 + tid];
    cs[blk * 136 + tid] = t;
  }
}

__global__ __launch_bounds__(384) void k1_qkv(const char* x, const char* wqkv, const char* w1, const char* w3, char* ws) {
  extern __shared__ __attribute__((aligned(16))) char smem[];
  const int b = blockIdx.x, xcd = b & 7, j = b >> 3;
  if (j >= 30) { colsum_job(smem, w1, w3, (float*)(ws + WS_CS), xcd * 2 + (j - 30)); return; }
  const int rt = xcd * 2 + j / 15, ct = j % 15;
  if (ct >= 10) qkv_body<true>(smem, x, wqkv, ws, rt, ct);
  else qkv_body<false>(smem, x, wqkv, ws, rt, ct);
}

constexpr int AT_K = 0, AT_V = 24 * 768, AT_Q = AT_V + 12 * 1280, AT_W = AT_Q + 7 * 1024, AT_END = AT_W + 16384;
__global__ __launch_bounds__(256, 2) void k2_attn(const char* wo, const char* w1, const char* w2, const char* w3, const char* w4, char* ws) {
  __shared__ __attribute__((aligned(16))) char smem[AT_END];
  const int b = blockIdx.x, xcd = b & 7, j = b >> 3;
  const int qb = xcd * 2 + (j >> 5), head = j & 31;
  const int tid = threadIdx.x, l = tid & 63, g = l >> 4, li = l & 15;
  const int w = __builtin_amdgcn_readfirstlane(tid >> 6);
  const char* Kh = ws + WS_K + (size_t)head * QK_HEAD;
  const char* Vh = ws + WS_V + (size_t)head * V_HEAD;
  const char* Qh = ws + WS_Q + (size_t)head * QK_HEAD;
  const unsigned lb = lds_addr(smem);
  {
    const int koff0 = (qb * 8 - 16) * 768, voff0 = (qb * 4 - 8) * 1280;
#pragma unroll
    for (int i = 0; i < 10; ++i) {
      const int p = w + 4 * i;
      const char* src;
      if (p < 18) { const int o = koff0 + p * 1024; src = Kh + (o < 0 ? 0 : o); }
      else if (p < 33) { const int o = voff0 + (p - 18) * 1024; src = Vh + (o < 0 ? 0 : o); }
      else src = Qh + qb * 6144 + (p < 39 ? p - 33 : 0) * 1024;
      glds16(src, l * 16, lb + p * 1024);
    }
  }
  const bool prep = b < 390;
  char* wdst = nullptr;
  int wks = 20;
  if (prep) {
    const char* wsrc; int ldw = D_ * 2, kt, nt;
    if (b < 150) {
      const int mi = b / 50, u = b - mi * 50;
      wsrc = mi == 0 ? wo : (mi == 1 ? w1 : w2); wdst = ws + WS_WO + (size_t)mi * (D_ * D_ * 2);
      kt = u / 5; nt = u - kt * 5;
    } else if (b < 270) {
      const int u = b - 150; wsrc = w3; wdst = ws + WS_W3; ldw = F_ * 2; kt = u / 12; nt = u - kt * 12;
    } else {
      const int u = b - 270; wsrc = w4; wdst = ws + WS_W4; wks = 48; kt = u / 5; nt = u - kt * 5;
    }
    wsrc += (size_t)(kt * 64 + w * 16) * ldw + nt * 256;
    wdst += (size_t)((nt * 8 + (w & 1) * 4) * wks + kt * 2 + (w >> 1)) * 1024 + l * 16;
#pragma unroll
    for (int i = 0; i < 4; ++i) glds16(wsrc + (size_t)i * 4 * ldw, wrm_voff(l, i >> 1, ldw), lb + AT_W + w * 4096 + i * 1024);
    wait_vm<4>();
  } else {
    wait_vm<0>();
  }
  __builtin_amdgcn_s_barrier();
  cfence();
  bf16x8 qf[2];
#pragma unroll
  for (int i = 0; i < 2; ++i) {
    u32x4 v = *(const u32x4*)(smem + AT_Q + (2 * w + i) * 768 + (g < 3 ? g : 2) * 256 + li * 16);
    if (g == 3) v = u32x4{0u, 0u, 0u, 0u};
    qf[i] = __builtin_bit_cast(bf16x8, v);
  }
  const char* Ks = smem + 2 * w * 768 + (g < 3 ? g : 2) * 256 + li * 16;
  const int kbase = qb * 128 + 32 * w - 256;
  f32x4 s0[17], s1[17];
  const f32x4 z4 = {0.f, 0.f, 0.f, 0.f};
#pragma unroll
  for (int c = 0; c < 18; ++c) {
    bf16x8 kf = *(const bf16x8*)(Ks + c * 768);
    if (c < 17) s0[c] = mfma16(kf, qf[0], z4);
    if (c > 0) s1[c - 1] = mfma16(kf, qf[1], z4);
  }
  const float NEG = -__builtin_inff();
#pragma unroll
  for (int r = 0; r < 4; ++r) {
    const int kj = 4 * g + r;
    if (kj <= li) { s0[0][r] = NEG; s1[0][r] = NEG; }
    if (kj > li) { s0[16][r] = NEG; s1[16][r] = NEG; }
  }
  if (kbase < 0) {
#pragma unroll
    for (int c = 0; c < 17; ++c) {
      if (kbase + 16 * c < 0) s0[c] = f32x4{NEG, NEG, NEG, NEG};
      if (kbase + 16 * (c + 1) < 0) s1[c] = f32x4{NEG, NEG, NEG, NEG};
    }
  }
  float m0 = NEG, m1 = NEG;
#pragma unroll
  for (int c = 0; c < 17; ++c)
#pragma unroll
    for (int r = 0; r < 4; ++r) { m0 = fmaxf(m0, s0[c][r]); m1 = fmaxf(m1, s1[c][r]); }
  m0 = fmaxf(m0, __shfl_xor(m0, 16)); m0 = fmaxf(m0, __shfl_xor(m0, 32));
  m1 = fmaxf(m1, __shfl_xor(m1, 16)); m1 = fmaxf(m1, __shfl_xor(m1, 32));
  const float C2 = 0.22360679774997896f * 1.4426950408889634f;
  const float b0 = -m0 * C2, b1 = -m1 * C2;
  u32x2 p0[18], p1[18];
#pragma unroll
  for (int c = 0; c < 17; ++c) {
    float e[4], f[4];
#pragma unroll
    for (int r = 0; r < 4; ++r) {
      e[r] = __builtin_amdgcn_exp2f(fmaf(s0[c][r], C2, b0));
      f[r] = __builtin_amdgcn_exp2f(fmaf(s1[c][r], C2, b1));
    }
    p0[c] = u32x2{pk2(e[0], e[1]), pk2(e[2], e[3])};
    p1[c + 1] = u32x2{pk2(f[0], f[1]), pk2(f[2], f[3])};
  }
  p0[17] = u32x2{0u, 0u}; p1[0] = u32x2{0u, 0u};
  const char* Vs0 = smem + AT_V + w * 1280 + (g * 16 + li) * 16;
  const char* Vs1 = smem + AT_V + w * 1280 + 1024 + (g * 4 + (li & 3)) * 16;
  f32x4 o00 = z4, o01 = z4, o10 = z4, o11 = z4;
#pragma unroll
  for (int s = 0; s < 9; ++s) {
    bf16x8 v0 = *(const bf16x8*)(Vs0 + s * 1280);
    u32x4 v1u = *(const u32x4*)(Vs1 + s * 1280);
    if (li == 4) v1u = u32x4{0x3F803F80u, 0x3F803F80u, 0x3F803F80u, 0x3F803F80u};
    bf16x8 v1 = __builtin_bit_cast(bf16x8, v1u);
    u32x4 pa = {p0[2 * s][0], p0[2 * s][1], p0[2 * s + 1][0], p0[2 * s + 1][1]};
    u32x4 pb = {p1[2 * s][0], p1[2 * s][1], p1[2 * s + 1][0], p1[2 * s + 1][1]};
    bf16x8 pfa = __builtin_bit_cast(bf16x8, pa), pfb = __builtin_bit_cast(bf16x8, pb);
    o00 = mfma16(v0, pfa, o00); o01 = mfma16(v1, pfa, o01);
    o10 = mfma16(v0, pfb, o10); o11 = mfma16(v1, pfb, o11);
  }
  const float r0 = 1.0f / __shfl(o01[0], 16 + li), r1 = 1.0f / __shfl(o11[0], 16 + li);
  char* Of = ws + WS_O;
  const int q0 = qb * 128 + 32 * w + li, col = head * 20 + 4 * g;
  *(u32x2*)(Of + ft_off(q0, col, 20)) = u32x2{pk2(o00[0] * r0, o00[1] * r0), pk2(o00[2] * r0, o00[3] * r0)};
  *(u32x2*)(Of + ft_off(q0 + 16, col, 20)) = u32x2{pk2(o10[0] * r1, o10[1] * r1), pk2(o10[2] * r1, o10[3] * r1)};
  if (g == 0) {
    *(u32x2*)(Of + ft_off(q0, col + 16, 20)) = u32x2{pk2(o01[0] * r0, o01[1] * r0), pk2(o01[2] * r0, o01[3] * r0)};
    *(u32x2*)(Of + ft_off(q0 + 16, col + 16, 20)) = u32x2{pk2(o11[0] * r1, o11[1] * r1), pk2(o11[2] * r1, o11[3] * r1)};
  }
  if (prep) {
    wait_vm<0>();
    __builtin_amdgcn_s_barrier();
    cfence();
#pragma unroll
    for (int jj = 0; jj < 4; ++jj) {
      bf16x8 f = wrm_frag(smem + AT_W + (w >> 1) * 8192 + wrm_lane(l, w & 1, jj));
      *(bf16x8*)(wdst + (size_t)jj * wks * 1024) = f;
    }
  }
}

using G80 = Gemm<1, 5, 20, 20, 10, 5>;
using G80L = Gemm<1, 5, 48, 24, 14, 6>;
using G96 = Gemm<2, 6, 20, 10, 10, 5>;
typedef __attribute__((ext_vector_type(2))) float f32x2;
DEV f32x2 unpk(unsigned u) { return f32x2{bf_lo(u), bf_hi(u)}; }
DEV unsigned pk2v(f32x2 v) { return pk2(v[0], v[1]); }
DEV f32x2 bfr2(f32x2 v) { return unpk(pk2v(v)); }
DEV f32x2 gelu2(f32x2 x) {
  const float c0 = -2.0f * 0.7978845608028654f * 1.4426950408889634f, c1 = c0 * 0.044715f;
  const f32x2 z = x * (x * x * c1 + c0);
  const f32x2 d = f32x2{__builtin_amdgcn_exp2f(z[0]), __builtin_amdgcn_exp2f(z[1])} + 1.0f;
  return x * f32x2{__builtin_amdgcn_rcpf(d[0]), __builtin_amdgcn_rcpf(d[1])};
}
DEV f32x2 lo2(f32x4 v) { return f32x2{v[0], v[1]}; }
DEV f32x2 hi2(f32x4 v) { return f32x2{v[2], v[3]}; }

DEV void put_stats(char* stb, int row, int ct, int g, f32x2 s, f32x2 q) {
  const float a = red_g(s[0] + s[1]), b = red_g(q[0] + q[1]);
  if (g == 0) *(float2*)(stb + row * 64 + ct * 8) = float2{a, b};
}
DEV void acc_stats(f32x2& s, f32x2& q, unsigned u) { const f32x2 v = unpk(u); s += v; q += v * v; }

__global__ __launch_bounds__(384) void k3_wo(const char* x, char* ws) {
  extern __shared__ __attribute__((aligned(16))) char smem[];
  const int b = blockIdx.x, xcd = b & 7, jj = b >> 3, rt = xcd * 4 + (jj >> 3), ct = jj & 7;
  const int tid = threadIdx.x, l = tid & 63, wave = tid >> 6, g = l >> 4;
  const int row = rt * 64 + (wave & 3) * 16 + (l & 15), n0 = ct * 80;
  u32x2 xr[5];
  if (wave < 4) {
#pragma unroll
    for (int j = 0; j < 5; ++j) xr[j] = *(const u32x2*)(x + (size_t)row * (D_ * 2) + (n0 + j * 16 + 4 * g) * 2);
  }
  G80 gm;
  if (!gm.run(smem, ws + WS_O + (size_t)rt * 4 * 20 * 1024, ws + WS_WO + (size_t)ct * 5 * 20 * 1024)) return;
  f32x2 ss = {0.f, 0.f}, sq = {0.f, 0.f};
#pragma unroll
  for (int j = 0; j < 5; ++j) {
    const f32x4 a = gm.acc[0][j];
    u32x2 v = {pk2v(unpk(xr[j][0]) + bfr2(lo2(a))), pk2v(unpk(xr[j][1]) + bfr2(hi2(a)))};
    *(u32x2*)(ws + WS_H + ft_off(row, n0 + j * 16 + 4 * g, 20)) = v;
    acc_stats(ss, sq, v[0]); acc_stats(ss, sq, v[1]);
  }
  put_stats(ws + WS_ST, row, ct, g, ss, sq);
  STDUMP(gm, ws, 3);
}

__global__ __launch_bounds__(384) void k4_w1(char* ws) {
  extern __shared__ __attribute__((aligned(16))) char smem[];
  const int b = blockIdx.x, xcd = b & 7, jj = b >> 3, rt = xcd * 4 + (jj >> 3), ct = jj & 7;
  const int tid = threadIdx.x, l = tid & 63, wave = tid >> 6, g = l >> 4;
  const int row = rt * 64 + (wave & 3) * 16 + (l & 15), n0 = ct * 80;
  f32x4 cs[5], ps;
  if (wave < 4) {
#pragma unroll
    for (int j = 0; j < 5; ++j) cs[j] = *(const f32x4*)(ws + WS_CS + (n0 + j * 16 + 4 * g) * 4);
    ps = *(const f32x4*)(ws + WS_ST + row * 64 + g * 16);
  }
  G80 gm;
  if (!gm.run(smem, ws + WS_H + (size_t)rt * 4 * 20 * 1024, ws + WS_W1 + (size_t)ct * 5 * 20 * 1024)) return;
  float mean, var, m1, s1;
  ln_stats(red_g(ps[0] + ps[2]), red_g(ps[1] + ps[3]), mean, var, m1, s1);
  const float r1 = 1.0f / s1;
  const float m2 = bfr((mean - m1) * r1), v2 = bfr(var * r1 * r1);
  const float s2 = bfr(sqrtf(bfr(v2 + 1e-5f)));
  const float rr = r1 / s2, nm = -(m1 + m2 * s1) * rr;
  const int grp = ct >> 1;
  const int sh = (grp & 2) ? ((grp & 1) ? -2 : 2) : ((grp & 1) ? -1 : 1);
  const int drow = row + sh;
  const bool zfill = sh > 0 ? (row < sh) : (row >= S_ + sh);
#pragma unroll
  for (int j = 0; j < 5; ++j) {
    const f32x4 y = gm.acc[0][j] * rr + cs[j] * nm;
    const u32x2 v = {pk2v(gelu2(bfr2(lo2(y)))), pk2v(gelu2(bfr2(hi2(y))))};
    const int col = n0 + j * 16 + 4 * g;
    if (drow >= 0 && drow < S_) *(u32x2*)(ws + WS_G + ft_off(drow, col, 20)) = v;
    if (zfill) *(u32x2*)(ws + WS_G + ft_off(row, col, 20)) = u32x2{0u, 0u};
  }
  STDUMP(gm, ws, 4);
}

__global__ __launch_bounds__(384) void k5_w2(char* ws) {
  extern __shared__ __attribute__((aligned(16))) char smem[];
  const int b = blockIdx.x, xcd = b & 7, jj = b >> 3, rt = xcd * 4 + (jj >> 3), ct = jj & 7;
  const int tid = threadIdx.x, l = tid & 63, wave = tid >> 6, g = l >> 4;
  const int row = rt * 64 + (wave & 3) * 16 + (l & 15), n0 = ct * 80;
  u32x2 hr[5];
  f32x4 ps;
  if (wave < 4) {
#pragma unroll
    for (int j = 0; j < 5; ++j) hr[j] = *(const u32x2*)(ws + WS_H + ft_off(row, n0 + j * 16 + 4 * g, 20));
    ps = *(const f32x4*)(ws + WS_ST + row * 64 + g * 16);
  }
  G80 gm;
  if (!gm.run(smem, ws + WS_G + (size_t)rt * 4 * 20 * 1024, ws + WS_W2 + (size_t)ct * 5 * 20 * 1024)) return;
  float mean, var, m1, s1;
  ln_stats(red_g(ps[0] + ps[2]), red_g(ps[1] + ps[3]), mean, var, m1, s1);
  const float r1 = 1.0f / s1;
  f32x2 ss = {0.f, 0.f}, sq = {0.f, 0.f};
#pragma unroll
  for (int j = 0; j < 5; ++j) {
    const f32x4 a = gm.acc[0][j];
    const f32x2 y0 = bfr2(bfr2(unpk(hr[j][0]) - m1) * r1) + bfr2(lo2(a));
    const f32x2 y1 = bfr2(bfr2(unpk(hr[j][1]) - m1) * r1) + bfr2(hi2(a));
    const u32x2 v = {pk2v(y0), pk2v(y1)};
    *(u32x2*)(ws + WS_X1 + ft_off(row, n0 + j * 16 + 4 * g, 20)) = v;
    acc_stats(ss, sq, v[0]); acc_stats(ss, sq, v[1]);
  }
  put_stats(ws + WS_ST2, row, ct, g, ss, sq);
  STDUMP(gm, ws, 5);
}

__global__ __launch_bounds__(384) void k6_w3(char* ws) {
  extern __shared__ __attribute__((aligned(16))) char smem[];
  const int b = blockIdx.x, xcd = b & 7, jj = b >> 3, rt = xcd * 2 + (jj >> 4), ct = jj & 15;
  const int tid = threadIdx.x, l = tid & 63, wave = tid >> 6, g = l >> 4;
  const int n0 = ct * 96, row0 = rt * 128 + (wave & 3) * 32 + (l & 15);
  f32x4 ps[2];
  if (wave < 4) {
#pragma unroll
    for (int i = 0; i < 2; ++i) ps[i] = *(const f32x4*)(ws + WS_ST2 + (row0 + i * 16) * 64 + g * 16);
  }
  G96 gm;
  if (!gm.run(smem, ws + WS_X1 + (size_t)rt * 8 * 20 * 1024, ws + WS_W3 + (size_t)ct * 6 * 20 * 1024)) return;
  f32x4 cs[6];
#pragma unroll
  for (int j = 0; j < 6; ++j) cs[j] = *(const f32x4*)(ws + WS_CS + (D_ + n0 + j * 16 + 4 * g) * 4);
#pragma unroll
  for (int i = 0; i < 2; ++i) {
    float mean, var, m, s;
    ln_stats(red_g(ps[i][0] + ps[i][2]), red_g(ps[i][1] + ps[i][3]), mean, var, m, s);
    const float rinv = 1.0f / s, nm = -m * rinv;
#pragma unroll
    for (int j = 0; j < 6; ++j) {
      const f32x4 y = gm.acc[i][j] * rinv + cs[j] * nm;
      *(u32x2*)(ws + WS_U + ft_off(row0 + i * 16, n0 + j * 16 + 4 * g, 48)) = u32x2{pk2v(gelu2(bfr2(lo2(y)))), pk2v(gelu2(bfr2(hi2(y))))};
    }
  }
  STDUMP(gm, ws, 6);
}

__global__ __launch_bounds__(384) void k7_w4(char* ws, char* out) {
  extern __shared__ __attribute__((aligned(16))) char smem[];
  const int b = blockIdx.x, xcd = b & 7, jj = b >> 3, rt = xcd * 4 + (jj >> 3), ct = jj & 7;
  const int tid = threadIdx.x, l = tid & 63, wave = tid >> 6, g = l >> 4;
  const int row = rt * 64 + (wave & 3) * 16 + (l & 15), n0 = ct * 80;
  u32x2 hr[5], xr[5];
  if (wave < 4) {
#pragma unroll
    for (int j = 0; j < 5; ++j) {
      hr[j] = *(const u32x2*)(ws + WS_H + ft_off(row, n0 + j * 16 + 4 * g, 20));
      xr[j] = *(const u32x2*)(ws + WS_X1 + ft_off(row, n0 + j * 16 + 4 * g, 20));
    }
  }
  G80L gm;
  if (!gm.run(smem, ws + WS_U + (size_t)rt * 4 * 48 * 1024, ws + WS_W4 + (size_t)ct * 5 * 48 * 1024)) return;
#pragma unroll
  for (int j = 0; j < 5; ++j) {
    const f32x4 a = gm.acc[0][j];
    const f32x2 y0 = unpk(hr[j][0]) + bfr2(unpk(xr[j][0]) + bfr2(lo2(a)));
    const f32x2 y1 = unpk(hr[j][1]) + bfr2(unpk(xr[j][1]) + bfr2(hi2(a)));
    *(u32x2*)(out + (size_t)row * (D_ * 2) + (n0 + j * 16 + 4 * g) * 2) = u32x2{pk2v(y0), pk2v(y1)};
  }
  STDUMP(gm, ws, 7);
}

extern "C" void kernel_launch(void* const* d_in, const int* in_sizes, int n_in, void* d_out, int out_size,
                              void* d_ws, size_t ws_size, hipStream_t stream) {
  const char* x = (const char*)d_in[0];
  const char* wqkv = (const char*)d_in[1];
  const char* wo = (const char*)d_in[2];
  const char* w1 = (const char*)d_in[4];
  const char* w2 = (const char*)d_in[5];
  const char* w3 = (const char*)d_in[6];
  const char* w4 = (const char*)d_in[7];
  char* ws = (char*)d_ws;
  if (ws_size < WS_END) return;
  constexpr int LQ = 4 * 32768 + 2048, L80 = 100 * 1024, L96 = 120 * 1024, L80L = 140 * 1024;
  (void)hipFuncSetAttribute((const void*)k1_qkv, hipFuncAttributeMaxDynamicSharedMemorySize, LQ);
  (void)hipFuncSetAttribute((const void*)k3_wo, hipFuncAttributeMaxDynamicSharedMemorySize, L80);
  (void)hipFuncSetAttribute((const void*)k4_w1, hipFuncAttributeMaxDynamicSharedMemorySize, L80);
  (void)hipFuncSetAttribute((const void*)k5_w2, hipFuncAttributeMaxDynamicSharedMemorySize, L80);
  (void)hipFuncSetAttribute((const void*)k6_w3, hipFuncAttributeMaxDynamicSharedMemorySize, L96);
  (void)hipFuncSetAttribute((const void*)k7_w4, hipFuncAttributeMaxDynamicSharedMemorySize, L80L);
  k1_qkv<<<256, 384, LQ, stream>>>(x, wqkv, w1, w3, ws);
  k2_attn<<<512, 256, 0, stream>>>(wo, w1, w2, w3, w4, ws);
  k3_wo<<<256, 384, L80, stream>>>(x, ws);
  k4_w1<<<256, 384, L80, stream>>>(ws);
  k5_w2<<<256, 384, L80, stream>>>(ws);
  k6_w3<<<256, 384, L96, stream>>>(ws);
  k7_w4<<<256, 384, L80L, stream>>>(ws, (char*)d_out);
}
```

```cpp
#include <hip/hip_runtime.h>
#include <hip/hip_bf16.h>
#include <stdint.h>
#include <type_traits>

typedef __attribute__((ext_vector_type(8))) __bf16 bf16x8;
typedef __attribute__((ext_vector_type(4))) __bf16 bf16x4;
typedef __attribute__((ext_vector_type(2))) __bf16 bf16x2;
typedef __attribute__((ext_vector_type(4))) short s16x4;
typedef __attribute__((ext_vector_type(4))) float f32x4;
typedef __attribute__((ext_vector_type(2))) unsigned u32x2;
typedef __attribute__((ext_vector_type(4))) unsigned u32x4;
#define LAS __attribute__((address_space(3)))
#define DEV __device__ __forceinline__

constexpr int S_ = 2048, D_ = 640, F_ = 1536, H_ = 32;

constexpr size_t QK_HEAD = 128 * 768;
constexpr size_t V_HEAD  = 64 * 1280;
constexpr size_t WS_Q  = 0;
constexpr size_t WS_K  = WS_Q + H_ * QK_HEAD;
constexpr size_t WS_V  = WS_K + H_ * QK_HEAD;
constexpr size_t WS_O  = WS_V + H_ * V_HEAD;
constexpr size_t WS_H  = WS_O + (size_t)S_ * D_ * 2;
constexpr size_t WS_G  = WS_H + (size_t)S_ * D_ * 2;
constexpr size_t WS_X1 = WS_G + (size_t)S_ * D_ * 2;
constexpr size_t WS_U  = WS_X1 + (size_t)S_ * D_ * 2;
constexpr size_t WS_ST = WS_U + (size_t)S_ * F_ * 2;
constexpr size_t WS_ST2 = WS_ST + (size_t)S_ * 64;
constexpr size_t WS_CS = WS_ST2 + (size_t)S_ * 64;
constexpr size_t WS_WO = WS_CS + 16384;
constexpr size_t WS_W1 = WS_WO + (size_t)D_ * D_ * 2;
constexpr size_t WS_W2 = WS_W1 + (size_t)D_ * D_ * 2;
constexpr size_t WS_W3 = WS_W2 + (size_t)D_ * D_ * 2;
constexpr size_t WS_W4 = WS_W3 + (size_t)D_ * F_ * 2;
constexpr size_t WS_FL = WS_W4 + (size_t)D_ * F_ * 2;
constexpr size_t FL_A = WS_FL;
constexpr size_t FL_3 = FL_A + 64 * 128;
constexpr size_t FL_4 = FL_3 + 128 * 128;
constexpr size_t FL_5 = FL_4 + 128 * 128;
constexpr size_t FL_6 = FL_5 + 128 * 128;
constexpr size_t WS_END = FL_6 + 64 * 128;
constexpr int FL_BYTES = (int)(WS_END - WS_FL);

template <int I, int N, class Fn> DEV void static_for(Fn&& f) {
  if constexpr (I < N) { f(std::integral_constant<int, I>{}); static_for<I + 1, N>(f); }
}
template <int N> DEV void wait_vm() { asm volatile("s_waitcnt vmcnt(%0) lgkmcnt(0)" ::"n"(N) : "memory"); }
DEV void glds16(const char* sbase, unsigned voff, unsigned lds) {
  asm volatile("s_mov_b32 m0, %0\n\ts_nop 0\n\tglobal_load_lds_dwordx4 %1, %2" ::"s"(lds), "v"(voff), "s"(sbase) : "memory");
}
DEV unsigned lds_addr(const void* p) { return (unsigned)(uintptr_t)(LAS const void*)p; }
#define ST(i)
#define STR(i)
#define STDECL
#define STDUMP(o, ws, kid)
#define STARG
#define STPASS(i)
#define STDUMP2(ws, kid)
DEV void wait_lgkm0() { asm volatile("s_waitcnt lgkmcnt(0)" ::: "memory"); }
DEV void cfence() { asm volatile("" ::: "memory"); }
DEV float bfr(float x) { return (float)(__bf16)x; }
DEV unsigned pk2(float a, float b) { bf16x2 v = {(__bf16)a, (__bf16)b}; return __builtin_bit_cast(unsigned, v); }
DEV float bf_lo(unsigned u) { return __builtin_bit_cast(float, u << 16); }
DEV float bf_hi(unsigned u) { return __builtin_bit_cast(float, u & 0xffff0000u); }
DEV f32x4 mfma16(bf16x8 a, bf16x8 b, f32x4 c) { return __builtin_amdgcn_mfma_f32_16x16x32_bf16(a, b, c, 0, 0, 0); }
DEV unsigned ft_off(int row, int k, int KS) {
  return (unsigned)(((row >> 4) * KS + (k >> 5)) * 1024 + ((row & 15) + 16 * ((k >> 3) & 3)) * 16 + (k & 7) * 2);
}

DEV void ln_stats(float sum, float sq, float& mean, float& var, float& m, float& s) {
  mean = sum * (1.0f / D_);
  var = fmaxf(sq * (1.0f / D_) - mean * mean, 0.f);
  m = bfr(mean);
  s = bfr(sqrtf(bfr(bfr(var) + 1e-5f)));
}

DEV void dot_sum(float& s, bf16x8 f) {
  const bf16x2 one2 = {(__bf16)1.0f, (__bf16)1.0f};
#pragma unroll
  for (int e = 0; e < 4; ++e) { bf16x2 v = {f[2 * e], f[2 * e + 1]}; s = __builtin_amdgcn_fdot2_f32_bf16(v, one2, s, false); }
}
DEV void dot_sq(float& s, bf16x8 f) {
#pragma unroll
  for (int e = 0; e < 4; ++e) { bf16x2 v = {f[2 * e], f[2 * e + 1]}; s = __builtin_amdgcn_fdot2_f32_bf16(v, v, s, false); }
}
DEV float red_g(float v) { v += __shfl_xor(v, 16); v += __shfl_xor(v, 32); return v; }

DEV unsigned wrm_voff(int l, int q, int ldw) {
  return (unsigned)((l >> 4) * ldw + ((((l & 15) >> 1) ^ ((l >> 4) | (q << 2))) * 32) + (l & 1) * 16);
}
DEV bf16x8 wrm_frag(const char* p) {
  s16x4 lo = __builtin_amdgcn_ds_read_tr16_b64_v4i16((LAS s16x4*)(p));
  s16x4 hi = __builtin_amdgcn_ds_read_tr16_b64_v4i16((LAS s16x4*)(p + 1024));
  typedef __attribute__((ext_vector_type(8))) short s16x8;
  s16x8 v = __builtin_shufflevector(lo, hi, 0, 1, 2, 3, 4, 5, 6, 7);
  return __builtin_bit_cast(bf16x8, v);
}
DEV unsigned wrm_lane(int l, int jhi, int jj) {
  const int g = l >> 4, i = l & 15;
  return (unsigned)((8 * g + (i >> 2)) * 256 + ((((jhi ^ (g & 1)) << 2) | (jj ^ (i >> 2))) * 32) + (i & 3) * 8);
}

struct WJob { const char* src; char* dst; int ldw, wks; };
DEV WJob make_job(int id, const char* wo, const char* w1, const char* w2, const char* w3, const char* w4, char* ws) {
  const bool live = id < 390;
  const int b = live ? id : 389;
  const char* wsrc; char* wdst; int ldw = D_ * 2, wks = 20, kt, nt;
  if (b < 150) {
    const int mi = b / 50, u = b - mi * 50;
    wsrc = mi == 0 ? wo : (mi == 1 ? w1 : w2); wdst = ws + WS_WO + (size_t)mi * (D_ * D_ * 2);
    kt = u / 5; nt = u - kt * 5;
  } else if (b < 270) {
    const int u = b - 150; wsrc = w3; wdst = ws + WS_W3; ldw = F_ * 2; kt = u / 12; nt = u - kt * 12;
  } else {
    const int u = b - 270; wsrc = w4; wdst = ws + WS_W4; wks = 48; kt = u / 5; nt = u - kt * 5;
  }
  WJob j;
  j.src = wsrc + (size_t)(kt * 64) * ldw + nt * 256;
  j.dst = live ? wdst + (size_t)(nt * 8 * wks + kt * 2) * 1024 : nullptr;
  j.ldw = ldw; j.wks = wks;
  return j;
}

template <bool SWAP>
struct GemmQ {
  f32x4 acc[4][4];
  STDECL;
  static constexpr int NG = 4, D = 3, NGRP = 10, SB = 32768, XCH = NG * SB;
  DEV bool run(char* smem, const char* Ab, const char* Wb, int k0, const WJob& job) {
    STR(0); ST(1);
    constexpr int LDA = D_ * 2, LDW = 3 * D_ * 2;
    const int tid = threadIdx.x, l = tid & 63;
    const int wave = __builtin_amdgcn_readfirstlane(tid >> 6);
    if (wave >= 4) {
      const bool isw = wave == 5;
      unsigned vo[2];
#pragma unroll
      for (int q = 0; q < 2; ++q)
        vo[q] = isw ? wrm_voff(l, q, LDW) : (unsigned)((l >> 3) * LDA + (((l & 7) ^ (q * 4 + (l >> 4))) * 16));
      const unsigned vx[4] = {vo[0], isw ? vo[0] : vo[1], isw ? vo[1] : vo[0], vo[1]};
      const unsigned dst0 = lds_addr(smem) + (isw ? 16384 : 0);
      const char* src0 = isw ? Wb : Ab;
      const int gstr = isw ? 64 * LDW : 128, istr = isw ? 4 * LDW : 8 * LDA;
      auto issue = [&](int grp) {
        int kg = grp + k0;
        kg = kg >= NGRP ? kg - NGRP : kg;
        const char* sp = src0 + (size_t)kg * gstr;
#pragma unroll
        for (int i = 0; i < 16; ++i) glds16(sp + (size_t)i * istr, vx[i & 3], dst0 + (grp % NG) * SB + i * 1024);
      };
      static_for<0, D>([&](auto gi) { issue(gi.value); });
      static_for<0, NGRP>([&](auto gi) {
        constexpr int grp = gi.value;
        constexpr int issued = grp + D < NGRP ? grp + D : NGRP;
        wait_vm<16 * (issued - grp - 1) + (grp > NGRP - D ? 16 : 0)>();
        __builtin_amdgcn_s_barrier();
        cfence();
        if constexpr (grp + D < NGRP) issue(grp + D);
        if constexpr (grp == NGRP - D) {
#pragma unroll
          for (int i = 0; i < 16; ++i)
            glds16(job.src + (size_t)i * 4 * job.ldw, wrm_voff(l, (i >> 1) & 1, job.ldw), dst0 + ((grp - 1) % NG) * SB + i * 1024);
        }
      });
      __builtin_amdgcn_s_barrier();
      wait_vm<0>();
      if (job.dst) {
        const char* tb = smem + ((NGRP - D - 1) % NG) * SB + (isw ? 16384 : 0);
#pragma unroll
        for (int kk = 0; kk < 2; ++kk)
#pragma unroll
          for (int j = 0; j < 8; ++j) {
            const bf16x8 f = wrm_frag(tb + kk * 8192 + wrm_lane(l, j >> 2, j & 3));
            *(bf16x8*)(job.dst + (size_t)(j * job.wks + kk) * 1024 + l * 16) = f;
          }
      }
      return false;
    }
    const int wm = wave >> 1, wn = wave & 1, g = l >> 4;
    float rs[2] = {0.f, 0.f}, rq[2] = {0.f, 0.f}, cd[2] = {0.f, 0.f};
#pragma unroll
    for (int i = 0; i < 4; ++i)
#pragma unroll
      for (int j = 0; j < 4; ++j) acc[i][j] = f32x4{0.f, 0.f, 0.f, 0.f};
    const unsigned a0 = (unsigned)((wm * 64 + (l & 15)) * 128 + ((g ^ ((l & 15) >> 1)) * 16));
    const char* al[2][2] = {{smem + a0 + wn * 4096, smem + a0 + 4096 - wn * 4096},
                            {smem + (a0 ^ 64u) + wn * 4096, smem + (a0 ^ 64u) + 4096 - wn * 4096}};
    const char* wl[4];
#pragma unroll
    for (int j = 0; j < 4; ++j) wl[j] = smem + 16384 + wrm_lane(l, wn, j ^ (2 * wm));
    bf16x8 af[2][4], wf[2][4];
    auto ldfrag = [&](auto ti) {
      constexpr int t = ti.value, kk = t & 1, so = ((t >> 1) % NG) * SB;
#pragma unroll
      for (int i = 0; i < 4; ++i) af[kk][i] = *(const bf16x8*)(al[kk][i >> 1] + so + (i & 1) * 2048);
#pragma unroll
      for (int j = 0; j < 4; ++j) wf[kk][j] = wrm_frag(wl[j] + so + kk * 8192);
    };
    __builtin_amdgcn_s_barrier();
    cfence();
    ST(3);
    ldfrag(std::integral_constant<int, 0>{});
    static_for<0, 2 * NGRP>([&](auto ti) {
      constexpr int t = ti.value, kk = t & 1;
      if constexpr (t + 1 < 2 * NGRP) {
        if constexpr (kk == 1) {
          wait_lgkm0();
          __builtin_amdgcn_s_barrier();
          cfence();
          ST(3 + (t + 1) / 2);
        }
        ldfrag(std::integral_constant<int, t + 1>{});
      }
      __builtin_amdgcn_sched_barrier(0);
#pragma unroll
      for (int i = 0; i < 4; ++i)
#pragma unroll
        for (int j = 0; j < 4; ++j)
          acc[i][j] = SWAP ? mfma16(wf[kk][j], af[kk][i], acc[i][j]) : mfma16(af[kk][i], wf[kk][j], acc[i][j]);
#pragma unroll
      for (int i = 0; i < 2; ++i) { dot_sum(rs[i], af[kk][i]); dot_sq(rq[i], af[kk][i]); dot_sum(cd[i], wf[kk][i]); }
#pragma unroll
      for (int q = 0; q < 8; ++q) {
        __builtin_amdgcn_sched_group_barrier(0x8, 2, 0);
        __builtin_amdgcn_sched_group_barrier(0x2, 3, 0);
      }
      asm volatile("" : "+v"(rs[0]), "+v"(rs[1]), "+v"(rq[0]), "+v"(rq[1]), "+v"(cd[0]), "+v"(cd[1]));
      __builtin_amdgcn_sched_barrier(0);
    });
    ST(21);
    float2* xs = (float2*)(smem + XCH);
    float* xc = (float*)(smem + XCH + 1024);
#pragma unroll
    for (int i = 0; i < 2; ++i) {
      const float a = red_g(rs[i]), b = red_g(rq[i]), c = red_g(cd[i]);
      float mean, var, m, sd;
      ln_stats(a, b, mean, var, m, sd);
      const float rinv = 1.0f / sd;
      if (l < 16) { xs[(wm * 4 + wn * 2 + i) * 16 + l] = float2{rinv, -m * rinv}; xc[(wn * 4 + wm * 2 + i) * 16 + l] = c; }
    }
    wait_lgkm0();
    __builtin_amdgcn_s_barrier();
    cfence();
    return true;
  }
};

template <bool ISV>
DEV void qkv_body(char* smem, const char* x, const char* wqkv, char* ws, int rt, int ct, int k0, const WJob& job) {
  using GQ = GemmQ<!ISV>;
  GQ gm;
  const int row0 = rt * 128, n0 = ct * 128;
  if (!gm.run(smem, x + (size_t)row0 * (D_ * 2), wqkv + n0 * 2, k0, job)) return;
  const int tid = threadIdx.x, l = tid & 63, wave = tid >> 6, wm = wave >> 1, wn = wave & 1, g = l >> 4;
  const float2* xs = (const float2*)(smem + GQ::XCH);
  const float* xc = (const float*)(smem + GQ::XCH + 1024);
  if constexpr (!ISV) {
    f32x4 cs[4];
    unsigned po[4];
#pragma unroll
    for (int j = 0; j < 4; ++j) {
      const int ja = wn * 4 + (j ^ (2 * wm));
      cs[j] = *(const f32x4*)(xc + ja * 16 + 4 * g);
      const int c = (n0 + ja * 16 + 4 * g) % D_, head = c / 20, d0 = c - head * 20;
      po[j] = (unsigned)(head * (int)QK_HEAD + (d0 >> 3) * 256 + (d0 & 7) * 2) | (d0 == 16 ? 1u : 0u);
    }
    char* dstb = ws + (ct < 5 ? WS_Q : WS_K) + (size_t)(row0 >> 4) * 768 + (l & 15) * 16;
#pragma unroll
    for (int i = 0; i < 4; ++i) {
      const int ia = wm * 4 + (i ^ (2 * wn));
      const float2 st = xs[ia * 16 + (l & 15)];
#pragma unroll
      for (int j = 0; j < 4; ++j) {
        const f32x4 y = gm.acc[i][j] * st.x + cs[j] * st.y;
        char* p = dstb + ia * 768 + (po[j] & ~1u);
        if (po[j] & 1u) *(u32x4*)p = u32x4{pk2(y[0], y[1]), pk2(y[2], y[3]), 0u, 0u};
        else *(u32x2*)p = u32x2{pk2(y[0], y[1]), pk2(y[2], y[3])};
      }
    }
  } else {
    float cd[4];
    unsigned po[4];
#pragma unroll
    for (int j = 0; j < 4; ++j) {
      const int ja = wn * 4 + (j ^ (2 * wm));
      cd[j] = xc[ja * 16 + (l & 15)];
      const int c = n0 - 2 * D_ + ja * 16 + (l & 15), head = c / 20, d = c - head * 20;
      po[j] = (unsigned)(head * (int)V_HEAD + (d < 16 ? (g * 16 + d) * 16 : 1024 + (g * 4 + d - 16) * 16));
    }
    char* dstb = ws + WS_V + (size_t)(row0 >> 5) * 1280;
#pragma unroll
    for (int i = 0; i < 4; ++i) {
      const int ia = wm * 4 + (i ^ (2 * wn));
      const f32x4 s01 = *(const f32x4*)(xs + ia * 16 + 4 * g), s23 = *(const f32x4*)(xs + ia * 16 + 4 * g + 2);
      const f32x4 rr = {s01[0], s01[2], s23[0], s23[2]}, nm = {s01[1], s01[3], s23[1], s23[3]};
#pragma unroll
      for (int j = 0; j < 4; ++j) {
        const f32x4 y = gm.acc[i][j] * rr + nm * cd[j];
        *(u32x2*)(dstb + (ia >> 1) * 1280 + (i & 1) * 8 + po[j]) = u32x2{pk2(y[0], y[1]), pk2(y[2], y[3])};
      }
    }
  }
  STDUMP(gm, ws, 1);
}

DEV void colsum_job(char* smem, const char* w1, const char* w3, float* cs, int blk) {
  const int tid = threadIdx.x, c = tid % 17, rg = tid / 17;
  const int gc = blk * 17 + c;
  const char* base = gc < 80 ? w1 + gc * 16 : w3 + (gc - 80) * 16;
  const int ld = gc < 80 ? D_ * 2 : F_ * 2;
  float a[8] = {0.f, 0.f, 0.f, 0.f, 0.f, 0.f, 0.f, 0.f};
  if (rg < 20) {
#pragma unroll 16
    for (int r = rg; r < D_; r += 20) {
      u32x4 v = *(const u32x4*)(base + (size_t)r * ld);
#pragma unroll
      for (int e = 0; e < 4; ++e) { a[2 * e] += bf_lo(v[e]); a[2 * e + 1] += bf_hi(v[e]); }
    }
  }
  float* sm = (float*)smem;
  if (rg < 20) {
#pragma unroll
    for (int e = 0; e < 8; ++e) sm[rg * 136 + c * 8 + e] = a[e];
  }
  __syncthreads();
  if (tid < 136) {
    float t = 0.f;
#pragma unroll
    for (int r = 0; r < 20; ++r) t += sm[r * 136 + tid];
    cs[blk * 136 + tid] = t;
  }
}

__global__ __launch_bounds__(384) void k1_qkv(const char* x, const char* wqkv, const char* wo, const char* w1, const char* w2,
                                              const char* w3, const char* w4, char* ws) {
  extern __shared__ __attribute__((aligned(16))) char smem[];
  const int b = blockIdx.x, xcd = b & 7, j = b >> 3, r = j & 3, c = j >> 2;
  if (threadIdx.x < 16) *(u32x4*)(ws + WS_FL + b * 256 + threadIdx.x * 16) = u32x4{0u, 0u, 0u, 0u};
  if ((xcd & 1) && c == 7) { colsum_job(smem, w1, w3, (float*)(ws + WS_CS), (xcd >> 1) * 4 + r); return; }
  const int rt = (xcd >> 1) * 4 + r, ct = (xcd & 1) * 8 + c;
  const int wave = __builtin_amdgcn_readfirstlane(threadIdx.x >> 6);
  const WJob job = make_job((rt * 15 + ct) * 2 + (wave & 1), wo, w1, w2, w3, w4, ws);
  const int k0 = (3 * r + 4 * c) % 10;
  if (ct >= 10) qkv_body<true>(smem, x, wqkv, ws, rt, ct, k0, job);
  else qkv_body<false>(smem, x, wqkv, ws, rt, ct, k0, job);
}

typedef __attribute__((ext_vector_type(2))) float f32x2;
DEV f32x2 unpk(unsigned u) { return f32x2{bf_lo(u), bf_hi(u)}; }
DEV unsigned pk2v(f32x2 v) { return pk2(v[0], v[1]); }
DEV f32x2 bfr2(f32x2 v) { return unpk(pk2v(v)); }
DEV f32x2 gelu2(f32x2 x) {
  const float c0 = -2.0f * 0.7978845608028654f * 1.4426950408889634f, c1 = c0 * 0.044715f;
  const f32x2 z = x * (x * x * c1 + c0);
  const f32x2 d = f32x2{__builtin_amdgcn_exp2f(z[0]), __builtin_amdgcn_exp2f(z[1])} + 1.0f;
  return x * f32x2{__builtin_amdgcn_rcpf(d[0]), __builtin_amdgcn_rcpf(d[1])};
}
DEV f32x2 lo2(f32x4 v) { return f32x2{v[0], v[1]}; }
DEV f32x2 hi2(f32x4 v) { return f32x2{v[2], v[3]}; }

typedef __amdgpu_buffer_rsrc_t rsrc_t;
DEV void st8(rsrc_t r, unsigned v, unsigned s, u32x2 d) { __builtin_amdgcn_raw_buffer_store_b64(d, r, v, s, 16); }
DEV u32x4 ld16(rsrc_t r, unsigned v, unsigned s) { return __builtin_amdgcn_raw_buffer_load_b128(r, v, s, 16); }
DEV void signal(rsrc_t r, unsigned s) {
  asm volatile("s_waitcnt vmcnt(0)" ::: "memory");
  if ((threadIdx.x & 63) == 0) __builtin_amdgcn_raw_buffer_store_b32(1u, r, 0u, s, 16);
  cfence();
}
DEV void wait_flags(const char* fl, unsigned v) {
  for (int it = 0; it < (1 << 21); ++it) {
    unsigned f;
    asm volatile("global_load_dword %0, %1, %2 sc1\n\ts_waitcnt vmcnt(0)" : "=v"(f) : "v"(v), "s"(fl) : "memory");
    if (__builtin_amdgcn_ballot_w64(f == 0u) == 0ull) break;
  }
}

constexpr int LDS_TOT = 163840, NGR = 64;
constexpr int AT_Q = 104 * 1024, AT_K = AT_Q + 16 * 768, AT_V = AT_K + 32 * 768;
constexpr int g_nt(int i) { return (i >= 30 && i < 40) ? 6 : 5; }
struct Sched { int off[NGR]; int at[NGR]; int outst[NGR]; };
constexpr Sched make_sched() {
  Sched s{};
  int pos = 0;
  for (int i = 0; i < NGR; ++i) {
    const int sz = 2048 * g_nt(i);
    if (pos + sz > LDS_TOT) pos = 0;
    s.off[i] = pos; pos += sz;
    int at = i < 10 ? -2 : -1;
    for (int k = 0; k < i; ++k)
      if (s.off[k] < s.off[i] + sz && s.off[i] < s.off[k] + 2048 * g_nt(k) && k + 1 > at) at = k + 1;
    if (i > 0 && s.at[i - 1] > at) at = s.at[i - 1];
    for (;;) {
      int n = 0;
      for (int k = (at + 1 > 10 ? at + 1 : 10); k <= i; ++k) n += g_nt(k);
      if (n <= 63) break;
      ++at;
    }
    s.at[i] = at;
  }
  for (int b = 0; b < NGR; ++b) {
    int n = 0;
    for (int k = (b + 1 > 10 ? b + 1 : 10); k < NGR; ++k) if (s.at[k] < b) n += g_nt(k);
    s.outst[b] = n;
  }
  return s;
}
constexpr Sched SCH = make_sched();
constexpr bool sched_ok() { for (int i = 10; i < NGR; ++i) if (SCH.at[i] > i - 3) return false; return SCH.off[9] + 10240 <= AT_Q; }
static_assert(sched_ok(), "weight stream schedule");

DEV void loader(char* smem, const char* ws, int pw, int ct8, int ct16) {
  const int l = threadIdx.x & 63;
  const unsigned lb = lds_addr(smem);
  const char* wsrc[5] = {ws + WS_WO + (size_t)ct8 * (5 * 20 * 1024), ws + WS_W1 + (size_t)ct8 * (5 * 20 * 1024), ws + WS_W2 + (size_t)ct8 * (5 * 20 * 1024),
                         ws + WS_W3 + (size_t)ct16 * (6 * 20 * 1024), ws + WS_W4 + (size_t)ct8 * (5 * 48 * 1024)};
  const unsigned vl = l * 16 + pw * 1024;
  auto issue_at = [&](auto bi) {
    static_for<10, NGR>([&](auto gi) {
      constexpr int i = gi.value;
      if constexpr (SCH.at[i] == bi.value) {
        constexpr int ph = i < 40 ? i / 10 : 4, g = i < 40 ? i % 10 : i - 40, NT = g_nt(i), KS = ph == 4 ? 48 : 20;
#pragma unroll
        for (int j = 0; j < NT; ++j) glds16(wsrc[ph] + j * (KS * 1024), vl + g * 2048, lb + SCH.off[i] + pw * (NT * 1024) + j * 1024);
      }
    });
  };
  issue_at(std::integral_constant<int, -1>{});
  static_for<0, NGR>([&](auto bi) {
    wait_vm<SCH.outst[bi.value]>();
    __builtin_amdgcn_s_barrier();
    cfence();
    issue_at(bi);
  });
}

template <int G0, int TM, int KS, int WIN>
DEV void gemm(f32x4 (&acc)[TM][g_nt(G0)], const char* smem, rsrc_t ws, unsigned a0 STARG) {
  constexpr int NT = g_nt(G0);
  const unsigned vl = (threadIdx.x & 63) * 16;
  unsigned wb[3] = {lds_addr(smem) + vl, lds_addr(smem) + vl + 65536, lds_addr(smem) + vl + 131072};
  asm volatile("" : "+v"(wb[0]), "+v"(wb[1]), "+v"(wb[2]));
  auto ldw = [&](int o) { return *(const LAS bf16x8*)(uintptr_t)(wb[o >> 16] + (o & 65535)); };
  bf16x8 af[WIN][TM], wf[2][NT];
  auto lda = [&](int u) { return __builtin_bit_cast(bf16x8, ld16(ws, vl + (u & 3) * 1024, a0 + (u >> 2) * 4096)); };
#pragma unroll
  for (int t = 0; t < WIN; ++t)
#pragma unroll
    for (int i = 0; i < TM; ++i) af[t][i] = lda(i * KS + t);
#pragma unroll
  for (int i = 0; i < TM; ++i)
#pragma unroll
    for (int j = 0; j < NT; ++j) acc[i][j] = f32x4{0.f, 0.f, 0.f, 0.f};
  wait_lgkm0();
  __builtin_amdgcn_s_barrier();
  cfence();
#pragma unroll
  for (int j = 0; j < NT; ++j) wf[0][j] = ldw(SCH.off[G0] + j * 1024);
  static_for<0, KS>([&](auto ti) {
    constexpr int t = ti.value;
    if constexpr (t + 1 < KS) {
      if constexpr ((t + 1) % 2 == 0) {
        wait_lgkm0();
        __builtin_amdgcn_s_barrier();
        cfence();
      }
      constexpr int so = SCH.off[G0 + (t + 1) / 2] + ((t + 1) & 1) * NT * 1024;
#pragma unroll
      for (int j = 0; j < NT; ++j) wf[(t + 1) & 1][j] = ldw(so + j * 1024);
    }
    __builtin_amdgcn_sched_barrier(0);
#pragma unroll
    for (int i = 0; i < TM; ++i)
#pragma unroll
      for (int j = 0; j < NT; ++j) acc[i][j] = mfma16(wf[t & 1][j], af[t % WIN][i], acc[i][j]);
    if constexpr (t + WIN < KS) {
#pragma unroll
      for (int i = 0; i < TM; ++i) af[t % WIN][i] = lda(i * KS + t + WIN);
    }
    __builtin_amdgcn_sched_barrier(0);
    if constexpr (t == 0) { ST(STI + 1); }
  });
}

DEV void put_stats(rsrc_t ws, unsigned stb, int li, int g, f32x2 s, f32x2 q) {
  const float a = red_g(s[0] + s[1]), b = red_g(q[0] + q[1]);
  if (g == 0) st8(ws, li * 64, stb, u32x2{__builtin_bit_cast(unsigned, a), __builtin_bit_cast(unsigned, b)});
}
DEV void acc_stats(f32x2& s, f32x2& q, unsigned u) { const f32x2 v = unpk(u); s += v; q += v * v; }

DEV void attention(char* smem, rsrc_t ws, int xcd, int head, int w) {
  const int l = threadIdx.x & 63, g = l >> 4, li = l & 15;
  bf16x8 qf[2];
#pragma unroll
  for (int i = 0; i < 2; ++i) {
    u32x4 v = *(const u32x4*)(smem + AT_Q + (2 * w + i) * 768 + (g < 3 ? g : 2) * 256 + li * 16);
    if (g == 3) v = u32x4{0u, 0u, 0u, 0u};
    qf[i] = __builtin_bit_cast(bf16x8, v);
  }
  const char* Ks = smem + AT_K + 2 * w * 768 + (g < 3 ? g : 2) * 256 + li * 16;
  const int kbase = xcd * 256 + 32 * w - 256;
  f32x4 s0[17], s1[17];
  const f32x4 z4 = {0.f, 0.f, 0.f, 0.f};
#pragma unroll
  for (int c = 0; c < 18; ++c) {
    bf16x8 kf = *(const bf16x8*)(Ks + c * 768);
    if (c < 17) s0[c] = mfma16(kf, qf[0], z4);
    if (c > 0) s1[c - 1] = mfma16(kf, qf[1], z4);
  }
  const float NEG = -__builtin_inff();
#pragma unroll
  for (int r = 0; r < 4; ++r) {
    const int kj = 4 * g + r;
    if (kj <= li) { s0[0][r] = NEG; s1[0][r] = NEG; }
    if (kj > li) { s0[16][r] = NEG; s1[16][r] = NEG; }
  }
  if (kbase < 0) {
#pragma unroll
    for (int c = 0; c < 17; ++c) {
      if (kbase + 16 * c < 0) s0[c] = f32x4{NEG, NEG, NEG, NEG};
      if (kbase + 16 * (c + 1) < 0) s1[c] = f32x4{NEG, NEG, NEG, NEG};
    }
  }
  float m0 = NEG, m1 = NEG;
#pragma unroll
  for (int c = 0; c < 17; ++c)
#pragma unroll
    for (int r = 0; r < 4; r += 2) {
      m0 = fmaxf(fmaxf(m0, s0[c][r]), s0[c][r + 1]);
      m1 = fmaxf(fmaxf(m1, s1[c][r]), s1[c][r + 1]);
    }
  m0 = fmaxf(m0, __shfl_xor(m0, 16)); m0 = fmaxf(m0, __shfl_xor(m0, 32));
  m1 = fmaxf(m1, __shfl_xor(m1, 16)); m1 = fmaxf(m1, __shfl_xor(m1, 32));
  const float C2 = 0.22360679774997896f * 1.4426950408889634f;
  const float b0 = -m0 * C2, b1 = -m1 * C2;
  u32x2 p0[18], p1[18];
#pragma unroll
  for (int c = 0; c < 17; ++c) {
    const f32x4 e = s0[c] * C2 + b0, f = s1[c] * C2 + b1;
    p0[c] = u32x2{pk2(__builtin_amdgcn_exp2f(e[0]), __builtin_amdgcn_exp2f(e[1])), pk2(__builtin_amdgcn_exp2f(e[2]), __builtin_amdgcn_exp2f(e[3]))};
    p1[c + 1] = u32x2{pk2(__builtin_amdgcn_exp2f(f[0]), __builtin_amdgcn_exp2f(f[1])), pk2(__builtin_amdgcn_exp2f(f[2]), __builtin_amdgcn_exp2f(f[3]))};
  }
  p0[17] = u32x2{0u, 0u}; p1[0] = u32x2{0u, 0u};
  const char* Vs0 = smem + AT_V + w * 1280 + (g * 16 + li) * 16;
  const char* Vs1 = smem + AT_V + w * 1280 + 1024 + (g * 4 + (li & 3)) * 16;
  f32x4 o00 = z4, o01 = z4, o10 = z4, o11 = z4;
#pragma unroll
  for (int s = 0; s < 9; ++s) {
    bf16x8 v0 = *(const bf16x8*)(Vs0 + s * 1280);
    u32x4 v1u = *(const u32x4*)(Vs1 + s * 1280);
    if (li == 4) v1u = u32x4{0x3F803F80u, 0x3F803F80u, 0x3F803F80u, 0x3F803F80u};
    bf16x8 v1 = __builtin_bit_cast(bf16x8, v1u);
    u32x4 pa = {p0[2 * s][0], p0[2 * s][1], p0[2 * s + 1][0], p0[2 * s + 1][1]};
    u32x4 pb = {p1[2 * s][0], p1[2 * s][1], p1[2 * s + 1][0], p1[2 * s + 1][1]};
    bf16x8 pfa = __builtin_bit_cast(bf16x8, pa), pfb = __builtin_bit_cast(bf16x8, pb);
    o00 = mfma16(v0, pfa, o00); o01 = mfma16(v1, pfa, o01);
    o10 = mfma16(v0, pfb, o10); o11 = mfma16(v1, pfb, o11);
  }
  const float r0 = 1.0f / __shfl(o01[0], 16 + li), r1 = 1.0f / __shfl(o11[0], 16 + li);
  const unsigned Of = (unsigned)WS_O + (xcd * 16 + 2 * w) * (20 * 1024);
  const unsigned c0 = ft_off(li, head * 20 + 4 * g, 20), c1 = ft_off(li, head * 20 + 16, 20);
  st8(ws, c0, Of, u32x2{pk2(o00[0] * r0, o00[1] * r0), pk2(o00[2] * r0, o00[3] * r0)});
  st8(ws, c0, Of + 20 * 1024, u32x2{pk2(o10[0] * r1, o10[1] * r1), pk2(o10[2] * r1, o10[3] * r1)});
  if (g == 0) {
    st8(ws, c1, Of, u32x2{pk2(o01[0] * r0, o01[1] * r0), pk2(o01[2] * r0, o01[3] * r0)});
    st8(ws, c1, Of + 20 * 1024, u32x2{pk2(o11[0] * r1, o11[1] * r1), pk2(o11[2] * r1, o11[3] * r1)});
  }
}

__global__ __launch_bounds__(512) void kp_main(const char* x, char* ws, char* out) {
  extern __shared__ __attribute__((aligned(16))) char smem[];
  STDECL;
  STR(0); ST(1);
  const int b = blockIdx.x, xcd = b & 7, jj = b >> 3;
  const int tid = threadIdx.x, l = tid & 63, g = l >> 4, li = l & 15;
  const int w = __builtin_amdgcn_readfirstlane(tid >> 6);
  const int ct = jj & 7, ct16 = jj & 15;
  {
    const char* Qh = ws + WS_Q + (size_t)jj * QK_HEAD + xcd * 16 * 768;
    const char* Kh = ws + WS_K + (size_t)jj * QK_HEAD;
    const char* Vh = ws + WS_V + (size_t)jj * V_HEAD;
    const unsigned lb = lds_addr(smem);
    const int koff0 = (xcd * 16 - 16) * 768, voff0 = (xcd * 8 - 8) * 1280;
#pragma unroll
    for (int i = 0; i < 7; ++i) {
      const int p = w + 8 * i;
      const char* src;
      if (p < 12) src = Qh + p * 1024;
      else if (p < 36) { const int o = koff0 + (p - 12) * 1024; src = Kh + (o < 0 ? 0 : o); }
      else { const int o = voff0 + (p - 36) * 1024; src = Vh + (o < 0 ? 0 : o); }
      glds16(src, l * 16, lb + AT_Q + p * 1024);
    }
    const char* Wo = ws + WS_WO + (size_t)ct * (5 * 20 * 1024);
#pragma unroll
    for (int i = 0; i < 13; ++i) {
      int u = w + 8 * i;
      u = u < 100 ? u : 99;
      const int t = u / 5, j = u - t * 5;
      glds16(Wo + (j * 20 + t) * 1024, l * 16, lb + u * 1024);
    }
    wait_vm<13>();
    __builtin_amdgcn_s_barrier();
    cfence();
  }
  ST(2);
  const rsrc_t wr = __builtin_amdgcn_make_buffer_rsrc(ws, 0, (int)WS_END, 0x00020000);
  attention(smem, wr, xcd, jj, w);
  ST(3);
  signal(wr, (unsigned)FL_A + (xcd * 8 + w) * 128 + jj * 4);
  ST(21);
  wait_lgkm0();
  __builtin_amdgcn_s_barrier();
  cfence();
  if (w >= 6) return;
  if (w >= 4) { loader(smem, ws, w - 4, ct, ct16); return; }

  const int rt = (xcd * 4 + (jj >> 3)) * 4 + w;
  const int n0 = ct * 80;
  const unsigned ve = (g >> 1) * 256 + (g & 1) * 8;
  const unsigned vs = ve + li * 16;
  const unsigned tb = rt * (20 * 1024), tc = tb + ct * (5 * 512);
  const size_t xo = (size_t)(rt * 16 + li) * (D_ * 2) + (n0 + 4 * g) * 2;
  u32x2 hr[5], xr[5];
  float m1, r1;
  {
#pragma unroll
    for (int j = 0; j < 5; ++j) xr[j] = *(const u32x2*)(x + xo + j * 32);
    wait_flags(ws + FL_A + (rt >> 1) * 128, (l & 31) * 4);
    ST(4);
    f32x4 acc[1][5];
    gemm<0, 1, 20, 20>(acc, smem, wr, (unsigned)WS_O + tb STPASS(4));
    ST(6);
    f32x2 ss = {0.f, 0.f}, sq = {0.f, 0.f};
#pragma unroll
    for (int j = 0; j < 5; ++j) {
      const f32x4 a = acc[0][j];
      hr[j] = u32x2{pk2v(unpk(xr[j][0]) + bfr2(lo2(a))), pk2v(unpk(xr[j][1]) + bfr2(hi2(a)))};
      st8(wr, vs + j * 512, (unsigned)WS_H + tc, hr[j]);
      acc_stats(ss, sq, hr[j][0]); acc_stats(ss, sq, hr[j][1]);
    }
    put_stats(wr, (unsigned)WS_ST + rt * 1024 + ct * 8, li, g, ss, sq);
    signal(wr, (unsigned)FL_3 + rt * 128 + ct * 4);
  }
  {
    f32x4 cs[5];
#pragma unroll
    for (int j = 0; j < 5; ++j) cs[j] = *(const f32x4*)(ws + WS_CS + (n0 + j * 16 + 4 * g) * 4);
    wait_flags(ws + FL_3 + rt * 128, (l & 7) * 4);
    ST(7);
    const f32x4 ps = __builtin_bit_cast(f32x4, ld16(wr, li * 64 + g * 16, (unsigned)WS_ST + rt * 1024));
    f32x4 acc[1][5];
    gemm<10, 1, 20, 20>(acc, smem, wr, (unsigned)WS_H + tb STPASS(7));
    ST(9);
    float mean, var, s1;
    ln_stats(red_g(ps[0] + ps[2]), red_g(ps[1] + ps[3]), mean, var, m1, s1);
    r1 = 1.0f / s1;
    const float m2 = bfr((mean - m1) * r1), v2 = bfr(var * r1 * r1);
    const float s2 = bfr(sqrtf(bfr(v2 + 1e-5f)));
    const float rr = r1 / s2, nm = -(m1 + m2 * s1) * rr;
    const int grp = ct >> 1;
    const int sh = (grp & 2) ? ((grp & 1) ? -2 : 2) : ((grp & 1) ? -1 : 1);
    const int row = rt * 16 + li, drow = row + sh, dl = li + sh + 16;
    const unsigned vd = ve + (dl & 15) * 16 + (dl >> 4) * (20 * 1024);
    const bool zfill = sh > 0 ? (row < sh) : (row >= S_ + sh);
#pragma unroll
    for (int j = 0; j < 5; ++j) {
      const f32x4 y = acc[0][j] * rr + cs[j] * nm;
      const u32x2 v = {pk2v(gelu2(bfr2(lo2(y)))), pk2v(gelu2(bfr2(hi2(y))))};
      if (drow >= 0 && drow < S_) st8(wr, vd + j * 512, (unsigned)WS_G - 20 * 1024 + tc, v);
      if (zfill) st8(wr, vs + j * 512, (unsigned)WS_G + tc, u32x2{0u, 0u});
    }
    ST(19);
    signal(wr, (unsigned)FL_4 + rt * 128 + ct * 4);
    ST(20);
  }
  {
    int nb = rt - 1 + ((l >> 3) & 3);
    nb = nb < 0 ? 0 : (nb > rt + 1 ? rt : (nb > 127 ? 127 : nb));
    wait_flags(ws + FL_4, nb * 128 + (l & 7) * 4);
    ST(10);
    f32x4 acc[1][5];
    gemm<20, 1, 20, 20>(acc, smem, wr, (unsigned)WS_G + tb STPASS(10));
    ST(12);
    f32x2 ss = {0.f, 0.f}, sq = {0.f, 0.f};
#pragma unroll
    for (int j = 0; j < 5; ++j) {
      const f32x4 a = acc[0][j];
      const f32x2 y0 = bfr2(bfr2(unpk(hr[j][0]) - m1) * r1) + bfr2(lo2(a));
      const f32x2 y1 = bfr2(bfr2(unpk(hr[j][1]) - m1) * r1) + bfr2(hi2(a));
      xr[j] = u32x2{pk2v(y0), pk2v(y1)};
      st8(wr, vs + j * 512, (unsigned)WS_X1 + tc, xr[j]);
      acc_stats(ss, sq, xr[j][0]); acc_stats(ss, sq, xr[j][1]);
    }
    put_stats(wr, (unsigned)WS_ST2 + rt * 1024 + ct * 8, li, g, ss, sq);
    signal(wr, (unsigned)FL_5 + rt * 128 + ct * 4);
  }
  {
    const int rb = (xcd * 2 + (jj >> 4)) * 4 + w;
    const int n6 = ct16 * 96;
    wait_flags(ws + FL_5 + rb * 256, ((l >> 3) & 1) * 128 + (l & 7) * 4);
    ST(13);
    f32x4 ps[2];
#pragma unroll
    for (int i = 0; i < 2; ++i) ps[i] = __builtin_bit_cast(f32x4, ld16(wr, li * 64 + g * 16 + i * 1024, (unsigned)WS_ST2 + rb * 2048));
    f32x4 acc[2][6];
    gemm<30, 2, 20, 10>(acc, smem, wr, (unsigned)WS_X1 + rb * (2 * 20 * 1024) STPASS(13));
    ST(15);
    f32x4 cs[6];
#pragma unroll
    for (int j = 0; j < 6; ++j) cs[j] = *(const f32x4*)(ws + WS_CS + (D_ + n6 + j * 16 + 4 * g) * 4);
#pragma unroll
    for (int i = 0; i < 2; ++i) {
      float mean, var, m, s;
      ln_stats(red_g(ps[i][0] + ps[i][2]), red_g(ps[i][1] + ps[i][3]), mean, var, m, s);
      const float rinv = 1.0f / s, nm = -m * rinv;
#pragma unroll
      for (int j = 0; j < 6; ++j) {
        const f32x4 y = acc[i][j] * rinv + cs[j] * nm;
        st8(wr, vs + j * 512, (unsigned)WS_U + (rb * 2 + i) * (48 * 1024) + ct16 * (6 * 512),
            u32x2{pk2v(gelu2(bfr2(lo2(y)))), pk2v(gelu2(bfr2(hi2(y))))});
      }
    }
    signal(wr, (unsigned)FL_6 + rb * 128 + ct16 * 4);
  }
  {
    wait_flags(ws + FL_6 + (rt >> 1) * 128, (l & 15) * 4);
    ST(16);
    f32x4 acc[1][5];
    gemm<40, 1, 48, 24>(acc, smem, wr, (unsigned)WS_U + rt * (48 * 1024) STPASS(16));
    ST(18);
#pragma unroll
    for (int j = 0; j < 5; ++j) {
      const f32x4 a = acc[0][j];
      const f32x2 y0 = unpk(hr[j][0]) + bfr2(unpk(xr[j][0]) + bfr2(lo2(a)));
      const f32x2 y1 = unpk(hr[j][1]) + bfr2(unpk(xr[j][1]) + bfr2(hi2(a)));
      *(u32x2*)(out + xo + j * 32) = u32x2{pk2v(y0), pk2v(y1)};
    }
  }
  STDUMP2(ws, 2);
}

extern "C" void kernel_launch(void* const* d_in, const int* in_sizes, int n_in, void* d_out, int out_size,
                              void* d_ws, size_t ws_size, hipStream_t stream) {
  const char* x = (const char*)d_in[0];
  const char* wqkv = (const char*)d_in[1];
  const char* wo = (const char*)d_in[2];
  const char* w1 = (const char*)d_in[4];
  const char* w2 = (const char*)d_in[5];
  const char* w3 = (const char*)d_in[6];
  const char* w4 = (const char*)d_in[7];
  char* ws = (char*)d_ws;
  if (ws_size < WS_END) return;
  constexpr int LQ = 4 * 32768 + 2048;
  (void)hipFuncSetAttribute((const void*)k1_qkv, hipFuncAttributeMaxDynamicSharedMemorySize, LQ);
  (void)hipFuncSetAttribute((const void*)kp_main, hipFuncAttributeMaxDynamicSharedMemorySize, LDS_TOT);
  k1_qkv<<<256, 384, LQ, stream>>>(x, wqkv, wo, w1, w2, w3, w4, ws);
  kp_main<<<256, 512, LDS_TOT, stream>>>(x, ws, (char*)d_out);
}
```

```cpp
#include <hip/hip_runtime.h>
#include <hip/hip_bf16.h>
#include <stdint.h>
#include <type_traits>

typedef __attribute__((ext_vector_type(8))) __bf16 bf16x8;
typedef __attribute__((ext_vector_type(4))) __bf16 bf16x4;
typedef __attribute__((ext_vector_type(2))) __bf16 bf16x2;
typedef __attribute__((ext_vector_type(4))) short s16x4;
typedef __attribute__((ext_vector_type(4))) float f32x4;
typedef __attribute__((ext_vector_type(2))) unsigned u32x2;
typedef __attribute__((ext_vector_type(4))) unsigned u32x4;
#define LAS __attribute__((address_space(3)))
#define DEV __device__ __forceinline__

constexpr int S_ = 2048, D_ = 640, F_ = 1536, H_ = 32;

constexpr size_t QK_HEAD = 128 * 768;
constexpr size_t V_HEAD  = 64 * 1280;
constexpr size_t WS_Q  = 0;
constexpr size_t WS_K  = WS_Q + H_ * QK_HEAD;
constexpr size_t WS_V  = WS_K + H_ * QK_HEAD;
constexpr size_t WS_O  = WS_V + H_ * V_HEAD;
constexpr size_t WS_H  = WS_O + (size_t)S_ * D_ * 2;
constexpr size_t WS_G  = WS_H + (size_t)S_ * D_ * 2;
constexpr size_t WS_X1 = WS_G + (size_t)S_ * D_ * 2;
constexpr size_t WS_U  = WS_X1 + (size_t)S_ * D_ * 2;
constexpr size_t WS_CS = WS_U + (size_t)S_ * F_ * 2;
constexpr size_t WS_WO = WS_CS + 16384;
constexpr size_t WS_W1 = WS_WO + (size_t)D_ * D_ * 2;
constexpr size_t WS_W2 = WS_W1 + (size_t)D_ * D_ * 2;
constexpr size_t WS_W3 = WS_W2 + (size_t)D_ * D_ * 2;
constexpr size_t WS_W4 = WS_W3 + (size_t)D_ * F_ * 2;
constexpr size_t WS_END = WS_W4 + (size_t)D_ * F_ * 2;
constexpr unsigned EMPTY = 0xffffffffu;

template <int I, int N, class Fn> DEV void static_for(Fn&& f) {
  if constexpr (I < N) { f(std::integral_constant<int, I>{}); static_for<I + 1, N>(f); }
}
template <int N> DEV void wait_vm() { asm volatile("s_waitcnt vmcnt(%0) lgkmcnt(0)" ::"n"(N) : "memory"); }
DEV void glds16(const char* sbase, unsigned voff, unsigned lds) {
  asm volatile("s_mov_b32 m0, %0\n\ts_nop 0\n\tglobal_load_lds_dwordx4 %1, %2" ::"s"(lds), "v"(voff), "s"(sbase) : "memory");
}
DEV unsigned lds_addr(const void* p) { return (unsigned)(uintptr_t)(LAS const void*)p; }
#define ST(i)
#define STR(i)
#define STDECL
#define STDUMP(o, ws, kid)
#define STARG
#define STPASS(i)
#define STDUMP2(ws, kid)
DEV void wait_lgkm0() { asm volatile("s_waitcnt lgkmcnt(0)" ::: "memory"); }
DEV void cfence() { asm volatile("" ::: "memory"); }
DEV float bfr(float x) { return (float)(__bf16)x; }
DEV unsigned pk2(float a, float b) { bf16x2 v = {(__bf16)a, (__bf16)b}; return __builtin_bit_cast(unsigned, v); }
DEV float bf_lo(unsigned u) { return __builtin_bit_cast(float, u << 16); }
DEV float bf_hi(unsigned u) { return __builtin_bit_cast(float, u & 0xffff0000u); }
DEV f32x4 mfma16(bf16x8 a, bf16x8 b, f32x4 c) { return __builtin_amdgcn_mfma_f32_16x16x32_bf16(a, b, c, 0, 0, 0); }
DEV unsigned ft_off(int row, int k, int KS) {
  return (unsigned)(((row >> 4) * KS + (k >> 5)) * 1024 + ((row & 15) + 16 * ((k >> 3) & 3)) * 16 + (k & 7) * 2);
}

DEV void ln_stats(float sum, float sq, float& mean, float& var, float& m, float& s) {
  mean = sum * (1.0f / D_);
  var = fmaxf(sq * (1.0f / D_) - mean * mean, 0.f);
  m = bfr(mean);
  s = bfr(sqrtf(bfr(bfr(var) + 1e-5f)));
}

DEV void dot_sum(float& s, bf16x8 f) {
  const bf16x2 one2 = {(__bf16)1.0f, (__bf16)1.0f};
#pragma unroll
  for (int e = 0; e < 4; ++e) { bf16x2 v = {f[2 * e], f[2 * e + 1]}; s = __builtin_amdgcn_fdot2_f32_bf16(v, one2, s, false); }
}
DEV void dot_sq(float& s, bf16x8 f) {
#pragma unroll
  for (int e = 0; e < 4; ++e) { bf16x2 v = {f[2 * e], f[2 * e + 1]}; s = __builtin_amdgcn_fdot2_f32_bf16(v, v, s, false); }
}
DEV unsigned f2u(float v) { return __builtin_bit_cast(unsigned, v); }
DEV float u2f(unsigned v) { return __builtin_bit_cast(float, v); }
DEV float red_g(float v) {
  const auto r = __builtin_amdgcn_permlane16_swap(f2u(v), f2u(v), false, false);
  v = u2f(r[0]) + u2f(r[1]);
  const auto s = __builtin_amdgcn_permlane32_swap(f2u(v), f2u(v), false, false);
  return u2f(s[0]) + u2f(s[1]);
}
DEV float max_g(float v) {
  const auto r = __builtin_amdgcn_permlane16_swap(f2u(v), f2u(v), false, false);
  v = fmaxf(u2f(r[0]), u2f(r[1]));
  const auto s = __builtin_amdgcn_permlane32_swap(f2u(v), f2u(v), false, false);
  return fmaxf(u2f(s[0]), u2f(s[1]));
}

DEV unsigned wrm_voff(int l, int q, int ldw) {
  return (unsigned)((l >> 4) * ldw + ((((l & 15) >> 1) ^ ((l >> 4) | (q << 2))) * 32) + (l & 1) * 16);
}
DEV bf16x8 wrm_frag(const char* p) {
  s16x4 lo = __builtin_amdgcn_ds_read_tr16_b64_v4i16((LAS s16x4*)(p));
  s16x4 hi = __builtin_amdgcn_ds_read_tr16_b64_v4i16((LAS s16x4*)(p + 1024));
  typedef __attribute__((ext_vector_type(8))) short s16x8;
  s16x8 v = __builtin_shufflevector(lo, hi, 0, 1, 2, 3, 4, 5, 6, 7);
  return __builtin_bit_cast(bf16x8, v);
}
DEV unsigned wrm_lane(int l, int jhi, int jj) {
  const int g = l >> 4, i = l & 15;
  return (unsigned)((8 * g + (i >> 2)) * 256 + ((((jhi ^ (g & 1)) << 2) | (jj ^ (i >> 2))) * 32) + (i & 3) * 8);
}

struct WJob { const char* src; char* dst; int ldw, wks, r4, perm; };
DEV WJob make_job(int id, const char* wo, const char* w1, const char* w2, const char* w3, const char* w4, char* ws) {
  const bool live = id < 390;
  const int b = live ? id : 389;
  const char* wsrc; char* wdst; int ldw = D_ * 2, wks = 20, kt, nt;
  if (b < 150) {
    const int mi = b / 50, u = b - mi * 50;
    wsrc = mi == 0 ? wo : (mi == 1 ? w1 : w2); wdst = ws + WS_WO + (size_t)mi * (D_ * D_ * 2);
    kt = u / 5; nt = u - kt * 5;
  } else if (b < 270) {
    const int u = b - 150; wsrc = w3; wdst = ws + WS_W3; ldw = F_ * 2; kt = u / 12; nt = u - kt * 12;
  } else {
    const int u = b - 270; wsrc = w4; wdst = ws + WS_W4; wks = 48; kt = u / 5; nt = u - kt * 5;
  }
  WJob j;
  j.src = wsrc + nt * 256;
  j.r4 = kt * 16; j.perm = b < 50;
  j.dst = live ? wdst + (size_t)(nt * 8 * wks + kt * 2) * 1024 : nullptr;
  j.ldw = ldw; j.wks = wks;
  return j;
}

template <bool SWAP>
struct GemmQ {
  f32x4 acc[4][4];
  STDECL;
  static constexpr int NG = 4, D = 3, NGRP = 10, SB = 32768, XCH = NG * SB;
  DEV bool run(char* smem, const char* Ab, const char* Wb, int k0, const WJob& job) {
    STR(0); ST(1);
    constexpr int LDA = D_ * 2, LDW = 3 * D_ * 2;
    const int tid = threadIdx.x, l = tid & 63;
    const int wave = __builtin_amdgcn_readfirstlane(tid >> 6);
    if (wave >= 4) {
      const bool isw = wave == 5;
      unsigned vo[2];
#pragma unroll
      for (int q = 0; q < 2; ++q)
        vo[q] = isw ? wrm_voff(l, q, LDW) : (unsigned)((l >> 3) * LDA + (((l & 7) ^ (q * 4 + (l >> 4))) * 16));
      const unsigned vx[4] = {vo[0], isw ? vo[0] : vo[1], isw ? vo[1] : vo[0], vo[1]};
      const unsigned dst0 = lds_addr(smem) + (isw ? 16384 : 0);
      const char* src0 = isw ? Wb : Ab;
      const int gstr = isw ? 64 * LDW : 128, istr = isw ? 4 * LDW : 8 * LDA;
      auto issue = [&](int grp) {
        int kg = grp + k0;
        kg = kg >= NGRP ? kg - NGRP : kg;
        const char* sp = src0 + (size_t)kg * gstr;
#pragma unroll
        for (int i = 0; i < 16; ++i) glds16(sp + (size_t)i * istr, vx[i & 3], dst0 + (grp % NG) * SB + i * 1024);
      };
      static_for<0, D>([&](auto gi) { issue(gi.value); });
      static_for<0, NGRP>([&](auto gi) {
        constexpr int grp = gi.value;
        constexpr int issued = grp + D < NGRP ? grp + D : NGRP;
        wait_vm<16 * (issued - grp - 1) + (grp > NGRP - D ? 16 : 0)>();
        __builtin_amdgcn_s_barrier();
        cfence();
        if constexpr (grp + D < NGRP) issue(grp + D);
        if constexpr (grp == NGRP - D) {
#pragma unroll
          for (int i = 0; i < 16; ++i) {
            int r4 = job.r4 + i;
            const int kq = r4 / 40, rr = r4 - kq * 40;
            if (job.perm) r4 = rr < 32 ? (8 * kq + (rr >> 2)) * 5 + (rr & 3) : (8 * kq + rr - 32) * 5 + 4;
            glds16(job.src + (size_t)r4 * 4 * job.ldw, wrm_voff(l, (i >> 1) & 1, job.ldw), dst0 + ((grp - 1) % NG) * SB + i * 1024);
          }
        }
      });
      __builtin_amdgcn_s_barrier();
      wait_vm<0>();
      if (job.dst) {
        const char* tb = smem + ((NGRP - D - 1) % NG) * SB + (isw ? 16384 : 0);
#pragma unroll
        for (int kk = 0; kk < 2; ++kk)
#pragma unroll
          for (int j = 0; j < 8; ++j) {
            const bf16x8 f = wrm_frag(tb + kk * 8192 + wrm_lane(l, j >> 2, j & 3));
            *(bf16x8*)(job.dst + (size_t)(j * job.wks + kk) * 1024 + l * 16) = f;
          }
      }
      return false;
    }
    const int wm = wave >> 1, wn = wave & 1, g = l >> 4;
    float rs[2] = {0.f, 0.f}, rq[2] = {0.f, 0.f}, cd[2] = {0.f, 0.f};
#pragma unroll
    for (int i = 0; i < 4; ++i)
#pragma unroll
      for (int j = 0; j < 4; ++j) acc[i][j] = f32x4{0.f, 0.f, 0.f, 0.f};
    const unsigned a0 = (unsigned)((wm * 64 + (l & 15)) * 128 + ((g ^ ((l & 15) >> 1)) * 16));
    const char* al[2][2] = {{smem + a0 + wn * 4096, smem + a0 + 4096 - wn * 4096},
                            {smem + (a0 ^ 64u) + wn * 4096, smem + (a0 ^ 64u) + 4096 - wn * 4096}};
    const char* wl[4];
#pragma unroll
    for (int j = 0; j < 4; ++j) wl[j] = smem + 16384 + wrm_lane(l, wn, j ^ (2 * wm));
    bf16x8 af[2][4], wf[2][4];
    auto ldfrag = [&](auto ti) {
      constexpr int t = ti.value, kk = t & 1, so = ((t >> 1) % NG) * SB;
#pragma unroll
      for (int i = 0; i < 4; ++i) af[kk][i] = *(const bf16x8*)(al[kk][i >> 1] + so + (i & 1) * 2048);
#pragma unroll
      for (int j = 0; j < 4; ++j) wf[kk][j] = wrm_frag(wl[j] + so + kk * 8192);
    };
    __builtin_amdgcn_s_barrier();
    cfence();
    ST(3);
    ldfrag(std::integral_constant<int, 0>{});
    static_for<0, 2 * NGRP>([&](auto ti) {
      constexpr int t = ti.value, kk = t & 1;
      if constexpr (t + 1 < 2 * NGRP) {
        if constexpr (kk == 1) {
          wait_lgkm0();
          __builtin_amdgcn_s_barrier();
          cfence();
          ST(3 + (t + 1) / 2);
        }
        ldfrag(std::integral_constant<int, t + 1>{});
      }
      __builtin_amdgcn_sched_barrier(0);
#pragma unroll
      for (int i = 0; i < 4; ++i)
#pragma unroll
        for (int j = 0; j < 4; ++j)
          acc[i][j] = SWAP ? mfma16(wf[kk][j], af[kk][i], acc[i][j]) : mfma16(af[kk][i], wf[kk][j], acc[i][j]);
#pragma unroll
      for (int i = 0; i < 2; ++i) { dot_sum(rs[i], af[kk][i]); dot_sq(rq[i], af[kk][i]); dot_sum(cd[i], wf[kk][i]); }
#pragma unroll
      for (int q = 0; q < 8; ++q) {
        __builtin_amdgcn_sched_group_barrier(0x8, 2, 0);
        __builtin_amdgcn_sched_group_barrier(0x2, 3, 0);
      }
      asm volatile("" : "+v"(rs[0]), "+v"(rs[1]), "+v"(rq[0]), "+v"(rq[1]), "+v"(cd[0]), "+v"(cd[1]));
      __builtin_amdgcn_sched_barrier(0);
    });
    ST(21);
    float2* xs = (float2*)(smem + XCH);
    float* xc = (float*)(smem + XCH + 1024);
#pragma unroll
    for (int i = 0; i < 2; ++i) {
      const float a = red_g(rs[i]), b = red_g(rq[i]), c = red_g(cd[i]);
      float mean, var, m, sd;
      ln_stats(a, b, mean, var, m, sd);
      const float rinv = 1.0f / sd;
      if (l < 16) { xs[(wm * 4 + wn * 2 + i) * 16 + l] = float2{rinv, -m * rinv}; xc[(wn * 4 + wm * 2 + i) * 16 + l] = c; }
    }
    wait_lgkm0();
    __builtin_amdgcn_s_barrier();
    cfence();
    return true;
  }
};

template <bool ISV>
DEV void qkv_body(char* smem, const char* x, const char* wqkv, char* ws, int rt, int ct, int k0, const WJob& job) {
  using GQ = GemmQ<!ISV>;
  GQ gm;
  const int row0 = rt * 128, n0 = ct * 128;
  if (!gm.run(smem, x + (size_t)row0 * (D_ * 2), wqkv + n0 * 2, k0, job)) return;
  const int tid = threadIdx.x, l = tid & 63, wave = tid >> 6, wm = wave >> 1, wn = wave & 1, g = l >> 4;
  const float2* xs = (const float2*)(smem + GQ::XCH);
  const float* xc = (const float*)(smem + GQ::XCH + 1024);
  if constexpr (!ISV) {
    f32x4 cs[4];
    unsigned po[4];
#pragma unroll
    for (int j = 0; j < 4; ++j) {
      const int ja = wn * 4 + (j ^ (2 * wm));
      cs[j] = *(const f32x4*)(xc + ja * 16 + 4 * g);
      const int c = (n0 + ja * 16 + 4 * g) % D_, head = c / 20, d0 = c - head * 20;
      po[j] = (unsigned)(head * (int)QK_HEAD + (d0 >> 3) * 256 + (d0 & 7) * 2) | (d0 == 16 ? 1u : 0u);
    }
    char* dstb = ws + (ct < 5 ? WS_Q : WS_K) + (size_t)(row0 >> 4) * 768 + (l & 15) * 16;
#pragma unroll
    for (int i = 0; i < 4; ++i) {
      const int ia = wm * 4 + (i ^ (2 * wn));
      const float2 st = xs[ia * 16 + (l & 15)];
#pragma unroll
      for (int j = 0; j < 4; ++j) {
        const f32x4 y = gm.acc[i][j] * st.x + cs[j] * st.y;
        char* p = dstb + ia * 768 + (po[j] & ~1u);
        if (po[j] & 1u) *(u32x4*)p = u32x4{pk2(y[0], y[1]), pk2(y[2], y[3]), 0u, 0u};
        else *(u32x2*)p = u32x2{pk2(y[0], y[1]), pk2(y[2], y[3])};
      }
    }
  } else {
    float cd[4];
    unsigned po[4];
#pragma unroll
    for (int j = 0; j < 4; ++j) {
      const int ja = wn * 4 + (j ^ (2 * wm));
      cd[j] = xc[ja * 16 + (l & 15)];
      const int c = n0 - 2 * D_ + ja * 16 + (l & 15), head = c / 20, d = c - head * 20;
      po[j] = (unsigned)(head * (int)V_HEAD + (d < 16 ? (g * 16 + d) * 16 : 1024 + (g * 4 + d - 16) * 16));
    }
    char* dstb = ws + WS_V + (size_t)(row0 >> 5) * 1280;
#pragma unroll
    for (int i = 0; i < 4; ++i) {
      const int ia = wm * 4 + (i ^ (2 * wn));
      const f32x4 s01 = *(const f32x4*)(xs + ia * 16 + 4 * g), s23 = *(const f32x4*)(xs + ia * 16 + 4 * g + 2);
      const f32x4 rr = {s01[0], s01[2], s23[0], s23[2]}, nm = {s01[1], s01[3], s23[1], s23[3]};
#pragma unroll
      for (int j = 0; j < 4; ++j) {
        const f32x4 y = gm.acc[i][j] * rr + nm * cd[j];
        *(u32x2*)(dstb + (ia >> 1) * 1280 + (i & 1) * 8 + po[j]) = u32x2{pk2(y[0], y[1]), pk2(y[2], y[3])};
      }
    }
  }
  STDUMP(gm, ws, 1);
}

DEV void colsum_job(char* smem, const char* w1, const char* w3, float* cs, int blk) {
  const int tid = threadIdx.x, c = tid % 17, rg = tid / 17;
  const int gc = blk * 17 + c;
  const char* base = gc < 80 ? w1 + gc * 16 : w3 + (gc - 80) * 16;
  const int ld = gc < 80 ? D_ * 2 : F_ * 2;
  float a[8] = {0.f, 0.f, 0.f, 0.f, 0.f, 0.f, 0.f, 0.f};
  if (rg < 20) {
#pragma unroll 16
    for (int r = rg; r < D_; r += 20) {
      u32x4 v = *(const u32x4*)(base + (size_t)r * ld);
#pragma unroll
      for (int e = 0; e < 4; ++e) { a[2 * e] += bf_lo(v[e]); a[2 * e + 1] += bf_hi(v[e]); }
    }
  }
  float* sm = (float*)smem;
  if (rg < 20) {
#pragma unroll
    for (int e = 0; e < 8; ++e) sm[rg * 136 + c * 8 + e] = a[e];
  }
  __syncthreads();
  if (tid < 136) {
    float t = 0.f;
#pragma unroll
    for (int r = 0; r < 20; ++r) t += sm[r * 136 + tid];
    cs[blk * 136 + tid] = t;
  }
}

__global__ __launch_bounds__(384) void k1_qkv(const char* x, const char* wqkv, const char* wo, const char* w1, const char* w2,
                                              const char* w3, const char* w4, char* ws) {
  extern __shared__ __attribute__((aligned(16))) char smem[];
  const int b = blockIdx.x, xcd = b & 7, j = b >> 3, r = j & 3, c = j >> 2;
  {
    const u32x4 e = {EMPTY, EMPTY, EMPTY, EMPTY};
    const int t = threadIdx.x, bt = b >> 4;
    *(u32x4*)(ws + WS_O + b * 10240 + t * 16) = e;
    if (t < 256) *(u32x4*)(ws + WS_O + b * 10240 + 6144 + t * 16) = e;
    else if (t < 336) *(u32x4*)(ws + WS_G + ((bt >> 1) * 16 + (bt & 1) * 15) * 20480 + (b & 15) * 1280 + (t - 256) * 16) = e;
  }
  if ((xcd & 1) && c == 7) { colsum_job(smem, w1, w3, (float*)(ws + WS_CS), (xcd >> 1) * 4 + r); return; }
  const int rt = (xcd >> 1) * 4 + r, ct = (xcd & 1) * 8 + c;
  const int wave = __builtin_amdgcn_readfirstlane(threadIdx.x >> 6);
  const WJob job = make_job((rt * 15 + ct) * 2 + (wave & 1), wo, w1, w2, w3, w4, ws);
  const int k0 = (3 * r + 4 * c) % 10;
  if (ct >= 10) qkv_body<true>(smem, x, wqkv, ws, rt, ct, k0, job);
  else qkv_body<false>(smem, x, wqkv, ws, rt, ct, k0, job);
}

typedef __attribute__((ext_vector_type(2))) float f32x2;
typedef unsigned long long u64;
DEV f32x2 unpk(unsigned u) { return f32x2{bf_lo(u), bf_hi(u)}; }
DEV unsigned pk2v(f32x2 v) { return pk2(v[0], v[1]); }
DEV f32x2 bfr2(f32x2 v) { return unpk(pk2v(v)); }
DEV f32x2 gelu2(f32x2 x) {
  const float c0 = -2.0f * 0.7978845608028654f * 1.4426950408889634f, c1 = c0 * 0.044715f;
  const f32x2 z = x * (x * x * c1 + c0);
  const f32x2 d = f32x2{__builtin_amdgcn_exp2f(z[0]), __builtin_amdgcn_exp2f(z[1])} + 1.0f;
  return x * f32x2{__builtin_amdgcn_rcpf(d[0]), __builtin_amdgcn_rcpf(d[1])};
}
DEV f32x2 lo2(f32x4 v) { return f32x2{v[0], v[1]}; }
DEV f32x2 hi2(f32x4 v) { return f32x2{v[2], v[3]}; }

typedef __amdgpu_buffer_rsrc_t rsrc_t;
constexpr unsigned OOB = 0x40000000u;
DEV unsigned full(unsigned v) { return v < EMPTY ? v : EMPTY - 1; }
DEV void st8(rsrc_t r, unsigned v, unsigned s, u32x2 d) { __builtin_amdgcn_raw_buffer_store_b64(u32x2{full(d[0]), full(d[1])}, r, v, s, 16); }
DEV void st16(rsrc_t r, unsigned v, unsigned s, u32x4 d) {
  __builtin_amdgcn_raw_buffer_store_b128(u32x4{full(d[0]), full(d[1]), full(d[2]), full(d[3])}, r, v, s, 16);
}
DEV void st_empty(rsrc_t r, unsigned v, unsigned s) { __builtin_amdgcn_raw_buffer_store_b128(u32x4{EMPTY, EMPTY, EMPTY, EMPTY}, r, v, s, 16); }
DEV u32x4 ld16(rsrc_t r, unsigned v, unsigned s) { return __builtin_amdgcn_raw_buffer_load_b128(r, v, s, 16); }
DEV u32x2 ld8(rsrc_t r, unsigned v, unsigned s) { return __builtin_amdgcn_raw_buffer_load_b64(r, v, s, 16); }
DEV u32x4 ld16p(rsrc_t r, unsigned v, unsigned s) { return __builtin_amdgcn_raw_buffer_load_b128(r, v, s, 0); }
DEV void vm0() { __builtin_amdgcn_s_waitcnt(0x0f70); cfence(); }

constexpr int LDS_TOT = 163840;
constexpr int AT_Q = 0, AT_K = AT_Q + 16 * 768, AT_V = AT_K + 32 * 768;
constexpr int SLOT = 25600, SLOT7 = 43008;
constexpr int L_W = 51200, L_W2 = 57344;
constexpr int L_SX = 159744;
constexpr int L_SY = 161792;
constexpr int C_P = L_SY, C_S = L_SY + 128, C_F = L_SY + 256, C_W2 = L_SY + 384, C_W4 = L_SY + 388, C_WC = L_SY + 392;
constexpr int L_LN = L_SY + 512;
DEV void lds_inc(unsigned a) {
  asm volatile("s_mov_b64 exec, 1\n\tds_add_u32 %0, %1\n\ts_mov_b64 exec, -1" ::"v"(a), "v"(1u) : "memory");
}
DEV void lds_wait(unsigned a, unsigned target) {
  for (int it = 0; it < (1 << 22); ++it) {
    const unsigned v = __builtin_amdgcn_readfirstlane(*(volatile LAS unsigned*)(uintptr_t)a);
    if (v >= target) break;
  }
  cfence();
}
DEV f32x4 lds_ld4(unsigned a) { return *(const LAS f32x4*)(uintptr_t)a; }
DEV unsigned max3u(unsigned a, unsigned b, unsigned c) { a = a > b ? a : b; return a > c ? a : c; }

template <int NT, int KW, int NTILE, int MODE, bool UNR, class TS, class TV, class TG, class TH>
DEV void m_phase(const bf16x8 (&W)[30], int l, unsigned lb, rsrc_t wr, int n0, int part, TS&& so, TV&& vo, TG&& guard, TH&& hook STARG) {
  constexpr int RS = NT * 64 + 16;
  const int g = l >> 4, li = l & 15;
  const unsigned pw = lb + part * (16 * RS) + li * RS + g * 16;
  u32x4 a[2][KW];
  auto issue = [&](auto i, auto bi) {
    constexpr int B = bi.value;
    const unsigned s = so(i), v = vo(i);
#pragma unroll
    for (int k = 0; k < (MODE == 1 ? KW - 1 : KW); ++k) a[B][k] = ld16(wr, v + k * 1024, s);
    if constexpr (MODE == 1) {
      const u32x2 t0 = ld8(wr, g * 256 + li * 8 + 4096, s), t1 = ld8(wr, g * 256 + li * 8 + 4096 + 128, s);
      a[B][KW - 1] = u32x4{t0[0], t0[1], t1[0], t1[1]};
    }
    cfence();
  };
  auto step = [&](auto i, auto bi) {
    constexpr int B = bi.value;
    const int n = n0 + i;
    for (int it = 0; it < (1 << 16); ++it) {
      vm0();
      unsigned m = 0;
#pragma unroll
      for (int k = 0; k < KW; ++k) m = max3u(max3u(m, a[B][k][0], a[B][k][1]), a[B][k][2], a[B][k][3]);
      if (__builtin_amdgcn_ballot_w64(m == EMPTY) == 0) break;
      issue(i, bi);
    }
    if (i == 0) { ST(STI); }
    if (i + 1 < NTILE) issue(i + 1, std::integral_constant<int, B ^ 1>{});
    __builtin_amdgcn_sched_barrier(0);
    f32x4 acc[NT];
#pragma unroll
    for (int j = 0; j < NT; ++j) acc[j] = f32x4{0.f, 0.f, 0.f, 0.f};
    constexpr int KS = MODE == 2 ? 3 : 0;
#pragma unroll
    for (int k = 0; k < KS; ++k)
#pragma unroll
      for (int j = 0; j < NT; ++j) acc[j] = mfma16(W[k * NT + j], __builtin_bit_cast(bf16x8, a[B][k]), acc[j]);
    if constexpr (MODE == 2) {
      float s0 = 0.f, s1 = 0.f, q0 = 0.f, q1 = 0.f;
#pragma unroll
      for (int k = 0; k < KW; ++k) {
        const bf16x8 f = __builtin_bit_cast(bf16x8, a[B][k]);
        dot_sum((k & 1) ? s1 : s0, f); dot_sq((k & 1) ? q1 : q0, f);
      }
      const float s = red_g(s0 + s1), q = red_g(q0 + q1);
      *(LAS f32x2*)(uintptr_t)(lb + L_SX + (n & 3) * 512 + li * 32 + part * 8) = f32x2{s, q};
#pragma unroll
      for (int t = 0; t < KS * NT; ++t) {
        __builtin_amdgcn_sched_group_barrier(0x8, 1, 0);
        __builtin_amdgcn_sched_group_barrier(0x2, 4, 0);
      }
      __builtin_amdgcn_sched_barrier(0);
      lds_inc(lb + C_S + n * 4);
    }
#pragma unroll
    for (int k = KS; k < KW; ++k)
#pragma unroll
      for (int j = 0; j < NT; ++j) acc[j] = mfma16(W[k * NT + j], __builtin_bit_cast(bf16x8, a[B][k]), acc[j]);
    __builtin_amdgcn_sched_barrier(0);
    const unsigned sl = guard(i);
#pragma unroll
    for (int j = 0; j < NT; ++j) *(LAS f32x4*)(uintptr_t)(pw + sl + j * 64) = acc[j];
    lds_inc(lb + C_P + n * 4);
    hook(i);
  };
  issue(0, std::integral_constant<int, 0>{});
  if constexpr (UNR) {
    static_for<0, NTILE>([&](auto i) { step(i, std::integral_constant<int, i.value & 1>{}); });
  } else {
#pragma unroll 1
    for (int i = 0; i < NTILE; i += 2) {
      step(i, std::integral_constant<int, 0>{});
      step(i + 1, std::integral_constant<int, 1>{});
    }
  }
  ST(STI + 1);
}

template <int NP, int RS>
DEV void e_sum(f32x4& lo, f32x4& hi, unsigned lb, unsigned a, int n) {
  lds_wait(lb + C_P + n * 4, NP);
  f32x4 p[NP][2];
#pragma unroll
  for (int q = 0; q < NP; ++q) { p[q][0] = lds_ld4(a + q * (16 * RS)); p[q][1] = lds_ld4(a + q * (16 * RS) + 16); }
  lds_inc(lb + C_F + n * 4);
#pragma unroll
  for (int s = 1; s < NP; s *= 2)
#pragma unroll
    for (int q = 0; q < NP; q += 2 * s) { p[q][0] += p[q + s][0]; p[q][1] += p[q + s][1]; }
  lo = p[0][0]; hi = p[0][1];
}
DEV void e_stats(float& sum, float& sq, unsigned lb, int n, int li) {
  lds_wait(lb + C_S + n * 4, 4);
  const f32x4 a = lds_ld4(lb + L_SX + (n & 3) * 512 + li * 32), b = lds_ld4(lb + L_SX + (n & 3) * 512 + li * 32 + 16);
  sum = (a[0] + a[2]) + (b[0] + b[2]); sq = (a[1] + a[3]) + (b[1] + b[3]);
}

template <int I>
DEV void attention(char* smem, int xcd, int w, u32x2& om, u32x2& ot) {
  const int l = threadIdx.x & 63, g = l >> 4, li = l & 15;
  u32x4 qv = *(const u32x4*)(smem + AT_Q + (2 * w + I) * 768 + (g < 3 ? g : 2) * 256 + li * 16);
  if (g == 3) qv = u32x4{0u, 0u, 0u, 0u};
  const bf16x8 qf = __builtin_bit_cast(bf16x8, qv);
  const char* Ks = smem + AT_K + (2 * w + I) * 768 + (g < 3 ? g : 2) * 256 + li * 16;
  const int kbase = xcd * 256 + 32 * w + 16 * I - 256;
  f32x4 s0[17];
  const f32x4 z4 = {0.f, 0.f, 0.f, 0.f};
#pragma unroll
  for (int c = 0; c < 17; ++c) s0[c] = mfma16(*(const bf16x8*)(Ks + c * 768), qf, z4);
  const float NEG = -__builtin_inff();
#pragma unroll
  for (int r = 0; r < 4; ++r) {
    const int kj = 4 * g + r;
    if (kj <= li) s0[0][r] = NEG;
    if (kj > li) s0[16][r] = NEG;
  }
  if (kbase < 0) {
#pragma unroll
    for (int c = 0; c < 17; ++c)
      if (kbase + 16 * c < 0) s0[c] = f32x4{NEG, NEG, NEG, NEG};
  }
  float m0 = NEG, m1 = NEG;
#pragma unroll
  for (int c = 0; c < 17; ++c) {
    m0 = fmaxf(fmaxf(m0, s0[c][0]), s0[c][1]);
    m1 = fmaxf(fmaxf(m1, s0[c][2]), s0[c][3]);
  }
  m0 = max_g(fmaxf(m0, m1));
  const float C2 = 0.22360679774997896f * 1.4426950408889634f;
  const float b0 = -m0 * C2;
  u32x2 p0[18];
#pragma unroll
  for (int c = 0; c < 17; ++c) {
    const f32x4 e = s0[c] * C2 + b0;
    p0[c + I] = u32x2{pk2(__builtin_amdgcn_exp2f(e[0]), __builtin_amdgcn_exp2f(e[1])), pk2(__builtin_amdgcn_exp2f(e[2]), __builtin_amdgcn_exp2f(e[3]))};
  }
  p0[I ? 0 : 17] = u32x2{0u, 0u};
  const char* Vs0 = smem + AT_V + w * 1280 + (g * 16 + li) * 16;
  const char* Vs1 = smem + AT_V + w * 1280 + 1024 + (g * 4 + (li & 3)) * 16;
  f32x4 o0 = z4, o1 = z4;
#pragma unroll
  for (int s = 0; s < 9; ++s) {
    bf16x8 v0 = *(const bf16x8*)(Vs0 + s * 1280);
    u32x4 v1u = *(const u32x4*)(Vs1 + s * 1280);
    if (li == 4) v1u = u32x4{0x3F803F80u, 0x3F803F80u, 0x3F803F80u, 0x3F803F80u};
    const u32x4 pa = {p0[2 * s][0], p0[2 * s][1], p0[2 * s + 1][0], p0[2 * s + 1][1]};
    const bf16x8 pf = __builtin_bit_cast(bf16x8, pa);
    o0 = mfma16(v0, pf, o0); o1 = mfma16(__builtin_bit_cast(bf16x8, v1u), pf, o1);
  }
  const float r0 = 1.0f / __shfl(o1[0], 16 + li);
  om = u32x2{pk2(o0[0] * r0, o0[1] * r0), pk2(o0[2] * r0, o0[3] * r0)};
  ot = u32x2{pk2(o1[0] * r0, o1[1] * r0), pk2(o1[2] * r0, o1[3] * r0)};
}

#define CTX \
  int tid_ = threadIdx.x, b_ = blockIdx.x; \
  asm volatile("" : "+v"(tid_), "+s"(b_)); \
  const int xcd = b_ & 7, jj = b_ >> 3, l = tid_ & 63, g = l >> 4, li = l & 15; \
  const int w = __builtin_amdgcn_readfirstlane(tid_ >> 6), q = w & 3, grp = w >> 2; \
  const int ct = jj & 7, ct16 = jj & 15; \
  const unsigned vl = l * 16; \
  const int G4 = (xcd * 4 + (jj >> 3)) * 4, dir = (jj >> 3) & 1; \
  const int B8 = (xcd * 2 + (jj >> 4)) * 8; \
  auto g4 = [&](int i) { return G4 + (dir ? 3 - i : i); }; \
  auto g8 = [&](int i) { return B8 + ((i & 1) ? 7 - (i >> 1) : (i >> 1)); }; \
  auto lane = [&](int) { return vl; }; \
  auto ring = [&](int n) { if (n >= 2) lds_wait(lb + C_F + (n - 2) * 4, 4); return (unsigned)((n & 1) * SLOT); }; \
  auto nohook = [](int) {}; \
  const int nu = q < 2 ? 3 : 2, un = (q < 2 ? 3 * q : 2 * q + 2) + (g < nu ? g : nu - 1); \
  const bool act = g < nu; \
  const int ko = ct * 10 + un; \
  const unsigned vft = (ko >> 2) * 1024 + (ko & 3) * 256 + li * 16, vst = act ? vft : OOB; \
  const unsigned pe = lb + li * 336 + un * 32; \
  const unsigned xo = li * (D_ * 2) + ko * 16; \
  const int k6 = ct16 * 12 + 3 * q + (g < 3 ? g : 2); \
  const unsigned v6 = g < 3 ? (unsigned)((k6 >> 2) * 1024 + (k6 & 3) * 256 + li * 16) : OOB; \
  const unsigned w4b = (unsigned)WS_W4 + ct * (5 * 48 * 1024) + w * 6144; \
  (void)0

__global__ __launch_bounds__(512) void kp_main(const char* x, char* ws, char* out) {
  extern __shared__ __attribute__((aligned(16))) char smem[];
  STDECL;
  STR(0); ST(1);
  const unsigned lb = lds_addr(smem);
  const rsrc_t wr = __builtin_amdgcn_make_buffer_rsrc(ws, 0, (int)WS_END, 0x00020000);
  bf16x8 W[30];
  int ga, gb;
  {
    CTX;
#pragma unroll
    for (int i = 0; i < 4; ++i) {
      st_empty(wr, vst, (unsigned)(grp ? WS_H : WS_G) + (G4 + i) * 20480);
      st_empty(wr, grp ? vst : OOB, (unsigned)WS_X1 + (G4 + i) * 20480);
    }
    const char* Qh = ws + WS_Q + (size_t)jj * QK_HEAD + xcd * 16 * 768;
    const char* Kh = ws + WS_K + (size_t)jj * QK_HEAD;
    const char* Vh = ws + WS_V + (size_t)jj * V_HEAD;
    const int koff0 = (xcd * 16 - 16) * 768, voff0 = (xcd * 8 - 8) * 1280;
#pragma unroll
    for (int i = 0; i < 7; ++i) {
      const int p = w + 8 * i;
      const char* src;
      if (p < 12) src = Qh + p * 1024;
      else if (p < 36) { const int o = koff0 + (p - 12) * 1024; src = Kh + (o < 0 ? 0 : o); }
      else { const int o = voff0 + (p - 36) * 1024; src = Vh + (o < 0 ? 0 : o); }
      glds16(src, vl, lb + AT_Q + p * 1024);
    }
    unsigned tch[4];
    constexpr unsigned WB = 3 * D_ * D_ * 2 + 2 * D_ * F_ * 2;
#pragma unroll
    for (int i = 0; i < 4; ++i) {
      const unsigned u = (i * 32 + jj) * 8 + w;
      tch[i] = __builtin_amdgcn_raw_buffer_load_b32(wr, u < WB / 8192 ? l * 128 : OOB, (unsigned)WS_WO + u * 8192, 0);
    }
    *(LAS unsigned*)(uintptr_t)(lb + L_SY + (tid_ & 127) * 4) = 0u;
    wait_vm<4>();
    __builtin_amdgcn_s_barrier();
    cfence();
    ST(2);
    u32x2 om0, ot0, om1, ot1;
    attention<0>(smem, xcd, w, om0, ot0);
    __builtin_amdgcn_sched_barrier(0);
    const unsigned wb = (unsigned)WS_WO + grp * (D_ * D_ * 2) + ct * (5 * 20 * 1024) + q * 5120;
#pragma unroll
    for (int k = 0; k < 5; ++k)
#pragma unroll
      for (int j = 0; j < 5; ++j) W[k * 5 + j] = __builtin_bit_cast(bf16x8, ld16p(wr, vl + k * 1024, wb + j * (20 * 1024)));
    const char* W2 = ws + WS_W2 + (size_t)ct * (5 * 20 * 1024);
#pragma unroll
    for (int i = 0; i < 13; ++i) {
      int u = w + 8 * i;
      u = u < 100 ? u : 99;
      const int t = u / 5, j = u - t * 5;
      glds16(W2 + (j * 20 + t) * 1024, vl, lb + L_W2 + u * 1024);
    }
    __builtin_amdgcn_sched_barrier(0);
    attention<1>(smem, xcd, w, om1, ot1);
    asm volatile("" :: "v"(tch[0]), "v"(tch[1]), "v"(tch[2]), "v"(tch[3]));
    {
      const auto s0 = __builtin_amdgcn_permlane16_swap(om0[0], om1[0], false, false);
      const auto s1 = __builtin_amdgcn_permlane16_swap(om0[1], om1[1], false, false);
      const auto t0 = __builtin_amdgcn_permlane16_swap(ot0[0], ot1[0], false, false);
      const auto t1 = __builtin_amdgcn_permlane16_swap(ot0[1], ot1[1], false, false);
      const unsigned Of = (unsigned)WS_O + (xcd * 16 + 2 * w) * 20480 + (jj >> 3) * 5120;
      st16(wr, (g & 1) * 20480 + (g >> 1) * 256 + li * 16, Of + (jj & 7) * 512, u32x4{s0[0], s1[0], s0[1], s1[1]});
      st8(wr, g < 2 ? g * 20480 + li * 8 : OOB, Of + 4096 + (jj & 7) * 128, u32x2{t0[0], t1[0]});
    }
    ST(3);
    vm0();
    __builtin_amdgcn_s_barrier();
    cfence();
    ga = grp; gb = grp;
    asm volatile("" : "+s"(ga), "+s"(gb));
  }
  if (ga == 0) {
    CTX;
#pragma unroll
    for (int i = 0; i < 8; ++i) st_empty(wr, v6, (unsigned)WS_U + (B8 + i) * 49152);
    m_phase<5, 5, 4, 1, false>(W, l, lb, wr, 0, q, [&](int i) { return (unsigned)WS_O + g4(i) * 20480 + q * 5120; }, lane, ring, nohook STPASS(4));
#pragma unroll
    for (int k = 0; k < 5; ++k)
#pragma unroll
      for (int j = 0; j < 5; ++j) W[k * 5 + j] = *(const LAS bf16x8*)(uintptr_t)(lb + L_W2 + (q * 25 + k * 5 + j) * 1024 + vl);
    lds_inc(lb + C_W2);
    f32x4 cs[2];
#pragma unroll
    for (int i = 0; i < 2; ++i) cs[i] = __builtin_bit_cast(f32x4, ld16p(wr, ko * 32 + i * 16, (unsigned)WS_CS));
    lds_wait(lb + C_W2, 4);
#pragma unroll
    for (int f = 0; f < 26; ++f) glds16(ws + w4b + 4 * 6144 + (f % 5) * (48 * 1024) + (f / 5) * 1024, vl, lb + L_W + (q * 26 + f) * 1024);
#pragma unroll 1
    for (int i = 0; i < 4; ++i) {
      const int n = 4 + i;
      float sum, sq, mean, var, m1, s1;
      e_stats(sum, sq, lb, n, li);
      ln_stats(sum, sq, mean, var, m1, s1);
      const float r1 = 1.0f / s1;
      const float m2 = bfr((mean - m1) * r1), v2 = bfr(var * r1 * r1);
      const float s2 = bfr(sqrtf(bfr(v2 + 1e-5f)));
      float rr = r1 / s2;
      const float nm = -(m1 + m2 * s1) * rr;
      if (w == 0) *(LAS f32x2*)(uintptr_t)(lb + L_LN + (i * 16 + li) * 8) = f32x2{m1, r1};
      f32x4 c0 = cs[0] * nm, c1 = cs[1] * nm;
      asm volatile("" : "+v"(rr), "+v"(c0), "+v"(c1));
      f32x4 lo, hi;
      e_sum<4, 336>(lo, hi, lb, pe + (n & 1) * SLOT, n);
      if (i == 0) { ST(6); }
      lo = lo * rr + c0; hi = hi * rr + c1;
      st16(wr, vst, (unsigned)WS_G + g4(i) * 20480,
           u32x4{pk2v(gelu2(bfr2(lo2(lo)))), pk2v(gelu2(bfr2(hi2(lo)))), pk2v(gelu2(bfr2(lo2(hi)))), pk2v(gelu2(bfr2(hi2(hi))))});
    }
    ST(7);
    {
      const int sr = li - ((q & 2) ? ((q & 1) ? -2 : 2) : ((q & 1) ? -1 : 1));
      const unsigned vg = (unsigned)(((sr >> 4) + 1) * 20480 + g * 256 + (sr & 15) * 16);
      m_phase<5, 5, 4, 0, false>(W, l, lb, wr, 8, q, [&](int i) { return (unsigned)WS_G + (g4(i) - 1) * 20480 + q * 5120; },
          [&](int i) { return (unsigned)(g4(i) * 16 + sr) < (unsigned)S_ ? vg : OOB; },
          [&](int i) { return ring(8 + i); }, [&](int i) { if (i == 0) lds_inc(lb + C_W4); } STPASS(8));
    }
#pragma unroll
    for (int k = 0; k < 6; ++k)
#pragma unroll
      for (int j = 0; j < 5; ++j) W[k * 5 + j] = __builtin_bit_cast(bf16x8, ld16p(wr, vl + k * 1024, w4b + j * (48 * 1024)));
    {
      const unsigned p6 = lb + li * 400 + (3 * q + (g < 3 ? g : 2)) * 32;
#pragma unroll
      for (int i = 0; i < 2; ++i) cs[i] = __builtin_bit_cast(f32x4, ld16p(wr, k6 * 32 + i * 16, (unsigned)WS_CS + D_ * 4));
#pragma unroll 1
      for (int i = 0; i < 8; ++i) {
        const int n = 12 + i;
        float sum, sq, mean, var, m, s;
        e_stats(sum, sq, lb, n, li);
        ln_stats(sum, sq, mean, var, m, s);
        float rinv = 1.0f / s;
        const float nm = -m * rinv;
        f32x4 c0 = cs[0] * nm, c1 = cs[1] * nm;
        asm volatile("" : "+v"(rinv), "+v"(c0), "+v"(c1));
        f32x4 lo, hi;
        e_sum<4, 400>(lo, hi, lb, p6 + (n & 1) * SLOT, n);
        if (i == 0) { ST(10); }
        lo = lo * rinv + c0; hi = hi * rinv + c1;
        st16(wr, v6, (unsigned)WS_U + g8(i) * 49152,
             u32x4{pk2v(gelu2(bfr2(lo2(lo)))), pk2v(gelu2(bfr2(hi2(lo)))), pk2v(gelu2(bfr2(lo2(hi)))), pk2v(gelu2(bfr2(hi2(hi))))});
      }
      ST(11);
    }
  }
  if (gb != 0) {
    CTX;
    const rsrc_t xr = __builtin_amdgcn_make_buffer_rsrc((void*)x, 0, S_ * D_ * 2, 0x00020000);
    {
      u32x4 xv[4];
#pragma unroll
      for (int i = 0; i < 4; ++i) xv[i] = ld16p(xr, xo, g4(i) * (16 * D_ * 2));
#pragma unroll
      for (int i = 0; i < 4; ++i) {
        f32x4 lo, hi;
        e_sum<4, 336>(lo, hi, lb, pe + (i & 1) * SLOT, i);
        if (i == 0) { ST(4); }
        st16(wr, vst, (unsigned)WS_H + g4(i) * 20480,
             u32x4{pk2v(unpk(xv[i][0]) + bfr2(lo2(lo))), pk2v(unpk(xv[i][1]) + bfr2(hi2(lo))),
                   pk2v(unpk(xv[i][2]) + bfr2(lo2(hi))), pk2v(unpk(xv[i][3]) + bfr2(hi2(hi)))});
      }
      ST(5);
    }
    m_phase<5, 5, 4, 2, false>(W, l, lb, wr, 4, q, [&](int i) { return (unsigned)WS_H + g4(i) * 20480 + q * 5120; }, lane,
        [&](int i) { return ring(4 + i); }, nohook STPASS(6));
    {
      u32x4 hv[4];
#pragma unroll
      for (int i = 0; i < 4; ++i) hv[i] = ld16(wr, vft, (unsigned)WS_H + g4(i) * 20480);
      cfence();
      const unsigned w3b = (unsigned)WS_W3 + ct16 * (6 * 20 * 1024) + q * 5120;
#pragma unroll
      for (int k = 0; k < 5; ++k)
#pragma unroll
        for (int j = 0; j < 6; ++j) W[k * 6 + j] = __builtin_bit_cast(bf16x8, ld16p(wr, vl + k * 1024, w3b + j * (20 * 1024)));
#pragma unroll
      for (int i = 0; i < 4; ++i) {
        const int n = 8 + i;
        f32x4 lo, hi;
        e_sum<4, 336>(lo, hi, lb, pe + (n & 1) * SLOT, n);
        if (i == 0) { ST(8); }
        const f32x2 mr = *(const LAS f32x2*)(uintptr_t)(lb + L_LN + (i * 16 + li) * 8);
        const float m1 = mr[0], r1 = mr[1];
        st16(wr, vst, (unsigned)WS_X1 + g4(i) * 20480,
             u32x4{pk2v(bfr2(bfr2(unpk(hv[i][0]) - m1) * r1) + bfr2(lo2(lo))), pk2v(bfr2(bfr2(unpk(hv[i][1]) - m1) * r1) + bfr2(hi2(lo))),
                   pk2v(bfr2(bfr2(unpk(hv[i][2]) - m1) * r1) + bfr2(lo2(hi))), pk2v(bfr2(bfr2(unpk(hv[i][3]) - m1) * r1) + bfr2(hi2(hi)))});
      }
      ST(9);
    }
    m_phase<6, 5, 8, 2, false>(W, l, lb, wr, 12, q, [&](int i) { return (unsigned)WS_X1 + g8(i) * 20480 + q * 5120; }, lane,
        [&](int i) { return ring(12 + i); }, nohook STPASS(10));
#pragma unroll
    for (int j = 1; j < 5; ++j) W[25 + j] = __builtin_bit_cast(bf16x8, ld16p(wr, vl + 5 * 1024, w4b + j * (48 * 1024)));
    lds_wait(lb + C_W4, 4);
#pragma unroll
    for (int f = 0; f < 26; ++f) W[f] = *(const LAS bf16x8*)(uintptr_t)(lb + L_W + (q * 26 + f) * 1024 + vl);
    lds_inc(lb + C_WC);
  }
  {
    CTX;
    const rsrc_t orr = __builtin_amdgcn_make_buffer_rsrc(out, 0, S_ * D_ * 2, 0x00020000);
    if (grp) { lds_wait(lb + C_P + 19 * 4, 4); }
    u32x4 hv[2], xv[2];
#pragma unroll
    for (int i = 0; i < 2; ++i) {
      const unsigned to = g4(2 * i + grp) * 20480;
      hv[i] = ld16(wr, vft, (unsigned)WS_H + to); xv[i] = ld16(wr, vft, (unsigned)WS_X1 + to);
    }
    auto fin = [&](int p, auto si) {
      constexpr int s = si.value;
      f32x4 lo, hi;
      e_sum<8, 336>(lo, hi, lb, pe + (p % 3) * SLOT7, 20 + p);
      const u32x4 y = {pk2v(unpk(hv[s][0]) + bfr2(unpk(xv[s][0]) + bfr2(lo2(lo)))), pk2v(unpk(hv[s][1]) + bfr2(unpk(xv[s][1]) + bfr2(hi2(lo)))),
                       pk2v(unpk(hv[s][2]) + bfr2(unpk(xv[s][2]) + bfr2(lo2(hi)))), pk2v(unpk(hv[s][3]) + bfr2(unpk(xv[s][3]) + bfr2(hi2(hi))))};
      __builtin_amdgcn_raw_buffer_store_b128(y, orr, act ? xo : OOB, g4(p) * (16 * D_ * 2), 0);
    };
    m_phase<5, 6, 4, 0, true>(W, l, lb, wr, 20, w, [&](int i) { return (unsigned)WS_U + g4(i) * 49152 + w * 6144; }, lane,
        [&](auto i) {
          if constexpr (i.value == 0) { lds_wait(lb + C_F + 18 * 4, 4); lds_wait(lb + C_F + 19 * 4, 4); }
          if constexpr (i.value == 1) lds_wait(lb + C_WC, 4);
          if constexpr (i.value == 3) lds_wait(lb + C_F + 20 * 4, 4);
          return (unsigned)((i.value % 3) * SLOT7);
        },
        [&](auto i) {
          if constexpr (i.value == 1) { if (grp == 0) fin(0, std::integral_constant<int, 0>{}); }
          if constexpr (i.value == 2) { if (grp == 1) fin(1, std::integral_constant<int, 0>{}); }
          if constexpr (i.value == 3) fin(2 + grp, std::integral_constant<int, 1>{});
        } STPASS(12));
    STDUMP2(ws, 2 + grp);
  }
}

extern "C" void kernel_launch(void* const* d_in, const int* in_sizes, int n_in, void* d_out, int out_size,
                              void* d_ws, size_t ws_size, hipStream_t stream) {
  const char* x = (const char*)d_in[0];
  const char* wqkv = (const char*)d_in[1];
  const char* wo = (const char*)d_in[2];
  const char* w1 = (const char*)d_in[4];
  const char* w2 = (const char*)d_in[5];
  const char* w3 = (const char*)d_in[6];
  const char* w4 = (const char*)d_in[7];
  char* ws = (char*)d_ws;
  if (ws_size < WS_END) return;
  constexpr int LQ = 4 * 32768 + 2048;
  (void)hipFuncSetAttribute((const void*)k1_qkv, hipFuncAttributeMaxDynamicSharedMemorySize, LQ);
  (void)hipFuncSetAttribute((const void*)kp_main, hipFuncAttributeMaxDynamicSharedMemorySize, LDS_TOT);
  k1_qkv<<<256, 384, LQ, stream>>>(x, wqkv, wo, w1, w2, w3, w4, ws);
  kp_main<<<256, 512, LDS_TOT, stream>>>(x, ws, (char*)d_out);
}
```

```cpp
#include <hip/hip_runtime.h>
#include <hip/hip_bf16.h>
#include <stdint.h>
#include <type_traits>

typedef __attribute__((ext_vector_type(8))) __bf16 bf16x8;
typedef __attribute__((ext_vector_type(4))) __bf16 bf16x4;
typedef __attribute__((ext_vector_type(2))) __bf16 bf16x2;
typedef __attribute__((ext_vector_type(4))) short s16x4;
typedef __attribute__((ext_vector_type(4))) float f32x4;
typedef __attribute__((ext_vector_type(2))) unsigned u32x2;
typedef __attribute__((ext_vector_type(4))) unsigned u32x4;
#define LAS __attribute__((address_space(3)))
#define DEV __device__ __forceinline__

constexpr int S_ = 2048, D_ = 640, F_ = 1536, H_ = 32;

constexpr size_t QK_HEAD = 128 * 768;
constexpr size_t V_HEAD  = 64 * 1280;
constexpr size_t WS_Q  = 0;
constexpr size_t WS_K  = WS_Q + H_ * QK_HEAD;
constexpr size_t WS_V  = WS_K + H_ * QK_HEAD;
constexpr size_t WS_O  = WS_V + H_ * V_HEAD;
constexpr size_t WS_H  = WS_O + (size_t)S_ * D_ * 2;
constexpr size_t WS_G  = WS_H + (size_t)S_ * D_ * 2;
constexpr size_t WS_X1 = WS_G + (size_t)S_ * D_ * 2;
constexpr size_t WS_U  = WS_X1 + (size_t)S_ * D_ * 2;
constexpr size_t WS_CS = WS_U + (size_t)S_ * F_ * 2;
constexpr size_t WS_WO = WS_CS + 16384;
constexpr size_t WS_W1 = WS_WO + (size_t)D_ * D_ * 2;
constexpr size_t WS_W2 = WS_W1 + (size_t)D_ * D_ * 2;
constexpr size_t WS_W3 = WS_W2 + (size_t)D_ * D_ * 2;
constexpr size_t WS_W4 = WS_W3 + (size_t)D_ * F_ * 2;
constexpr size_t WS_END = WS_W4 + (size_t)D_ * F_ * 2;
constexpr unsigned EMPTY = 0xffffffffu;

template <int I, int N, class Fn> DEV void static_for(Fn&& f) {
  if constexpr (I < N) { f(std::integral_constant<int, I>{}); static_for<I + 1, N>(f); }
}
template <int N> DEV void wait_vm() { asm volatile("s_waitcnt vmcnt(%0) lgkmcnt(0)" ::"n"(N) : "memory"); }
DEV void glds16(const char* sbase, unsigned voff, unsigned lds) {
  asm volatile("s_mov_b32 m0, %0\n\ts_nop 0\n\tglobal_load_lds_dwordx4 %1, %2" ::"s"(lds), "v"(voff), "s"(sbase) : "memory");
}
DEV unsigned lds_addr(const void* p) { return (unsigned)(uintptr_t)(LAS const void*)p; }
#define ST(i)
#define STR(i)
#define STDECL
#define STDUMP(o, ws, kid)
#define STARG
#define STPASS(i)
#define STDUMP2(ws, kid)
DEV void wait_lgkm0() { asm volatile("s_waitcnt lgkmcnt(0)" ::: "memory"); }
DEV void cfence() { asm volatile("" ::: "memory"); }
DEV float bfr(float x) { return (float)(__bf16)x; }
DEV unsigned pk2(float a, float b) { bf16x2 v = {(__bf16)a, (__bf16)b}; return __builtin_bit_cast(unsigned, v); }
DEV float bf_lo(unsigned u) { return __builtin_bit_cast(float, u << 16); }
DEV float bf_hi(unsigned u) { return __builtin_bit_cast(float, u & 0xffff0000u); }
DEV f32x4 mfma16(bf16x8 a, bf16x8 b, f32x4 c) { return __builtin_amdgcn_mfma_f32_16x16x32_bf16(a, b, c, 0, 0, 0); }
DEV unsigned ft_off(int row, int k, int KS) {
  return (unsigned)(((row >> 4) * KS + (k >> 5)) * 1024 + ((row & 15) + 16 * ((k >> 3) & 3)) * 16 + (k & 7) * 2);
}

DEV void ln_stats(float sum, float sq, float& mean, float& var, float& m, float& s) {
  mean = sum * (1.0f / D_);
  var = fmaxf(sq * (1.0f / D_) - mean * mean, 0.f);
  m = bfr(mean);
  s = bfr(sqrtf(bfr(bfr(var) + 1e-5f)));
}

DEV void dot_sum(float& s, bf16x8 f) {
  const bf16x2 one2 = {(__bf16)1.0f, (__bf16)1.0f};
#pragma unroll
  for (int e = 0; e < 4; ++e) { bf16x2 v = {f[2 * e], f[2 * e + 1]}; s = __builtin_amdgcn_fdot2_f32_bf16(v, one2, s, false); }
}
DEV void dot_sq(float& s, bf16x8 f) {
#pragma unroll
  for (int e = 0; e < 4; ++e) { bf16x2 v = {f[2 * e], f[2 * e + 1]}; s = __builtin_amdgcn_fdot2_f32_bf16(v, v, s, false); }
}
DEV unsigned f2u(float v) { return __builtin_bit_cast(unsigned, v); }
DEV float u2f(unsigned v) { return __builtin_bit_cast(float, v); }
DEV float red_g(float v) {
  const auto r = __builtin_amdgcn_permlane16_swap(f2u(v), f2u(v), false, false);
  v = u2f(r[0]) + u2f(r[1]);
  const auto s = __builtin_amdgcn_permlane32_swap(f2u(v), f2u(v), false, false);
  return u2f(s[0]) + u2f(s[1]);
}
DEV float max_g(float v) {
  const auto r = __builtin_amdgcn_permlane16_swap(f2u(v), f2u(v), false, false);
  v = fmaxf(u2f(r[0]), u2f(r[1]));
  const auto s = __builtin_amdgcn_permlane32_swap(f2u(v), f2u(v), false, false);
  return fmaxf(u2f(s[0]), u2f(s[1]));
}

DEV unsigned wrm_voff(int l, int q, int ldw) {
  return (unsigned)((l >> 4) * ldw + ((((l & 15) >> 1) ^ ((l >> 4) | (q << 2))) * 32) + (l & 1) * 16);
}
DEV bf16x8 wrm_frag(const char* p) {
  s16x4 lo = __builtin_amdgcn_ds_read_tr16_b64_v4i16((LAS s16x4*)(p));
  s16x4 hi = __builtin_amdgcn_ds_read_tr16_b64_v4i16((LAS s16x4*)(p + 1024));
  typedef __attribute__((ext_vector_type(8))) short s16x8;
  s16x8 v = __builtin_shufflevector(lo, hi, 0, 1, 2, 3, 4, 5, 6, 7);
  return __builtin_bit_cast(bf16x8, v);
}
DEV unsigned wrm_lane(int l, int jhi, int jj) {
  const int g = l >> 4, i = l & 15;
  return (unsigned)((8 * g + (i >> 2)) * 256 + ((((jhi ^ (g & 1)) << 2) | (jj ^ (i >> 2))) * 32) + (i & 3) * 8);
}

struct WJob { const char* src; char* dst; int ldw, wks, r4, perm; };
DEV WJob make_job(int id, const char* wo, const char* w1, const char* w2, const char* w3, const char* w4, char* ws) {
  const bool live = id < 390;
  const int b = live ? id : 389;
  const char* wsrc; char* wdst; int ldw = D_ * 2, wks = 20, kt, nt;
  if (b < 150) {
    const int mi = b / 50, u = b - mi * 50;
    wsrc = mi == 0 ? wo : (mi == 1 ? w1 : w2); wdst = ws + WS_WO + (size_t)mi * (D_ * D_ * 2);
    kt = u / 5; nt = u - kt * 5;
  } else if (b < 270) {
    const int u = b - 150; wsrc = w3; wdst = ws + WS_W3; ldw = F_ * 2; kt = u / 12; nt = u - kt * 12;
  } else {
    const int u = b - 270; wsrc = w4; wdst = ws + WS_W4; wks = 48; kt = u / 5; nt = u - kt * 5;
  }
  WJob j;
  j.src = wsrc + nt * 256;
  j.r4 = kt * 16; j.perm = b < 50;
  j.dst = live ? wdst + (size_t)(nt * 8 * wks + kt * 2) * 1024 : nullptr;
  j.ldw = ldw; j.wks = wks;
  return j;
}

template <bool SWAP>
struct GemmQ {
  f32x4 acc[4][4];
  STDECL;
  static constexpr int NG = 4, D = 3, NGRP = 10, SB = 32768, XCH = NG * SB;
  DEV bool run(char* smem, const char* Ab, const char* Wb, int k0, const WJob& job) {
    STR(0); ST(1);
    constexpr int LDA = D_ * 2, LDW = 3 * D_ * 2;
    const int tid = threadIdx.x, l = tid & 63;
    const int wave = __builtin_amdgcn_readfirstlane(tid >> 6);
    if (wave >= 4) {
      const bool isw = wave == 5;
      unsigned vo[2];
#pragma unroll
      for (int q = 0; q < 2; ++q)
        vo[q] = isw ? wrm_voff(l, q, LDW) : (unsigned)((l >> 3) * LDA + (((l & 7) ^ (q * 4 + (l >> 4))) * 16));
      const unsigned vx[4] = {vo[0], isw ? vo[0] : vo[1], isw ? vo[1] : vo[0], vo[1]};
      const unsigned dst0 = lds_addr(smem) + (isw ? 16384 : 0);
      const char* src0 = isw ? Wb : Ab;
      const int gstr = isw ? 64 * LDW : 128, istr = isw ? 4 * LDW : 8 * LDA;
      auto issue = [&](int grp) {
        int kg = grp + k0;
        kg = kg >= NGRP ? kg - NGRP : kg;
        const char* sp = src0 + (size_t)kg * gstr;
#pragma unroll
        for (int i = 0; i < 16; ++i) glds16(sp + (size_t)i * istr, vx[i & 3], dst0 + (grp % NG) * SB + i * 1024);
      };
      static_for<0, D>([&](auto gi) { issue(gi.value); });
      static_for<0, NGRP>([&](auto gi) {
        constexpr int grp = gi.value;
        constexpr int issued = grp + D < NGRP ? grp + D : NGRP;
        wait_vm<16 * (issued - grp - 1) + (grp > NGRP - D ? 16 : 0)>();
        __builtin_amdgcn_s_barrier();
        cfence();
        if constexpr (grp + D < NGRP) issue(grp + D);
        if constexpr (grp == NGRP - D) {
#pragma unroll
          for (int i = 0; i < 16; ++i) {
            int r4 = job.r4 + i;
            const int kq = r4 / 40, rr = r4 - kq * 40;
            if (job.perm) r4 = rr < 32 ? (8 * kq + (rr >> 2)) * 5 + (rr & 3) : (8 * kq + rr - 32) * 5 + 4;
            glds16(job.src + (size_t)r4 * 4 * job.ldw, wrm_voff(l, (i >> 1) & 1, job.ldw), dst0 + ((grp - 1) % NG) * SB + i * 1024);
          }
        }
      });
      __builtin_amdgcn_s_barrier();
      wait_vm<0>();
      if (job.dst) {
        const char* tb = smem + ((NGRP - D - 1) % NG) * SB + (isw ? 16384 : 0);
#pragma unroll
        for (int kk = 0; kk < 2; ++kk)
#pragma unroll
          for (int j = 0; j < 8; ++j) {
            const bf16x8 f = wrm_frag(tb + kk * 8192 + wrm_lane(l, j >> 2, j & 3));
            *(bf16x8*)(job.dst + (size_t)(j * job.wks + kk) * 1024 + l * 16) = f;
          }
      }
      return false;
    }
    const int wm = wave >> 1, wn = wave & 1, g = l >> 4;
    float rs[2] = {0.f, 0.f}, rq[2] = {0.f, 0.f}, cd[2] = {0.f, 0.f};
#pragma unroll
    for (int i = 0; i < 4; ++i)
#pragma unroll
      for (int j = 0; j < 4; ++j) acc[i][j] = f32x4{0.f, 0.f, 0.f, 0.f};
    const unsigned a0 = (unsigned)((wm * 64 + (l & 15)) * 128 + ((g ^ ((l & 15) >> 1)) * 16));
    const char* al[2][2] = {{smem + a0 + wn * 4096, smem + a0 + 4096 - wn * 4096},
                            {smem + (a0 ^ 64u) + wn * 4096, smem + (a0 ^ 64u) + 4096 - wn * 4096}};
    const char* wl[4];
#pragma unroll
    for (int j = 0; j < 4; ++j) wl[j] = smem + 16384 + wrm_lane(l, wn, j ^ (2 * wm));
    bf16x8 af[2][4], wf[2][4];
    auto ldfrag = [&](auto ti) {
      constexpr int t = ti.value, kk = t & 1, so = ((t >> 1) % NG) * SB;
#pragma unroll
      for (int i = 0; i < 4; ++i) af[kk][i] = *(const bf16x8*)(al[kk][i >> 1] + so + (i & 1) * 2048);
#pragma unroll
      for (int j = 0; j < 4; ++j) wf[kk][j] = wrm_frag(wl[j] + so + kk * 8192);
    };
    __builtin_amdgcn_s_barrier();
    cfence();
    ST(3);
    ldfrag(std::integral_constant<int, 0>{});
    static_for<0, 2 * NGRP>([&](auto ti) {
      constexpr int t = ti.value, kk = t & 1;
      if constexpr (t + 1 < 2 * NGRP) {
        if constexpr (kk == 1) {
          wait_lgkm0();
          __builtin_amdgcn_s_barrier();
          cfence();
          ST(3 + (t + 1) / 2);
        }
        ldfrag(std::integral_constant<int, t + 1>{});
      }
      __builtin_amdgcn_sched_barrier(0);
#pragma unroll
      for (int i = 0; i < 4; ++i)
#pragma unroll
        for (int j = 0; j < 4; ++j)
          acc[i][j] = SWAP ? mfma16(wf[kk][j], af[kk][i], acc[i][j]) : mfma16(af[kk][i], wf[kk][j], acc[i][j]);
#pragma unroll
      for (int i = 0; i < 2; ++i) { dot_sum(rs[i], af[kk][i]); dot_sq(rq[i], af[kk][i]); dot_sum(cd[i], wf[kk][i]); }
#pragma unroll
      for (int q = 0; q < 8; ++q) {
        __builtin_amdgcn_sched_group_barrier(0x8, 2, 0);
        __builtin_amdgcn_sched_group_barrier(0x2, 3, 0);
      }
      asm volatile("" : "+v"(rs[0]), "+v"(rs[1]), "+v"(rq[0]), "+v"(rq[1]), "+v"(cd[0]), "+v"(cd[1]));
      __builtin_amdgcn_sched_barrier(0);
    });
    ST(21);
    float2* xs = (float2*)(smem + XCH);
    float* xc = (float*)(smem + XCH + 1024);
#pragma unroll
    for (int i = 0; i < 2; ++i) {
      const float a = red_g(rs[i]), b = red_g(rq[i]), c = red_g(cd[i]);
      float mean, var, m, sd;
      ln_stats(a, b, mean, var, m, sd);
      const float rinv = 1.0f / sd;
      if (l < 16) { xs[(wm * 4 + wn * 2 + i) * 16 + l] = float2{rinv, -m * rinv}; xc[(wn * 4 + wm * 2 + i) * 16 + l] = c; }
    }
    wait_lgkm0();
    __builtin_amdgcn_s_barrier();
    cfence();
    return true;
  }
};

template <bool ISV>
DEV void qkv_body(char* smem, const char* x, const char* wqkv, char* ws, int rt, int ct, int k0, const WJob& job) {
  using GQ = GemmQ<!ISV>;
  GQ gm;
  const int row0 = rt * 128, n0 = ct * 128;
  if (!gm.run(smem, x + (size_t)row0 * (D_ * 2), wqkv + n0 * 2, k0, job)) return;
  const int tid = threadIdx.x, l = tid & 63, wave = tid >> 6, wm = wave >> 1, wn = wave & 1, g = l >> 4;
  const float2* xs = (const float2*)(smem + GQ::XCH);
  const float* xc = (const float*)(smem + GQ::XCH + 1024);
  if constexpr (!ISV) {
    f32x4 cs[4];
    unsigned po[4];
#pragma unroll
    for (int j = 0; j < 4; ++j) {
      const int ja = wn * 4 + (j ^ (2 * wm));
      cs[j] = *(const f32x4*)(xc + ja * 16 + 4 * g);
      const int c = (n0 + ja * 16 + 4 * g) % D_, head = c / 20, d0 = c - head * 20;
      po[j] = (unsigned)(head * (int)QK_HEAD + (d0 >> 3) * 256 + (d0 & 7) * 2) | (d0 == 16 ? 1u : 0u);
    }
    char* dstb = ws + (ct < 5 ? WS_Q : WS_K) + (size_t)(row0 >> 4) * 768 + (l & 15) * 16;
#pragma unroll
    for (int i = 0; i < 4; ++i) {
      const int ia = wm * 4 + (i ^ (2 * wn));
      const float2 st = xs[ia * 16 + (l & 15)];
#pragma unroll
      for (int j = 0; j < 4; ++j) {
        const f32x4 y = gm.acc[i][j] * st.x + cs[j] * st.y;
        char* p = dstb + ia * 768 + (po[j] & ~1u);
        if (po[j] & 1u) *(u32x4*)p = u32x4{pk2(y[0], y[1]), pk2(y[2], y[3]), 0u, 0u};
        else *(u32x2*)p = u32x2{pk2(y[0], y[1]), pk2(y[2], y[3])};
      }
    }
  } else {
    float cd[4];
    unsigned po[4];
#pragma unroll
    for (int j = 0; j < 4; ++j) {
      const int ja = wn * 4 + (j ^ (2 * wm));
      cd[j] = xc[ja * 16 + (l & 15)];
      const int c = n0 - 2 * D_ + ja * 16 + (l & 15), head = c / 20, d = c - head * 20;
      po[j] = (unsigned)(head * (int)V_HEAD + (d < 16 ? (g * 16 + d) * 16 : 1024 + (g * 4 + d - 16) * 16));
    }
    char* dstb = ws + WS_V + (size_t)(row0 >> 5) * 1280;
#pragma unroll
    for (int i = 0; i < 4; ++i) {
      const int ia = wm * 4 + (i ^ (2 * wn));
      const f32x4 s01 = *(const f32x4*)(xs + ia * 16 + 4 * g), s23 = *(const f32x4*)(xs + ia * 16 + 4 * g + 2);
      const f32x4 rr = {s01[0], s01[2], s23[0], s23[2]}, nm = {s01[1], s01[3], s23[1], s23[3]};
#pragma unroll
      for (int j = 0; j < 4; ++j) {
        const f32x4 y = gm.acc[i][j] * rr + nm * cd[j];
        *(u32x2*)(dstb + (ia >> 1) * 1280 + (i & 1) * 8 + po[j]) = u32x2{pk2(y[0], y[1]), pk2(y[2], y[3])};
      }
    }
  }
  STDUMP(gm, ws, 1);
}

DEV void colsum_job(char* smem, const char* w1, const char* w3, float* cs, int blk) {
  const int tid = threadIdx.x, c = tid % 17, rg = tid / 17;
  const int gc = blk * 17 + c;
  const char* base = gc < 80 ? w1 + gc * 16 : w3 + (gc - 80) * 16;
  const int ld = gc < 80 ? D_ * 2 : F_ * 2;
  float a[8] = {0.f, 0.f, 0.f, 0.f, 0.f, 0.f, 0.f, 0.f};
  if (rg < 20) {
#pragma unroll 16
    for (int r = rg; r < D_; r += 20) {
      u32x4 v = *(const u32x4*)(base + (size_t)r * ld);
#pragma unroll
      for (int e = 0; e < 4; ++e) { a[2 * e] += bf_lo(v[e]); a[2 * e + 1] += bf_hi(v[e]); }
    }
  }
  float* sm = (float*)smem;
  if (rg < 20) {
#pragma unroll
    for (int e = 0; e < 8; ++e) sm[rg * 136 + c * 8 + e] = a[e];
  }
  __syncthreads();
  if (tid < 136) {
    float t = 0.f;
#pragma unroll
    for (int r = 0; r < 20; ++r) t += sm[r * 136 + tid];
    cs[blk * 136 + tid] = t;
  }
}

__global__ __launch_bounds__(384) void k1_qkv(const char* x, const char* wqkv, const char* wo, const char* w1, const char* w2,
                                              const char* w3, const char* w4, char* ws) {
  extern __shared__ __attribute__((aligned(16))) char smem[];
  const int b = blockIdx.x, xcd = b & 7, j = b >> 3, r = j & 3, c = j >> 2;
  {
    const u32x4 e = {EMPTY, EMPTY, EMPTY, EMPTY};
    const int t = threadIdx.x, bt = b >> 4;
    *(u32x4*)(ws + WS_O + b * 10240 + t * 16) = e;
    if (t < 256) *(u32x4*)(ws + WS_O + b * 10240 + 6144 + t * 16) = e;
    else if (t < 336) *(u32x4*)(ws + WS_G + ((bt >> 1) * 16 + (bt & 1) * 15) * 20480 + (b & 15) * 1280 + (t - 256) * 16) = e;
  }
  if ((xcd & 1) && c == 7) { colsum_job(smem, w1, w3, (float*)(ws + WS_CS), (xcd >> 1) * 4 + r); return; }
  const int rt = (xcd >> 1) * 4 + r, ct = (xcd & 1) * 8 + c;
  const int wave = __builtin_amdgcn_readfirstlane(threadIdx.x >> 6);
  const WJob job = make_job((rt * 15 + ct) * 2 + (wave & 1), wo, w1, w2, w3, w4, ws);
  const int k0 = (3 * r + 4 * c) % 10;
  if (ct >= 10) qkv_body<true>(smem, x, wqkv, ws, rt, ct, k0, job);
  else qkv_body<false>(smem, x, wqkv, ws, rt, ct, k0, job);
}

typedef __attribute__((ext_vector_type(2))) float f32x2;
typedef unsigned long long u64;
DEV f32x2 unpk(unsigned u) { return f32x2{bf_lo(u), bf_hi(u)}; }
DEV unsigned pk2v(f32x2 v) { return pk2(v[0], v[1]); }
DEV f32x2 bfr2(f32x2 v) { return unpk(pk2v(v)); }
DEV f32x2 gelu2(f32x2 x) {
  const float c0 = -2.0f * 0.7978845608028654f * 1.4426950408889634f, c1 = c0 * 0.044715f;
  const f32x2 z = x * (x * x * c1 + c0);
  const f32x2 d = f32x2{__builtin_amdgcn_exp2f(z[0]), __builtin_amdgcn_exp2f(z[1])} + 1.0f;
  return x * f32x2{__builtin_amdgcn_rcpf(d[0]), __builtin_amdgcn_rcpf(d[1])};
}
DEV f32x2 lo2(f32x4 v) { return f32x2{v[0], v[1]}; }
DEV f32x2 hi2(f32x4 v) { return f32x2{v[2], v[3]}; }

typedef __amdgpu_buffer_rsrc_t rsrc_t;
constexpr unsigned OOB = 0x40000000u;
DEV unsigned full(unsigned v) { return v < EMPTY ? v : EMPTY - 1; }
DEV void st8(rsrc_t r, unsigned v, unsigned s, u32x2 d) { __builtin_amdgcn_raw_buffer_store_b64(u32x2{full(d[0]), full(d[1])}, r, v, s, 16); }
template <int AUX> DEV void st16r(rsrc_t r, unsigned v, unsigned s, u32x4 d) {
  asm("" : "+v"(d));
  __builtin_amdgcn_raw_buffer_store_b128(d, r, v, s, AUX);
  asm volatile("s_nop 1" ::"v"(d) : "memory");
}
DEV void st16(rsrc_t r, unsigned v, unsigned s, u32x4 d) { st16r<16>(r, v, s, u32x4{full(d[0]), full(d[1]), full(d[2]), full(d[3])}); }
DEV void st_empty(rsrc_t r, unsigned v, unsigned s) { st16r<16>(r, v, s, u32x4{EMPTY, EMPTY, EMPTY, EMPTY}); }
DEV u32x4 ld16(rsrc_t r, unsigned v, unsigned s) { return __builtin_amdgcn_raw_buffer_load_b128(r, v, s, 16); }
DEV u32x2 ld8(rsrc_t r, unsigned v, unsigned s) { return __builtin_amdgcn_raw_buffer_load_b64(r, v, s, 16); }
DEV u32x4 ld16p(rsrc_t r, unsigned v, unsigned s) { return __builtin_amdgcn_raw_buffer_load_b128(r, v, s, 0); }
DEV void vm0() { __builtin_amdgcn_s_waitcnt(0x0f70); cfence(); }

constexpr int LDS_TOT = 163840;
constexpr int AT_Q = 0, AT_K = AT_Q + 16 * 768, AT_V = AT_K + 32 * 768;
constexpr int SLOT = 25600, SLOT7 = 43008;
constexpr int L_W = 51200, L_W2 = 57344;
constexpr int L_SX = 159744;
constexpr int L_SY = 161792;
constexpr int C_P = L_SY, C_S = L_SY + 128, C_F = L_SY + 256, C_W2 = L_SY + 384, C_W4 = L_SY + 388, C_WC = L_SY + 392, C_OK = L_SY + 396, C_WL = L_SY + 400;
constexpr int L_LN = L_SY + 512;
constexpr int L_DUM = L_SY + 1024;
DEV void touch(const char* p, int l, unsigned lb) {
  asm volatile("s_mov_b32 m0, %0\n\ts_nop 0\n\tglobal_load_lds_dword %1, %2" ::"s"(lb + L_DUM + 512), "v"(l * 128), "s"(p) : "memory");
}
DEV void lds_inc(unsigned a) {
  asm volatile("s_mov_b64 exec, 1\n\tds_add_u32 %0, %1\n\ts_mov_b64 exec, -1" ::"v"(a), "v"(1u) : "memory");
}
DEV void lds_wait(unsigned a, unsigned target) {
  for (int it = 0; it < (1 << 22); ++it) {
    const unsigned v = __builtin_amdgcn_readfirstlane(*(volatile LAS unsigned*)(uintptr_t)a);
    if (v >= target) break;
  }
  cfence();
}
DEV f32x4 lds_ld4(unsigned a) { return *(const LAS f32x4*)(uintptr_t)a; }
DEV unsigned max3u(unsigned a, unsigned b, unsigned c) { a = a > b ? a : b; return a > c ? a : c; }

template <int NT, int KW, int NTILE, int MODE, bool UNR, class TS, class TV, class TG, class TL, class TH>
DEV void m_phase(const bf16x8 (&W)[30], int l, unsigned lb, rsrc_t wr, int n0, int part, TS&& so, TV&& vo, TG&& guard, TL&& slot,
                 TH&& hook STARG) {
  constexpr int RS = NT * 64 + 16;
  const int g = l >> 4, li = l & 15;
  const unsigned pw = lb + part * (16 * RS) + li * RS + g * 16;
  const unsigned sx = g == (li >> 2) ? lb + L_SX + li * 32 + part * 8 : lb + L_DUM + l * 8 - 1536;
  u32x4 a[2][KW];
  auto issue = [&](auto i, auto bi) {
    constexpr int B = bi.value;
    const unsigned s = so(i), v = vo(i);
#pragma unroll
    for (int k = 0; k < (MODE == 1 ? KW - 1 : KW); ++k) a[B][k] = ld16(wr, v + k * 1024, s);
    if constexpr (MODE == 1) {
      const u32x2 t0 = ld8(wr, g * 256 + li * 8 + 4096, s), t1 = ld8(wr, g * 256 + li * 8 + 4096 + 128, s);
      a[B][KW - 1] = u32x4{t0[0], t0[1], t1[0], t1[1]};
    }
    cfence();
  };
  auto step = [&](auto i, auto bi) {
    constexpr int B = bi.value;
    const int n = n0 + i;
#pragma unroll 1
    for (int it = 0;; ++it) {
      vm0();
      unsigned m = 0;
#pragma unroll
      for (int k = 0; k < KW; ++k) m = max3u(max3u(m, a[B][k][0], a[B][k][1]), a[B][k][2], a[B][k][3]);
      if (__builtin_amdgcn_ballot_w64(m == EMPTY) == 0 || it >= (1 << 16)) break;
      issue(i, bi);
    }
    if (i == 0) { ST(STI); }
    const f32x4 z4 = {0.f, 0.f, 0.f, 0.f};
    f32x4 sa = z4, qa = z4;
    if constexpr (MODE == 2) {
      const u32x4 one = {0x3F803F80u, 0x3F803F80u, 0x3F803F80u, 0x3F803F80u};
#pragma unroll
      for (int k = 0; k < KW; ++k) {
        const bf16x8 f = __builtin_bit_cast(bf16x8, a[B][k]);
        qa = mfma16(f, f, qa); sa = mfma16(__builtin_bit_cast(bf16x8, one), f, sa);
      }
      __builtin_amdgcn_sched_barrier(0);
    }
    if (i + 1 < NTILE) issue(i + 1, std::integral_constant<int, B ^ 1>{});
    const unsigned ga = guard(i);
    const unsigned gv = *(volatile LAS unsigned*)(uintptr_t)ga;
    __builtin_amdgcn_sched_barrier(0);
    f32x4 acc[NT];
#pragma unroll
    for (int j = 0; j < NT; ++j) acc[j] = mfma16(W[j], __builtin_bit_cast(bf16x8, a[B][0]), z4);
    if constexpr (MODE == 2) {
      const float q01 = (li & 1) ? qa[1] : qa[0], q23 = (li & 1) ? qa[3] : qa[2];
      *(LAS f32x2*)(uintptr_t)(sx + (g == (li >> 2) ? (n & 3) * 512 : 1536)) = f32x2{sa[0], (li & 2) ? q23 : q01};
      lds_inc(lb + C_S + n * 4);
      __builtin_amdgcn_sched_barrier(0);
    }
#pragma unroll
    for (int k = 1; k < KW; ++k)
#pragma unroll
      for (int j = 0; j < NT; ++j) acc[j] = mfma16(W[k * NT + j], __builtin_bit_cast(bf16x8, a[B][k]), acc[j]);
    __builtin_amdgcn_sched_barrier(0);
    if (__builtin_amdgcn_readfirstlane(gv) < 4u) lds_wait(ga, 4);
    const unsigned sl = slot(i);
#pragma unroll
    for (int j = 0; j < NT; ++j) *(LAS f32x4*)(uintptr_t)(pw + sl + j * 64) = acc[j];
    lds_inc(lb + C_P + n * 4);
    hook(i);
  };
  issue(0, std::integral_constant<int, 0>{});
  if constexpr (UNR) {
    static_for<0, NTILE>([&](auto i) { step(i, std::integral_constant<int, i.value & 1>{}); });
  } else {
#pragma unroll 1
    for (int i = 0; i < NTILE; i += 2) {
      step(i, std::integral_constant<int, 0>{});
      step(i + 1, std::integral_constant<int, 1>{});
    }
  }
  ST(STI + 1);
}

template <int NP, int RS>
DEV void e_sum(f32x4& lo, f32x4& hi, unsigned lb, unsigned a, int n) {
  lds_wait(lb + C_P + n * 4, NP);
  f32x4 p[NP][2];
#pragma unroll
  for (int q = 0; q < NP; ++q) { p[q][0] = lds_ld4(a + q * (16 * RS)); p[q][1] = lds_ld4(a + q * (16 * RS) + 16); }
  lds_inc(lb + C_F + n * 4);
#pragma unroll
  for (int s = 1; s < NP; s *= 2)
#pragma unroll
    for (int q = 0; q < NP; q += 2 * s) { p[q][0] += p[q + s][0]; p[q][1] += p[q + s][1]; }
  lo = p[0][0]; hi = p[0][1];
}
DEV void e_stats(float& sum, float& sq, unsigned lb, int n, int li) {
  lds_wait(lb + C_S + n * 4, 4);
  const f32x4 a = lds_ld4(lb + L_SX + (n & 3) * 512 + li * 32), b = lds_ld4(lb + L_SX + (n & 3) * 512 + li * 32 + 16);
  sum = (a[0] + a[2]) + (b[0] + b[2]); sq = (a[1] + a[3]) + (b[1] + b[3]);
}

template <int I>
DEV void attention(char* smem, int xcd, int w, u32x2& om, u32x2& ot) {
  const int l = threadIdx.x & 63, g = l >> 4, li = l & 15;
  u32x4 qv = *(const u32x4*)(smem + AT_Q + (2 * w + I) * 768 + (g < 3 ? g : 2) * 256 + li * 16);
  if (g == 3) qv = u32x4{0u, 0u, 0u, 0u};
  const bf16x8 qf = __builtin_bit_cast(bf16x8, qv);
  const char* Ks = smem + AT_K + (2 * w + I) * 768 + (g < 3 ? g : 2) * 256 + li * 16;
  const int kbase = xcd * 256 + 32 * w + 16 * I - 256;
  f32x4 s0[17];
  const f32x4 z4 = {0.f, 0.f, 0.f, 0.f};
#pragma unroll
  for (int c = 0; c < 17; ++c) s0[c] = mfma16(*(const bf16x8*)(Ks + c * 768), qf, z4);
  const float NEG = -__builtin_inff();
#pragma unroll
  for (int r = 0; r < 4; ++r) {
    const int kj = 4 * g + r;
    if (kj <= li) s0[0][r] = NEG;
    if (kj > li) s0[16][r] = NEG;
  }
  if (kbase < 0) {
#pragma unroll
    for (int c = 0; c < 17; ++c)
      if (kbase + 16 * c < 0) s0[c] = f32x4{NEG, NEG, NEG, NEG};
  }
  float m0 = NEG, m1 = NEG;
#pragma unroll
  for (int c = 0; c < 17; ++c) {
    m0 = fmaxf(fmaxf(m0, s0[c][0]), s0[c][1]);
    m1 = fmaxf(fmaxf(m1, s0[c][2]), s0[c][3]);
  }
  m0 = max_g(fmaxf(m0, m1));
  const float C2 = 0.22360679774997896f * 1.4426950408889634f;
  const float b0 = -m0 * C2;
  u32x2 p0[18];
#pragma unroll
  for (int c = 0; c < 17; ++c) {
    const f32x4 e = s0[c] * C2 + b0;
    p0[c + I] = u32x2{pk2(__builtin_amdgcn_exp2f(e[0]), __builtin_amdgcn_exp2f(e[1])), pk2(__builtin_amdgcn_exp2f(e[2]), __builtin_amdgcn_exp2f(e[3]))};
  }
  p0[I ? 0 : 17] = u32x2{0u, 0u};
  const char* Vs0 = smem + AT_V + w * 1280 + (g * 16 + li) * 16;
  const char* Vs1 = smem + AT_V + w * 1280 + 1024 + (g * 4 + (li & 3)) * 16;
  f32x4 o0 = z4, o1 = z4;
#pragma unroll
  for (int s = 0; s < 9; ++s) {
    bf16x8 v0 = *(const bf16x8*)(Vs0 + s * 1280);
    u32x4 v1u = *(const u32x4*)(Vs1 + s * 1280);
    if (li == 4) v1u = u32x4{0x3F803F80u, 0x3F803F80u, 0x3F803F80u, 0x3F803F80u};
    const u32x4 pa = {p0[2 * s][0], p0[2 * s][1], p0[2 * s + 1][0], p0[2 * s + 1][1]};
    const bf16x8 pf = __builtin_bit_cast(bf16x8, pa);
    o0 = mfma16(v0, pf, o0); o1 = mfma16(__builtin_bit_cast(bf16x8, v1u), pf, o1);
  }
  const float r0 = 1.0f / __shfl(o1[0], 16 + li);
  om = u32x2{pk2(o0[0] * r0, o0[1] * r0), pk2(o0[2] * r0, o0[3] * r0)};
  ot = u32x2{pk2(o1[0] * r0, o1[1] * r0), pk2(o1[2] * r0, o1[3] * r0)};
}

#define CTX \
  int tid_ = threadIdx.x, b_ = blockIdx.x; \
  asm volatile("" : "+v"(tid_)); asm volatile("" : "+s"(b_)); \
  const int xcd = b_ & 7, jj = b_ >> 3, l = tid_ & 63, g = l >> 4, li = l & 15; \
  const int w = __builtin_amdgcn_readfirstlane(tid_ >> 6), q = w & 3, grp = w >> 2; \
  const int ct = jj & 7, ct16 = jj & 15; \
  const unsigned vl = l * 16; \
  const int G4 = (xcd * 4 + (jj >> 3)) * 4, dir = (jj >> 3) & 1; \
  const int B8 = (xcd * 2 + (jj >> 4)) * 8; \
  auto g4 = [&](int i) { return G4 + (dir ? 3 - i : i); }; \
  auto g8 = [&](int i) { return B8 + ((i & 1) ? 7 - (i >> 1) : (i >> 1)); }; \
  auto lane = [&](int) { return vl; }; \
  auto ring = [&](int n) { return lb + (n >= 2 ? C_F + (n - 2) * 4 : C_OK); }; \
  auto slot2 = [&](int i) { return (unsigned)((i & 1) * SLOT); }; \
  const int wid = jj * 4 + q; \
  auto nohook = [](int) {}; \
  const int nu = q < 2 ? 3 : 2, un = (q < 2 ? 3 * q : 2 * q + 2) + (g < nu ? g : nu - 1); \
  const bool act = g < nu; \
  const int ko = ct * 10 + un; \
  const unsigned vft = (ko >> 2) * 1024 + (ko & 3) * 256 + li * 16, vst = act ? vft : OOB; \
  const unsigned pe = lb + li * 336 + un * 32; \
  const unsigned xo = li * (D_ * 2) + ko * 16; \
  const int k6 = ct16 * 12 + 3 * q + (g < 3 ? g : 2); \
  const unsigned v6 = g < 3 ? (unsigned)((k6 >> 2) * 1024 + (k6 & 3) * 256 + li * 16) : OOB; \
  const unsigned w4b = (unsigned)WS_W4 + ct * (5 * 48 * 1024) + w * 6144; \
  (void)0

__global__ __launch_bounds__(512) void kp_main(const char* x, char* ws, char* out) {
  extern __shared__ __attribute__((aligned(16))) char smem[];
  STDECL;
  STR(0); ST(1);
  const unsigned lb = lds_addr(smem);
  const rsrc_t wr = __builtin_amdgcn_make_buffer_rsrc(ws, 0, (int)WS_END, 0x00020000);
  bf16x8 W[30];
  int ga, gb;
  {
    CTX;
#pragma unroll
    for (int i = 0; i < 4; ++i) {
      st_empty(wr, vst, (unsigned)(grp ? WS_H : WS_G) + (G4 + i) * 20480);
      st_empty(wr, grp ? vst : OOB, (unsigned)WS_X1 + (G4 + i) * 20480);
    }
    const char* Qh = ws + WS_Q + (size_t)jj * QK_HEAD + xcd * 16 * 768;
    const char* Kh = ws + WS_K + (size_t)jj * QK_HEAD;
    const char* Vh = ws + WS_V + (size_t)jj * V_HEAD;
    const int koff0 = (xcd * 16 - 16) * 768, voff0 = (xcd * 8 - 8) * 1280;
#pragma unroll
    for (int i = 0; i < 7; ++i) {
      const int p = w + 8 * i;
      const char* src;
      if (p < 12) src = Qh + p * 1024;
      else if (p < 36) { const int o = koff0 + (p - 12) * 1024; src = Kh + (o < 0 ? 0 : o); }
      else { const int o = voff0 + (p - 36) * 1024; src = Vh + (o < 0 ? 0 : o); }
      glds16(src, vl, lb + AT_Q + p * 1024);
    }
    *(LAS unsigned*)(uintptr_t)(lb + L_SY + (tid_ & 127) * 4) = (tid_ & 127) == (C_OK - L_SY) / 4 ? 4u : 0u;
    __builtin_amdgcn_s_barrier();
    const int tu = jj * 8 + w;
    touch(ws + WS_WO + (tu < 200 ? tu : 199) * 8192, l, lb);
    wait_vm<1>();
    __builtin_amdgcn_s_barrier();
    cfence();
    ST(2);
    u32x2 om0, ot0, om1, ot1;
    attention<0>(smem, xcd, w, om0, ot0);
    __builtin_amdgcn_sched_barrier(0);
    const unsigned wb = (unsigned)WS_WO + grp * (D_ * D_ * 2) + ct * (5 * 20 * 1024) + q * 5120;
#pragma unroll
    for (int k = 0; k < 5; ++k)
#pragma unroll
      for (int j = 0; j < 5; ++j) W[k * 5 + j] = __builtin_bit_cast(bf16x8, ld16p(wr, vl + k * 1024, wb + j * (20 * 1024)));
    if (grp && wid < 100) touch(ws + WS_W2 + wid * 8192, l, lb);
    __builtin_amdgcn_sched_barrier(0);
    attention<1>(smem, xcd, w, om1, ot1);
    {
      const auto s0 = __builtin_amdgcn_permlane16_swap(om0[0], om1[0], false, false);
      const auto s1 = __builtin_amdgcn_permlane16_swap(om0[1], om1[1], false, false);
      const auto t0 = __builtin_amdgcn_permlane16_swap(ot0[0], ot1[0], false, false);
      const auto t1 = __builtin_amdgcn_permlane16_swap(ot0[1], ot1[1], false, false);
      const unsigned Of = (unsigned)WS_O + (xcd * 16 + 2 * w) * 20480 + (jj >> 3) * 5120;
      st16(wr, (g & 1) * 20480 + (g >> 1) * 256 + li * 16, Of + (jj & 7) * 512, u32x4{s0[0], s1[0], s0[1], s1[1]});
      st8(wr, g < 2 ? g * 20480 + li * 8 : OOB, Of + 4096 + (jj & 7) * 128, u32x2{t0[0], t1[0]});
    }
    ST(3);
    __builtin_amdgcn_s_barrier();
    cfence();
    ga = grp; gb = grp;
    asm volatile("" : "+s"(ga), "+s"(gb));
  }
  if (ga == 0) {
    CTX;
#pragma unroll
    for (int i = 0; i < 8; ++i) st_empty(wr, v6, (unsigned)WS_U + (B8 + i) * 49152);
    m_phase<5, 5, 4, 1, false>(W, l, lb, wr, 0, q, [&](int i) { return (unsigned)WS_O + g4(i) * 20480 + q * 5120; }, lane, ring, slot2, nohook STPASS(4));
    lds_wait(lb + C_WL, 4);
#pragma unroll
    for (int k = 0; k < 5; ++k)
#pragma unroll
      for (int j = 0; j < 5; ++j) W[k * 5 + j] = *(const LAS bf16x8*)(uintptr_t)(lb + L_W2 + (q * 25 + k * 5 + j) * 1024 + vl);
    lds_inc(lb + C_W2);
    f32x4 cs[2];
#pragma unroll
    for (int i = 0; i < 2; ++i) cs[i] = __builtin_bit_cast(f32x4, ld16p(wr, ko * 32 + i * 16, (unsigned)WS_CS));
    lds_wait(lb + C_W2, 4);
    auto stage4 = [&](int f) { glds16(ws + w4b + 4 * 6144 + (f % 5) * (48 * 1024) + (f / 5) * 1024, vl, lb + L_W + (q * 26 + f) * 1024); };
#pragma unroll
    for (int f = 0; f < 8; ++f) stage4(f);
#pragma unroll 1
    for (int i = 0; i < 4; ++i) {
      const int n = 4 + i;
      float sum, sq, mean, var, m1, s1;
      e_stats(sum, sq, lb, n, li);
      ln_stats(sum, sq, mean, var, m1, s1);
      const float r1 = 1.0f / s1;
      const float m2 = bfr((mean - m1) * r1), v2 = bfr(var * r1 * r1);
      const float s2 = bfr(sqrtf(bfr(v2 + 1e-5f)));
      float rr = r1 / s2;
      const float nm = -(m1 + m2 * s1) * rr;
      if (w == 0) *(LAS f32x2*)(uintptr_t)(lb + L_LN + (i * 16 + li) * 8) = f32x2{m1, r1};
      f32x4 c0 = cs[0] * nm, c1 = cs[1] * nm;
      asm volatile("" : "+v"(rr), "+v"(c0), "+v"(c1));
      f32x4 lo, hi;
      e_sum<4, 336>(lo, hi, lb, pe + (n & 1) * SLOT, n);
      if (i == 0) { ST(6); }
      lo = lo * rr + c0; hi = hi * rr + c1;
      st16(wr, vst, (unsigned)WS_G + g4(i) * 20480,
           u32x4{pk2v(gelu2(bfr2(lo2(lo)))), pk2v(gelu2(bfr2(hi2(lo)))), pk2v(gelu2(bfr2(lo2(hi)))), pk2v(gelu2(bfr2(hi2(hi))))});
      cfence();
      if (i < 3) {
#pragma unroll
        for (int f = 0; f < 6; ++f) stage4(8 + 6 * i + f);
        if (i < 2 && i * 128 + wid < 240) touch(ws + WS_W3 + (i * 128 + wid) * 8192, l, lb);
      }
    }
    ST(7);
    {
      const int sr = li - ((q & 2) ? ((q & 1) ? -2 : 2) : ((q & 1) ? -1 : 1));
      const unsigned vg = (unsigned)(((sr >> 4) + 1) * 20480 + g * 256 + (sr & 15) * 16);
      m_phase<5, 5, 4, 0, false>(W, l, lb, wr, 8, q, [&](int i) { return (unsigned)WS_G + (g4(i) - 1) * 20480 + q * 5120; },
          [&](int i) { return (unsigned)(g4(i) * 16 + sr) < (unsigned)S_ ? vg : OOB; },
          [&](int i) { return ring(8 + i); }, slot2, [&](int i) { if (i == 0) lds_inc(lb + C_W4); } STPASS(8));
    }
    auto load4 = [&](auto kc) {
#pragma unroll
      for (int k = kc.value; k < kc.value + 2; ++k)
#pragma unroll
        for (int j = 0; j < 5; ++j) W[k * 5 + j] = __builtin_bit_cast(bf16x8, ld16p(wr, vl + k * 1024, w4b + j * (48 * 1024)));
      cfence();
    };
    {
      const unsigned p6 = lb + li * 400 + (3 * q + (g < 3 ? g : 2)) * 32;
#pragma unroll
      for (int i = 0; i < 2; ++i) cs[i] = __builtin_bit_cast(f32x4, ld16p(wr, k6 * 32 + i * 16, (unsigned)WS_CS + D_ * 4));
#pragma unroll 1
      for (int i = 0; i < 8; ++i) {
        const int n = 12 + i;
        float sum, sq, mean, var, m, s;
        e_stats(sum, sq, lb, n, li);
        ln_stats(sum, sq, mean, var, m, s);
        float rinv = 1.0f / s;
        const float nm = -m * rinv;
        f32x4 c0 = cs[0] * nm, c1 = cs[1] * nm;
        asm volatile("" : "+v"(rinv), "+v"(c0), "+v"(c1));
        f32x4 lo, hi;
        e_sum<4, 400>(lo, hi, lb, p6 + (n & 1) * SLOT, n);
        if (i == 0) { ST(10); }
        lo = lo * rinv + c0; hi = hi * rinv + c1;
        st16(wr, v6, (unsigned)WS_U + g8(i) * 49152,
             u32x4{pk2v(gelu2(bfr2(lo2(lo)))), pk2v(gelu2(bfr2(hi2(lo)))), pk2v(gelu2(bfr2(lo2(hi)))), pk2v(gelu2(bfr2(hi2(hi))))});
        cfence();
        if (i == 0) load4(std::integral_constant<int, 0>{});
        if (i == 2) load4(std::integral_constant<int, 2>{});
        if (i == 4) load4(std::integral_constant<int, 4>{});
      }
      ST(11);
    }
  }
  if (gb != 0) {
    CTX;
    const rsrc_t xr = __builtin_amdgcn_make_buffer_rsrc((void*)x, 0, S_ * D_ * 2, 0x00020000);
    {
      u32x4 xv[4];
#pragma unroll
      for (int i = 0; i < 4; ++i) xv[i] = ld16p(xr, xo, g4(i) * (16 * D_ * 2));
      cfence();
      const char* W2 = ws + WS_W2 + (size_t)ct * (5 * 20 * 1024) + q * 5120;
      auto stage2 = [&](int f) { glds16(W2 + (f % 5) * (20 * 1024) + (f / 5) * 1024, vl, lb + L_W2 + (q * 25 + f) * 1024); };
#pragma unroll
      for (int f = 0; f < 9; ++f) stage2(f);
#pragma unroll
      for (int i = 0; i < 4; ++i) {
        f32x4 lo, hi;
        e_sum<4, 336>(lo, hi, lb, pe + (i & 1) * SLOT, i);
        if (i == 0) { ST(4); }
        st16(wr, vst, (unsigned)WS_H + g4(i) * 20480,
             u32x4{pk2v(unpk(xv[i][0]) + bfr2(lo2(lo))), pk2v(unpk(xv[i][1]) + bfr2(hi2(lo))),
                   pk2v(unpk(xv[i][2]) + bfr2(lo2(hi))), pk2v(unpk(xv[i][3]) + bfr2(hi2(hi)))});
        cfence();
        if (i < 2) {
#pragma unroll
          for (int f = 0; f < 8; ++f) stage2(9 + 8 * i + f);
        }
        if (i == 1) { const int u = wid < 120 ? wid : 119; touch(ws + WS_W4 + (u / 3) * 49152 + 24576 + (u % 3) * 8192, l, lb); }
        if (i == 2) { wait_vm<2>(); lds_inc(lb + C_WL); }
      }
      ST(5);
    }
    m_phase<5, 5, 4, 2, false>(W, l, lb, wr, 4, q, [&](int i) { return (unsigned)WS_H + g4(i) * 20480 + q * 5120; }, lane,
        [&](int i) { return ring(4 + i); }, slot2, nohook STPASS(6));
    {
      u32x4 hv[4];
#pragma unroll
      for (int i = 0; i < 4; ++i) hv[i] = ld16(wr, vft, (unsigned)WS_H + g4(i) * 20480);
      cfence();
      const unsigned w3b = (unsigned)WS_W3 + ct16 * (6 * 20 * 1024) + q * 5120;
      auto load3 = [&](int f) { W[f] = __builtin_bit_cast(bf16x8, ld16p(wr, vl + (f / 6) * 1024, w3b + (f % 6) * (20 * 1024))); };
#pragma unroll
      for (int f = 0; f < 9; ++f) load3(f);
      cfence();
#pragma unroll
      for (int i = 0; i < 4; ++i) {
        const int n = 8 + i;
        f32x4 lo, hi;
        e_sum<4, 336>(lo, hi, lb, pe + (n & 1) * SLOT, n);
        if (i == 0) { ST(8); }
        const f32x2 mr = *(const LAS f32x2*)(uintptr_t)(lb + L_LN + (i * 16 + li) * 8);
        const float m1 = mr[0], r1 = mr[1];
        st16(wr, vst, (unsigned)WS_X1 + g4(i) * 20480,
             u32x4{pk2v(bfr2(bfr2(unpk(hv[i][0]) - m1) * r1) + bfr2(lo2(lo))), pk2v(bfr2(bfr2(unpk(hv[i][1]) - m1) * r1) + bfr2(hi2(lo))),
                   pk2v(bfr2(bfr2(unpk(hv[i][2]) - m1) * r1) + bfr2(lo2(hi))), pk2v(bfr2(bfr2(unpk(hv[i][3]) - m1) * r1) + bfr2(hi2(hi)))});
        cfence();
        if (i < 3) {
#pragma unroll
          for (int f = 0; f < 7; ++f) load3(9 + 7 * i + f);
        }
        if (i == 0 && wid < 120) touch(ws + WS_W4 + (wid / 3) * 49152 + (wid % 3) * 8192, l, lb);
        cfence();
      }
      ST(9);
    }
    m_phase<6, 5, 8, 2, false>(W, l, lb, wr, 12, q, [&](int i) { return (unsigned)WS_X1 + g8(i) * 20480 + q * 5120; }, lane,
        [&](int i) { return ring(12 + i); }, slot2, nohook STPASS(10));
#pragma unroll
    for (int j = 1; j < 5; ++j) W[25 + j] = __builtin_bit_cast(bf16x8, ld16p(wr, vl + 5 * 1024, w4b + j * (48 * 1024)));
    lds_wait(lb + C_W4, 4);
#pragma unroll
    for (int f = 0; f < 26; ++f) W[f] = *(const LAS bf16x8*)(uintptr_t)(lb + L_W + (q * 26 + f) * 1024 + vl);
    lds_inc(lb + C_WC);
  }
  {
    CTX;
    const rsrc_t orr = __builtin_amdgcn_make_buffer_rsrc(out, 0, S_ * D_ * 2, 0x00020000);
    if (grp) { lds_wait(lb + C_P + 19 * 4, 4); }
    u32x4 hv[2], xv[2];
#pragma unroll
    for (int i = 0; i < 2; ++i) {
      const unsigned to = g4(2 * i + grp) * 20480;
      hv[i] = ld16(wr, vft, (unsigned)WS_H + to); xv[i] = ld16(wr, vft, (unsigned)WS_X1 + to);
    }
    auto fin = [&](int p, auto si) {
      constexpr int s = si.value;
      f32x4 lo, hi;
      e_sum<8, 336>(lo, hi, lb, pe + (p % 3) * SLOT7, 20 + p);
      const u32x4 y = {pk2v(unpk(hv[s][0]) + bfr2(unpk(xv[s][0]) + bfr2(lo2(lo)))), pk2v(unpk(hv[s][1]) + bfr2(unpk(xv[s][1]) + bfr2(hi2(lo)))),
                       pk2v(unpk(hv[s][2]) + bfr2(unpk(xv[s][2]) + bfr2(lo2(hi)))), pk2v(unpk(hv[s][3]) + bfr2(unpk(xv[s][3]) + bfr2(hi2(hi))))};
      st16r<0>(orr, act ? xo : OOB, g4(p) * (16 * D_ * 2), y);
    };
    m_phase<5, 6, 4, 0, true>(W, l, lb, wr, 20, w, [&](int i) { return (unsigned)WS_U + g4(i) * 49152 + w * 6144; }, lane,
        [&](auto i) { return lb + (i.value == 0 ? C_F + 19 * 4 : i.value == 1 ? C_WC : i.value == 2 ? C_OK : C_F + 20 * 4); },
        [&](auto i) { return (unsigned)((i.value % 3) * SLOT7); },
        [&](auto i) {
          if constexpr (i.value == 1) { if (grp == 0) fin(0, std::integral_constant<int, 0>{}); }
          if constexpr (i.value == 2) { if (grp == 1) fin(1, std::integral_constant<int, 0>{}); }
          if constexpr (i.value == 3) fin(2 + grp, std::integral_constant<int, 1>{});
        } STPASS(12));
    STDUMP2(ws, 2 + grp);
  }
}

extern "C" void kernel_launch(void* const* d_in, const int* in_sizes, int n_in, void* d_out, int out_size,
                              void* d_ws, size_t ws_size, hipStream_t stream) {
  const char* x = (const char*)d_in[0];
  const char* wqkv = (const char*)d_in[1];
  const char* wo = (const char*)d_in[2];
  const char* w1 = (const char*)d_in[4];
  const char* w2 = (const char*)d_in[5];
  const char* w3 = (const char*)d_in[6];
  const char* w4 = (const char*)d_in[7];
  char* ws = (char*)d_ws;
  if (ws_size < WS_END) return;
  constexpr int LQ = 4 * 32768 + 2048;
  (void)hipFuncSetAttribute((const void*)k1_qkv, hipFuncAttributeMaxDynamicSharedMemorySize, LQ);
  (void)hipFuncSetAttribute((const void*)kp_main, hipFuncAttributeMaxDynamicSharedMemorySize, LDS_TOT);
  k1_qkv<<<256, 384, LQ, stream>>>(x, wqkv, wo, w1, w2, w3, w4, ws);
  kp_main<<<256, 512, LDS_TOT, stream>>>(x, ws, (char*)d_out);
}
```

```cpp
#include <hip/hip_runtime.h>
#include <hip/hip_bf16.h>
#include <stdint.h>
#include <type_traits>

typedef __attribute__((ext_vector_type(8))) __bf16 bf16x8;
typedef __attribute__((ext_vector_type(4))) __bf16 bf16x4;
typedef __attribute__((ext_vector_type(2))) __bf16 bf16x2;
typedef __attribute__((ext_vector_type(4))) short s16x4;
typedef __attribute__((ext_vector_type(4))) float f32x4;
typedef __attribute__((ext_vector_type(2))) unsigned u32x2;
typedef __attribute__((ext_vector_type(4))) unsigned u32x4;
#define LAS __attribute__((address_space(3)))
#define DEV __device__ __forceinline__

constexpr int S_ = 2048, D_ = 640, F_ = 1536, H_ = 32;

constexpr size_t QK_HEAD = 128 * 768;
constexpr size_t V_HEAD  = 64 * 1280;
constexpr size_t WS_Q  = 0;
constexpr size_t WS_K  = WS_Q + H_ * QK_HEAD;
constexpr size_t WS_V  = WS_K + H_ * QK_HEAD;
constexpr size_t WS_O  = WS_V + H_ * V_HEAD;
constexpr size_t WS_H  = WS_O + (size_t)S_ * D_ * 2;
constexpr size_t WS_G  = WS_H + (size_t)S_ * D_ * 2;
constexpr size_t WS_X1 = WS_G + (size_t)S_ * D_ * 2;
constexpr size_t WS_U  = WS_X1 + (size_t)S_ * D_ * 2;
constexpr size_t WS_CS = WS_U + (size_t)S_ * F_ * 2;
constexpr size_t WS_WO = WS_CS + 16384;
constexpr size_t WS_W1 = WS_WO + (size_t)D_ * D_ * 2;
constexpr size_t WS_W2 = WS_W1 + (size_t)D_ * D_ * 2;
constexpr size_t WS_W3 = WS_W2 + (size_t)D_ * D_ * 2;
constexpr size_t WS_W4 = WS_W3 + (size_t)D_ * F_ * 2;
constexpr size_t WS_END = WS_W4 + (size_t)D_ * F_ * 2;
constexpr unsigned EMPTY = 0xffffffffu;

template <int I, int N, class Fn> DEV void static_for(Fn&& f) {
  if constexpr (I < N) { f(std::integral_constant<int, I>{}); static_for<I + 1, N>(f); }
}
template <int N> DEV void wait_vm() { asm volatile("s_waitcnt vmcnt(%0) lgkmcnt(0)" ::"n"(N) : "memory"); }
DEV void glds16(const char* sbase, unsigned voff, unsigned lds) {
  asm volatile("s_mov_b32 m0, %0\n\ts_nop 0\n\tglobal_load_lds_dwordx4 %1, %2" ::"s"(lds), "v"(voff), "s"(sbase) : "memory");
}
DEV unsigned lds_addr(const void* p) { return (unsigned)(uintptr_t)(LAS const void*)p; }
#define ST(i)
#define STR(i)
#define STDECL
#define STDUMP(o, ws, kid)
#define STARG
#define STPASS(i)
#define STDUMP2(ws, kid)
DEV void wait_lgkm0() { asm volatile("s_waitcnt lgkmcnt(0)" ::: "memory"); }
DEV void cfence() { asm volatile("" ::: "memory"); }
DEV float bfr(float x) { return (float)(__bf16)x; }
DEV unsigned pk2(float a, float b) { bf16x2 v = {(__bf16)a, (__bf16)b}; return __builtin_bit_cast(unsigned, v); }
DEV float bf_lo(unsigned u) { return __builtin_bit_cast(float, u << 16); }
DEV float bf_hi(unsigned u) { return __builtin_bit_cast(float, u & 0xffff0000u); }
DEV f32x4 mfma16(bf16x8 a, bf16x8 b, f32x4 c) { return __builtin_amdgcn_mfma_f32_16x16x32_bf16(a, b, c, 0, 0, 0); }
DEV unsigned ft_off(int row, int k, int KS) {
  return (unsigned)(((row >> 4) * KS + (k >> 5)) * 1024 + ((row & 15) + 16 * ((k >> 3) & 3)) * 16 + (k & 7) * 2);
}

DEV void ln_stats(float sum, float sq, float& mean, float& var, float& m, float& s) {
  mean = sum * (1.0f / D_);
  var = fmaxf(sq * (1.0f / D_) - mean * mean, 0.f);
  m = bfr(mean);
  s = bfr(sqrtf(bfr(bfr(var) + 1e-5f)));
}

DEV void dot_sum(float& s, bf16x8 f) {
  const bf16x2 one2 = {(__bf16)1.0f, (__bf16)1.0f};
#pragma unroll
  for (int e = 0; e < 4; ++e) { bf16x2 v = {f[2 * e], f[2 * e + 1]}; s = __builtin_amdgcn_fdot2_f32_bf16(v, one2, s, false); }
}
DEV void dot_sq(float& s, bf16x8 f) {
#pragma unroll
  for (int e = 0; e < 4; ++e) { bf16x2 v = {f[2 * e], f[2 * e + 1]}; s = __builtin_amdgcn_fdot2_f32_bf16(v, v, s, false); }
}
DEV unsigned f2u(float v) { return __builtin_bit_cast(unsigned, v); }
DEV float u2f(unsigned v) { return __builtin_bit_cast(float, v); }
DEV float red_g(float v) {
  const auto r = __builtin_amdgcn_permlane16_swap(f2u(v), f2u(v), false, false);
  v = u2f(r[0]) + u2f(r[1]);
  const auto s = __builtin_amdgcn_permlane32_swap(f2u(v), f2u(v), false, false);
  return u2f(s[0]) + u2f(s[1]);
}
DEV float max_g(float v) {
  const auto r = __builtin_amdgcn_permlane16_swap(f2u(v), f2u(v), false, false);
  v = fmaxf(u2f(r[0]), u2f(r[1]));
  const auto s = __builtin_amdgcn_permlane32_swap(f2u(v), f2u(v), false, false);
  return fmaxf(u2f(s[0]), u2f(s[1]));
}

DEV unsigned wrm_voff(int l, int q, int ldw) {
  return (unsigned)((l >> 4) * ldw + ((((l & 15) >> 1) ^ ((l >> 4) | (q << 2))) * 32) + (l & 1) * 16);
}
DEV bf16x8 wrm_frag(const char* p) {
  s16x4 lo = __builtin_amdgcn_ds_read_tr16_b64_v4i16((LAS s16x4*)(p));
  s16x4 hi = __builtin_amdgcn_ds_read_tr16_b64_v4i16((LAS s16x4*)(p + 1024));
  typedef __attribute__((ext_vector_type(8))) short s16x8;
  s16x8 v = __builtin_shufflevector(lo, hi, 0, 1, 2, 3, 4, 5, 6, 7);
  return __builtin_bit_cast(bf16x8, v);
}
DEV unsigned wrm_lane(int l, int jhi, int jj) {
  const int g = l >> 4, i = l & 15;
  return (unsigned)((8 * g + (i >> 2)) * 256 + ((((jhi ^ (g & 1)) << 2) | (jj ^ (i >> 2))) * 32) + (i & 3) * 8);
}

struct WJob { const char* src; char* dst; int ldw, wks, r4, perm; };
DEV WJob make_job(int id, const char* wo, const char* w1, const char* w2, const char* w3, const char* w4, char* ws) {
  const bool live = id < 390;
  const int b = live ? id : 389;
  const char* wsrc; char* wdst; int ldw = D_ * 2, wks = 20, kt, nt;
  if (b < 150) {
    const int mi = b / 50, u = b - mi * 50;
    wsrc = mi == 0 ? wo : (mi == 1 ? w1 : w2); wdst = ws + WS_WO + (size_t)mi * (D_ * D_ * 2);
    kt = u / 5; nt = u - kt * 5;
  } else if (b < 270) {
    const int u = b - 150; wsrc = w3; wdst = ws + WS_W3; ldw = F_ * 2; kt = u / 12; nt = u - kt * 12;
  } else {
    const int u = b - 270; wsrc = w4; wdst = ws + WS_W4; wks = 48; kt = u / 5; nt = u - kt * 5;
  }
  WJob j;
  j.src = wsrc + nt * 256;
  j.r4 = kt * 16; j.perm = b < 50;
  j.dst = live ? wdst + (size_t)(nt * 8 * wks + kt * 2) * 1024 : nullptr;
  j.ldw = ldw; j.wks = wks;
  return j;
}

template <bool SWAP>
struct GemmQ {
  f32x4 acc[4][4];
  STDECL;
  static constexpr int NG = 4, D = 3, NGRP = 10, SB = 32768, XCH = NG * SB;
  DEV bool run(char* smem, const char* Ab, const char* Wb, int k0, const WJob& job) {
    STR(0); ST(1);
    constexpr int LDA = D_ * 2, LDW = 3 * D_ * 2;
    const int tid = threadIdx.x, l = tid & 63;
    const int wave = __builtin_amdgcn_readfirstlane(tid >> 6);
    if (wave >= 4) {
      const bool isw = wave == 5;
      unsigned vo[2];
#pragma unroll
      for (int q = 0; q < 2; ++q)
        vo[q] = isw ? wrm_voff(l, q, LDW) : (unsigned)((l >> 3) * LDA + (((l & 7) ^ (q * 4 + (l >> 4))) * 16));
      const unsigned vx[4] = {vo[0], isw ? vo[0] : vo[1], isw ? vo[1] : vo[0], vo[1]};
      const unsigned dst0 = lds_addr(smem) + (isw ? 16384 : 0);
      const char* src0 = isw ? Wb : Ab;
      const int gstr = isw ? 64 * LDW : 128, istr = isw ? 4 * LDW : 8 * LDA;
      auto issue = [&](int grp) {
        int kg = grp + k0;
        kg = kg >= NGRP ? kg - NGRP : kg;
        const char* sp = src0 + (size_t)kg * gstr;
#pragma unroll
        for (int i = 0; i < 16; ++i) glds16(sp + (size_t)i * istr, vx[i & 3], dst0 + (grp % NG) * SB + i * 1024);
      };
      static_for<0, D>([&](auto gi) { issue(gi.value); });
      static_for<0, NGRP>([&](auto gi) {
        constexpr int grp = gi.value;
        constexpr int issued = grp + D < NGRP ? grp + D : NGRP;
        wait_vm<16 * (issued - grp - 1) + (grp > NGRP - D ? 16 : 0)>();
        __builtin_amdgcn_s_barrier();
        cfence();
        if constexpr (grp + D < NGRP) issue(grp + D);
        if constexpr (grp == NGRP - D) {
#pragma unroll
          for (int i = 0; i < 16; ++i) {
            int r4 = job.r4 + i;
            const int kq = r4 / 40, rr = r4 - kq * 40;
            if (job.perm) r4 = rr < 32 ? (8 * kq + (rr >> 2)) * 5 + (rr & 3) : (8 * kq + rr - 32) * 5 + 4;
            glds16(job.src + (size_t)r4 * 4 * job.ldw, wrm_voff(l, (i >> 1) & 1, job.ldw), dst0 + ((grp - 1) % NG) * SB + i * 1024);
          }
        }
      });
      __builtin_amdgcn_s_barrier();
      wait_vm<0>();
      if (job.dst) {
        const char* tb = smem + ((NGRP - D - 1) % NG) * SB + (isw ? 16384 : 0);
#pragma unroll
        for (int kk = 0; kk < 2; ++kk)
#pragma unroll
          for (int j = 0; j < 8; ++j) {
            const bf16x8 f = wrm_frag(tb + kk * 8192 + wrm_lane(l, j >> 2, j & 3));
            *(bf16x8*)(job.dst + (size_t)(j * job.wks + kk) * 1024 + l * 16) = f;
          }
      }
      return false;
    }
    const int wm = wave >> 1, wn = wave & 1, g = l >> 4;
    float rs[2] = {0.f, 0.f}, rq[2] = {0.f, 0.f}, cd[2] = {0.f, 0.f};
#pragma unroll
    for (int i = 0; i < 4; ++i)
#pragma unroll
      for (int j = 0; j < 4; ++j) acc[i][j] = f32x4{0.f, 0.f, 0.f, 0.f};
    const unsigned a0 = (unsigned)((wm * 64 + (l & 15)) * 128 + ((g ^ ((l & 15) >> 1)) * 16));
    const char* al[2][2] = {{smem + a0 + wn * 4096, smem + a0 + 4096 - wn * 4096},
                            {smem + (a0 ^ 64u) + wn * 4096, smem + (a0 ^ 64u) + 4096 - wn * 4096}};
    const char* wl[4];
#pragma unroll
    for (int j = 0; j < 4; ++j) wl[j] = smem + 16384 + wrm_lane(l, wn, j ^ (2 * wm));
    bf16x8 af[2][4], wf[2][4];
    auto ldfrag = [&](auto ti) {
      constexpr int t = ti.value, kk = t & 1, so = ((t >> 1) % NG) * SB;
#pragma unroll
      for (int i = 0; i < 4; ++i) af[kk][i] = *(const bf16x8*)(al[kk][i >> 1] + so + (i & 1) * 2048);
#pragma unroll
      for (int j = 0; j < 4; ++j) wf[kk][j] = wrm_frag(wl[j] + so + kk * 8192);
    };
    __builtin_amdgcn_s_barrier();
    cfence();
    ST(3);
    ldfrag(std::integral_constant<int, 0>{});
    static_for<0, 2 * NGRP>([&](auto ti) {
      constexpr int t = ti.value, kk = t & 1;
      if constexpr (t + 1 < 2 * NGRP) {
        if constexpr (kk == 1) {
          wait_lgkm0();
          __builtin_amdgcn_s_barrier();
          cfence();
          ST(3 + (t + 1) / 2);
        }
        ldfrag(std::integral_constant<int, t + 1>{});
      }
      __builtin_amdgcn_sched_barrier(0);
#pragma unroll
      for (int i = 0; i < 4; ++i)
#pragma unroll
        for (int j = 0; j < 4; ++j)
          acc[i][j] = SWAP ? mfma16(wf[kk][j], af[kk][i], acc[i][j]) : mfma16(af[kk][i], wf[kk][j], acc[i][j]);
#pragma unroll
      for (int i = 0; i < 2; ++i) { dot_sum(rs[i], af[kk][i]); dot_sq(rq[i], af[kk][i]); dot_sum(cd[i], wf[kk][i]); }
#pragma unroll
      for (int q = 0; q < 8; ++q) {
        __builtin_amdgcn_sched_group_barrier(0x8, 2, 0);
        __builtin_amdgcn_sched_group_barrier(0x2, 3, 0);
      }
      asm volatile("" : "+v"(rs[0]), "+v"(rs[1]), "+v"(rq[0]), "+v"(rq[1]), "+v"(cd[0]), "+v"(cd[1]));
      __builtin_amdgcn_sched_barrier(0);
    });
    ST(21);
    float2* xs = (float2*)(smem + XCH);
    float* xc = (float*)(smem + XCH + 1024);
#pragma unroll
    for (int i = 0; i < 2; ++i) {
      const float a = red_g(rs[i]), b = red_g(rq[i]), c = red_g(cd[i]);
      float mean, var, m, sd;
      ln_stats(a, b, mean, var, m, sd);
      const float rinv = 1.0f / sd;
      if (l < 16) { xs[(wm * 4 + wn * 2 + i) * 16 + l] = float2{rinv, -m * rinv}; xc[(wn * 4 + wm * 2 + i) * 16 + l] = c; }
    }
    wait_lgkm0();
    __builtin_amdgcn_s_barrier();
    cfence();
    return true;
  }
};

template <bool ISV>
DEV void qkv_body(char* smem, const char* x, const char* wqkv, char* ws, int rt, int ct, int k0, const WJob& job) {
  using GQ = GemmQ<!ISV>;
  GQ gm;
  const int row0 = rt * 128, n0 = ct * 128;
  if (!gm.run(smem, x + (size_t)row0 * (D_ * 2), wqkv + n0 * 2, k0, job)) return;
  const int tid = threadIdx.x, l = tid & 63, wave = tid >> 6, wm = wave >> 1, wn = wave & 1, g = l >> 4;
  const float2* xs = (const float2*)(smem + GQ::XCH);
  const float* xc = (const float*)(smem + GQ::XCH + 1024);
  if constexpr (!ISV) {
    f32x4 cs[4];
    unsigned po[4];
#pragma unroll
    for (int j = 0; j < 4; ++j) {
      const int ja = wn * 4 + (j ^ (2 * wm));
      cs[j] = *(const f32x4*)(xc + ja * 16 + 4 * g);
      const int c = (n0 + ja * 16 + 4 * g) % D_, head = c / 20, d0 = c - head * 20;
      po[j] = (unsigned)(head * (int)QK_HEAD + (d0 >> 3) * 256 + (d0 & 7) * 2) | (d0 == 16 ? 1u : 0u);
    }
    char* dstb = ws + (ct < 5 ? WS_Q : WS_K) + (size_t)(row0 >> 4) * 768 + (l & 15) * 16;
#pragma unroll
    for (int i = 0; i < 4; ++i) {
      const int ia = wm * 4 + (i ^ (2 * wn));
      const float2 st = xs[ia * 16 + (l & 15)];
#pragma unroll
      for (int j = 0; j < 4; ++j) {
        const f32x4 y = gm.acc[i][j] * st.x + cs[j] * st.y;
        char* p = dstb + ia * 768 + (po[j] & ~1u);
        if (po[j] & 1u) *(u32x4*)p = u32x4{pk2(y[0], y[1]), pk2(y[2], y[3]), 0u, 0u};
        else *(u32x2*)p = u32x2{pk2(y[0], y[1]), pk2(y[2], y[3])};
      }
    }
  } else {
    float cd[4];
    unsigned po[4];
#pragma unroll
    for (int j = 0; j < 4; ++j) {
      const int ja = wn * 4 + (j ^ (2 * wm));
      cd[j] = xc[ja * 16 + (l & 15)];
      const int c = n0 - 2 * D_ + ja * 16 + (l & 15), head = c / 20, d = c - head * 20;
      po[j] = (unsigned)(head * (int)V_HEAD + (d < 16 ? (g * 16 + d) * 16 : 1024 + (g * 4 + d - 16) * 16));
    }
    char* dstb = ws + WS_V + (size_t)(row0 >> 5) * 1280;
#pragma unroll
    for (int i = 0; i < 4; ++i) {
      const int ia = wm * 4 + (i ^ (2 * wn));
      const f32x4 s01 = *(const f32x4*)(xs + ia * 16 + 4 * g), s23 = *(const f32x4*)(xs + ia * 16 + 4 * g + 2);
      const f32x4 rr = {s01[0], s01[2], s23[0], s23[2]}, nm = {s01[1], s01[3], s23[1], s23[3]};
#pragma unroll
      for (int j = 0; j < 4; ++j) {
        const f32x4 y = gm.acc[i][j] * rr + nm * cd[j];
        *(u32x2*)(dstb + (ia >> 1) * 1280 + (i & 1) * 8 + po[j]) = u32x2{pk2(y[0], y[1]), pk2(y[2], y[3])};
      }
    }
  }
  STDUMP(gm, ws, 1);
}

DEV void colsum_job(char* smem, const char* w1, const char* w3, float* cs, int blk) {
  const int tid = threadIdx.x, c = tid % 17, rg = tid / 17;
  const int gc = blk * 17 + c;
  const char* base = gc < 80 ? w1 + gc * 16 : w3 + (gc - 80) * 16;
  const int ld = gc < 80 ? D_ * 2 : F_ * 2;
  float a[8] = {0.f, 0.f, 0.f, 0.f, 0.f, 0.f, 0.f, 0.f};
  if (rg < 20) {
#pragma unroll 16
    for (int r = rg; r < D_; r += 20) {
      u32x4 v = *(const u32x4*)(base + (size_t)r * ld);
#pragma unroll
      for (int e = 0; e < 4; ++e) { a[2 * e] += bf_lo(v[e]); a[2 * e + 1] += bf_hi(v[e]); }
    }
  }
  float* sm = (float*)smem;
  if (rg < 20) {
#pragma unroll
    for (int e = 0; e < 8; ++e) sm[rg * 136 + c * 8 + e] = a[e];
  }
  __syncthreads();
  if (tid < 136) {
    float t = 0.f;
#pragma unroll
    for (int r = 0; r < 20; ++r) t += sm[r * 136 + tid];
    cs[blk * 136 + tid] = t;
  }
}

__global__ __launch_bounds__(384) void k1_qkv(const char* x, const char* wqkv, const char* wo, const char* w1, const char* w2,
                                              const char* w3, const char* w4, char* ws) {
  extern __shared__ __attribute__((aligned(16))) char smem[];
  const int b = blockIdx.x, xcd = b & 7, j = b >> 3, r = j & 3, c = j >> 2;
  {
    const u32x4 e = {EMPTY, EMPTY, EMPTY, EMPTY};
    const int t = threadIdx.x, bt = b >> 4;
    *(u32x4*)(ws + WS_O + b * 10240 + t * 16) = e;
    if (t < 256) *(u32x4*)(ws + WS_O + b * 10240 + 6144 + t * 16) = e;
    else if (t < 336) *(u32x4*)(ws + WS_G + ((bt >> 1) * 16 + (bt & 1) * 15) * 20480 + (b & 15) * 1280 + (t - 256) * 16) = e;
  }
  if ((xcd & 1) && c == 7) { colsum_job(smem, w1, w3, (float*)(ws + WS_CS), (xcd >> 1) * 4 + r); return; }
  const int rt = (xcd >> 1) * 4 + r, ct = (xcd & 1) * 8 + c;
  const int wave = __builtin_amdgcn_readfirstlane(threadIdx.x >> 6);
  const WJob job = make_job((rt * 15 + ct) * 2 + (wave & 1), wo, w1, w2, w3, w4, ws);
  const int k0 = (3 * r + 4 * c) % 10;
  if (ct >= 10) qkv_body<true>(smem, x, wqkv, ws, rt, ct, k0, job);
  else qkv_body<false>(smem, x, wqkv, ws, rt, ct, k0, job);
}

typedef __attribute__((ext_vector_type(2))) float f32x2;
typedef unsigned long long u64;
DEV f32x2 unpk(unsigned u) { return f32x2{bf_lo(u), bf_hi(u)}; }
DEV unsigned pk2v(f32x2 v) { return pk2(v[0], v[1]); }
DEV f32x2 bfr2(f32x2 v) { unsigned u = pk2v(v); asm("" : "+v"(u)); return unpk(u); }
DEV void ln_fast(float sum, float sq, float& mean, float& var, float& m, float& s) {
  mean = sum * (1.0f / D_);
  var = fmaxf(sq * (1.0f / D_) - mean * mean, 0.f);
  const f32x2 r = bfr2(f32x2{mean, var});
  m = r[0];
  s = bfr(__builtin_amdgcn_sqrtf(bfr(r[1] + 1e-5f)));
}
DEV f32x2 gelu2(f32x2 x) {
  const float c0 = -2.0f * 0.7978845608028654f * 1.4426950408889634f, c1 = c0 * 0.044715f;
  const f32x2 z = x * (x * x * c1 + c0);
  const f32x2 d = f32x2{__builtin_amdgcn_exp2f(z[0]), __builtin_amdgcn_exp2f(z[1])} + 1.0f;
  return x * f32x2{__builtin_amdgcn_rcpf(d[0]), __builtin_amdgcn_rcpf(d[1])};
}
DEV f32x2 lo2(f32x4 v) { return f32x2{v[0], v[1]}; }
DEV f32x2 hi2(f32x4 v) { return f32x2{v[2], v[3]}; }

typedef __amdgpu_buffer_rsrc_t rsrc_t;
constexpr unsigned OOB = 0x40000000u;
DEV unsigned full(unsigned v) { return v < EMPTY ? v : EMPTY - 1; }
DEV void st8(rsrc_t r, unsigned v, unsigned s, u32x2 d) { __builtin_amdgcn_raw_buffer_store_b64(u32x2{full(d[0]), full(d[1])}, r, v, s, 16); }
template <int AUX> DEV void st16r(rsrc_t r, unsigned v, unsigned s, u32x4 d) {
  asm("" : "+v"(d));
  __builtin_amdgcn_raw_buffer_store_b128(d, r, v, s, AUX);
  asm volatile("s_nop 1" ::"v"(d) : "memory");
}
DEV void st16(rsrc_t r, unsigned v, unsigned s, u32x4 d) { st16r<16>(r, v, s, u32x4{full(d[0]), full(d[1]), full(d[2]), full(d[3])}); }
DEV void st_empty(rsrc_t r, unsigned v, unsigned s) { st16r<16>(r, v, s, u32x4{EMPTY, EMPTY, EMPTY, EMPTY}); }
DEV u32x4 ld16(rsrc_t r, unsigned v, unsigned s) { return __builtin_amdgcn_raw_buffer_load_b128(r, v, s, 16); }
DEV u32x2 ld8(rsrc_t r, unsigned v, unsigned s) { return __builtin_amdgcn_raw_buffer_load_b64(r, v, s, 16); }
DEV u32x4 ld16p(rsrc_t r, unsigned v, unsigned s) { return __builtin_amdgcn_raw_buffer_load_b128(r, v, s, 0); }
DEV void vm0() { __builtin_amdgcn_s_waitcnt(0x0f70); cfence(); }

constexpr int LDS_TOT = 163840;
constexpr int AT_Q = 0, AT_K = AT_Q + 16 * 768, AT_V = AT_K + 32 * 768;
constexpr int SLOT = 25600, SLOT7 = 43008;
constexpr int L_W = 51200, L_W2 = 57344;
constexpr int L_SX = 159744;
constexpr int L_SY = 161792;
constexpr int C_P = L_SY, C_S = L_SY + 128, C_F = L_SY + 256, C_W2 = L_SY + 384, C_W4 = L_SY + 388, C_WC = L_SY + 392, C_OK = L_SY + 396, C_WL = L_SY + 400;
constexpr int L_LN = L_SY + 512;
constexpr int L_DUM = L_SY + 1024;
DEV void touch(const char* p, int l, unsigned lb) {
  asm volatile("s_mov_b32 m0, %0\n\ts_nop 0\n\tglobal_load_lds_dword %1, %2" ::"s"(lb + L_DUM + 512), "v"(l * 128), "s"(p) : "memory");
}
DEV void lds_inc(unsigned a) {
  asm volatile("s_mov_b64 exec, 1\n\tds_add_u32 %0, %1\n\ts_mov_b64 exec, -1" ::"v"(a), "v"(1u) : "memory");
}
DEV void lds_wait(unsigned a, unsigned target) {
  for (int it = 0; it < (1 << 22); ++it) {
    const unsigned v = __builtin_amdgcn_readfirstlane(*(volatile LAS unsigned*)(uintptr_t)a);
    if (v >= target) break;
  }
  cfence();
}
DEV f32x4 lds_ld4(unsigned a) { return *(const LAS f32x4*)(uintptr_t)a; }
DEV unsigned max3u(unsigned a, unsigned b, unsigned c) { a = a > b ? a : b; return a > c ? a : c; }

template <int KW, int MODE>
DEV void m_issue(u32x4 (&a)[6], rsrc_t wr, int l, unsigned s, unsigned v) {
#pragma unroll
  for (int k = 0; k < (MODE == 1 ? KW - 1 : KW); ++k) a[k] = ld16(wr, v + k * 1024, s);
  if constexpr (MODE == 1) {
    const unsigned t = (l >> 4) * 256 + (l & 15) * 8 + 4096;
    const u32x2 t0 = ld8(wr, t, s), t1 = ld8(wr, t + 128, s);
    a[KW - 1] = u32x4{t0[0], t0[1], t1[0], t1[1]};
  }
  cfence();
}
template <int NT, int KW, int NTILE, int MODE, int NB, int PRE, class TS, class TV, class TG, class TL, class TH>
DEV void m_phase(const bf16x8 (&W)[30], u32x4 (&a)[3][6], int l, unsigned lb, rsrc_t wr, int n0, int part, TS&& so, TV&& vo, TG&& guard,
                 TL&& slot, TH&& hook STARG) {
  constexpr int RS = NT * 64 + 16;
  const int g = l >> 4, li = l & 15;
  const unsigned pw = lb + part * (16 * RS) + li * RS + g * 16;
  const bool dg = g == (li >> 2);
  const unsigned sx = dg ? lb + L_SX + li * 32 + part * 8 : lb + L_DUM + l * 8, sm = dg ? 512u : 0u;
  static_for<PRE, NB - 1>([&](auto i) { m_issue<KW, MODE>(a[i.value], wr, l, so(i), vo(i)); });
  static_for<0, NTILE>([&](auto i) {
    constexpr int B = i.value % NB;
    const int n = n0 + i;
    auto bad = [&]() {
      unsigned m = 0;
#pragma unroll
      for (int k = 0; k < KW; ++k) m = max3u(max3u(m, a[B][k][0], a[B][k][1]), a[B][k][2], a[B][k][3]);
      return __builtin_amdgcn_ballot_w64(m == EMPTY) != 0;
    };
    if (bad()) {
      int it = 0;
#pragma unroll 1
      do { m_issue<KW, MODE>(a[B], wr, l, so(i), vo(i)); vm0(); } while (bad() && ++it < (1 << 16));
    }
    cfence();
    if (i == 0) { ST(STI); }
    const f32x4 z4 = {0.f, 0.f, 0.f, 0.f};
    f32x4 sa = z4, qa = z4;
    if constexpr (MODE == 2) {
      const u32x4 one = {0x3F803F80u, 0x3F803F80u, 0x3F803F80u, 0x3F803F80u};
#pragma unroll
      for (int k = 0; k < KW; ++k) {
        const bf16x8 f = __builtin_bit_cast(bf16x8, a[B][k]);
        qa = mfma16(f, f, qa); sa = mfma16(__builtin_bit_cast(bf16x8, one), f, sa);
      }
      __builtin_amdgcn_sched_barrier(0);
    }
    if constexpr (i.value + NB - 1 < NTILE) {
      constexpr std::integral_constant<int, i.value + NB - 1> j{};
      m_issue<KW, MODE>(a[j.value % NB], wr, l, so(j), vo(j));
    }
    const unsigned ga = guard(i);
    const unsigned gv = *(volatile LAS unsigned*)(uintptr_t)ga;
    __builtin_amdgcn_sched_barrier(0);
    f32x4 acc[NT];
#pragma unroll
    for (int j = 0; j < NT; ++j) acc[j] = mfma16(W[j], __builtin_bit_cast(bf16x8, a[B][0]), z4);
    if constexpr (MODE == 2) {
      const float q01 = (li & 1) ? qa[1] : qa[0], q23 = (li & 1) ? qa[3] : qa[2];
      *(LAS f32x2*)(uintptr_t)(sx + (i.value & 3) * sm) = f32x2{sa[0], (li & 2) ? q23 : q01};
      lds_inc(lb + C_S + n * 4);
      __builtin_amdgcn_sched_barrier(0);
    }
#pragma unroll
    for (int k = 1; k < KW; ++k)
#pragma unroll
      for (int j = 0; j < NT; ++j) acc[j] = mfma16(W[k * NT + j], __builtin_bit_cast(bf16x8, a[B][k]), acc[j]);
    __builtin_amdgcn_sched_barrier(0);
    if (__builtin_amdgcn_readfirstlane(gv) < 4u) lds_wait(ga, 4);
    const unsigned sl = slot(i);
#pragma unroll
    for (int j = 0; j < NT; ++j) *(LAS f32x4*)(uintptr_t)(pw + sl + j * 64) = acc[j];
    lds_inc(lb + C_P + n * 4);
    hook(i);
  });
  ST(STI + 1);
}

DEV unsigned lds_cnt(unsigned a) { const unsigned v = *(volatile LAS unsigned*)(uintptr_t)a; cfence(); return v; }
template <int NP, int RS>
DEV void e_sum(f32x4& lo, f32x4& hi, unsigned lb, unsigned a, int n) {
  f32x4 p[NP][2];
#pragma unroll 1
  for (int it = 0;; ++it) {
    const unsigned c = lds_cnt(lb + C_P + n * 4);
#pragma unroll
    for (int q = 0; q < NP; ++q) { p[q][0] = lds_ld4(a + q * (16 * RS)); p[q][1] = lds_ld4(a + q * (16 * RS) + 16); }
    if (__builtin_amdgcn_readfirstlane(c) >= (unsigned)NP || it) break;
    lds_wait(lb + C_P + n * 4, NP);
  }
  lds_inc(lb + C_F + n * 4);
#pragma unroll
  for (int s = 1; s < NP; s *= 2)
#pragma unroll
    for (int q = 0; q < NP; q += 2 * s) { p[q][0] += p[q + s][0]; p[q][1] += p[q + s][1]; }
  lo = p[0][0]; hi = p[0][1];
}
struct StatRd { unsigned c; f32x4 a, b; };
DEV StatRd e_stat0(unsigned lb, int n, int li) {
  StatRd r;
  r.c = lds_cnt(lb + C_S + n * 4);
  r.a = lds_ld4(lb + L_SX + (n & 3) * 512 + li * 32); r.b = lds_ld4(lb + L_SX + (n & 3) * 512 + li * 32 + 16);
  cfence();
  return r;
}
DEV void e_stat1(StatRd r, float& sum, float& sq, unsigned lb, int n, int li) {
  if (__builtin_amdgcn_readfirstlane(r.c) < 4u) { lds_wait(lb + C_S + n * 4, 4); r = e_stat0(lb, n, li); }
  const f32x4 t = r.a + r.b;
  sum = t[0] + t[2]; sq = t[1] + t[3];
}

template <int I>
DEV void attention(char* smem, int xcd, int w, u32x2& om, u32x2& ot) {
  const int l = threadIdx.x & 63, g = l >> 4, li = l & 15;
  u32x4 qv = *(const u32x4*)(smem + AT_Q + (2 * w + I) * 768 + (g < 3 ? g : 2) * 256 + li * 16);
  if (g == 3) qv = u32x4{0u, 0u, 0u, 0u};
  const bf16x8 qf = __builtin_bit_cast(bf16x8, qv);
  const char* Ks = smem + AT_K + (2 * w + I) * 768 + (g < 3 ? g : 2) * 256 + li * 16;
  const int kbase = xcd * 256 + 32 * w + 16 * I - 256;
  f32x4 s0[17];
  const f32x4 z4 = {0.f, 0.f, 0.f, 0.f};
#pragma unroll
  for (int c = 0; c < 17; ++c) s0[c] = mfma16(*(const bf16x8*)(Ks + c * 768), qf, z4);
  const float NEG = -__builtin_inff();
#pragma unroll
  for (int r = 0; r < 4; ++r) {
    const int kj = 4 * g + r;
    if (kj <= li) s0[0][r] = NEG;
    if (kj > li) s0[16][r] = NEG;
  }
  if (kbase < 0) {
#pragma unroll
    for (int c = 0; c < 17; ++c)
      if (kbase + 16 * c < 0) s0[c] = f32x4{NEG, NEG, NEG, NEG};
  }
  float m0 = NEG, m1 = NEG;
#pragma unroll
  for (int c = 0; c < 17; ++c) {
    m0 = fmaxf(fmaxf(m0, s0[c][0]), s0[c][1]);
    m1 = fmaxf(fmaxf(m1, s0[c][2]), s0[c][3]);
  }
  m0 = max_g(fmaxf(m0, m1));
  const float C2 = 0.22360679774997896f * 1.4426950408889634f;
  const float b0 = -m0 * C2;
  u32x2 p0[18];
#pragma unroll
  for (int c = 0; c < 17; ++c) {
    const f32x4 e = s0[c] * C2 + b0;
    p0[c + I] = u32x2{pk2(__builtin_amdgcn_exp2f(e[0]), __builtin_amdgcn_exp2f(e[1])), pk2(__builtin_amdgcn_exp2f(e[2]), __builtin_amdgcn_exp2f(e[3]))};
  }
  p0[I ? 0 : 17] = u32x2{0u, 0u};
  const char* Vs0 = smem + AT_V + w * 1280 + (g * 16 + li) * 16;
  const char* Vs1 = smem + AT_V + w * 1280 + 1024 + (g * 4 + (li & 3)) * 16;
  f32x4 o0 = z4, o1 = z4;
#pragma unroll
  for (int s = 0; s < 9; ++s) {
    bf16x8 v0 = *(const bf16x8*)(Vs0 + s * 1280);
    u32x4 v1u = *(const u32x4*)(Vs1 + s * 1280);
    if (li == 4) v1u = u32x4{0x3F803F80u, 0x3F803F80u, 0x3F803F80u, 0x3F803F80u};
    const u32x4 pa = {p0[2 * s][0], p0[2 * s][1], p0[2 * s + 1][0], p0[2 * s + 1][1]};
    const bf16x8 pf = __builtin_bit_cast(bf16x8, pa);
    o0 = mfma16(v0, pf, o0); o1 = mfma16(__builtin_bit_cast(bf16x8, v1u), pf, o1);
  }
  const float r0 = 1.0f / __shfl(o1[0], 16 + li);
  om = u32x2{pk2(o0[0] * r0, o0[1] * r0), pk2(o0[2] * r0, o0[3] * r0)};
  ot = u32x2{pk2(o1[0] * r0, o1[1] * r0), pk2(o1[2] * r0, o1[3] * r0)};
}

#define CTX \
  int tid_ = threadIdx.x, b_ = blockIdx.x; \
  asm volatile("" : "+v"(tid_)); asm volatile("" : "+s"(b_)); \
  const int xcd = b_ & 7, jj = b_ >> 3, l = tid_ & 63, g = l >> 4, li = l & 15; \
  const int w = __builtin_amdgcn_readfirstlane(tid_ >> 6), q = w & 3, grp = w >> 2; \
  const int ct = jj & 7, ct16 = jj & 15; \
  const unsigned vl = l * 16; \
  const int G4 = (xcd * 4 + (jj >> 3)) * 4, dir = (jj >> 3) & 1; \
  const int B8 = (xcd * 2 + (jj >> 4)) * 8; \
  auto g4 = [&](int i) { return G4 + (dir ? 3 - i : i); }; \
  auto g8 = [&](int i) { return B8 + ((i & 1) ? 7 - (i >> 1) : (i >> 1)); }; \
  auto lane = [&](int) { return vl; }; \
  auto ring = [&](int n) { return lb + (n >= 2 ? C_F + (n - 2) * 4 : C_OK); }; \
  auto slot2 = [&](int i) { return (unsigned)((i & 1) * SLOT); }; \
  const int wid = jj * 4 + q; \
  auto nohook = [](int) {}; \
  const int nu = q < 2 ? 3 : 2, un = (q < 2 ? 3 * q : 2 * q + 2) + (g < nu ? g : nu - 1); \
  const bool act = g < nu; \
  const int ko = ct * 10 + un; \
  const unsigned vft = (ko >> 2) * 1024 + (ko & 3) * 256 + li * 16, vst = act ? vft : OOB; \
  const unsigned pe = lb + li * 336 + un * 32; \
  const unsigned xo = li * (D_ * 2) + ko * 16; \
  const int k6 = ct16 * 12 + 3 * q + (g < 3 ? g : 2); \
  const unsigned v6 = g < 3 ? (unsigned)((k6 >> 2) * 1024 + (k6 & 3) * 256 + li * 16) : OOB; \
  const unsigned w4b = (unsigned)WS_W4 + ct * (5 * 48 * 1024) + w * 6144; \
  (void)0

__global__ __launch_bounds__(512) void kp_main(const char* x, char* ws, char* out) {
  extern __shared__ __attribute__((aligned(16))) char smem[];
  STDECL;
  STR(0); ST(1);
  const unsigned lb = lds_addr(smem);
  const rsrc_t wr = __builtin_amdgcn_make_buffer_rsrc(ws, 0, (int)WS_END, 0x00020000);
  bf16x8 W[30];
  u32x4 a[3][6];
  int ga, gb;
  {
    CTX;
#pragma unroll
    for (int i = 0; i < 4; ++i) {
      st_empty(wr, vst, (unsigned)(grp ? WS_H : WS_G) + (G4 + i) * 20480);
      st_empty(wr, grp ? vst : OOB, (unsigned)WS_X1 + (G4 + i) * 20480);
    }
    const unsigned qo = (unsigned)WS_Q + jj * (unsigned)QK_HEAD + xcd * 16 * 768;
    const unsigned ko0 = (unsigned)WS_K + jj * (unsigned)QK_HEAD, vo0 = (unsigned)WS_V + jj * (unsigned)V_HEAD;
    const int koff0 = (xcd * 16 - 16) * 768, voff0 = (xcd * 8 - 8) * 1280;
#pragma unroll
    for (int i = 0; i < 7; ++i) {
      const int p = w + 8 * i, ok = koff0 + (p - 12) * 1024, ov = voff0 + (p - 36) * 1024;
      const unsigned src = p < 12 ? qo + p * 1024 : p < 36 ? ko0 + (ok < 0 ? 0 : ok) : vo0 + (ov < 0 ? 0 : ov);
      glds16(ws + src, vl, lb + AT_Q + p * 1024);
    }
    *(LAS unsigned*)(uintptr_t)(lb + L_SY + (tid_ & 127) * 4) = (tid_ & 127) == (C_OK - L_SY) / 4 ? 4u : 0u;
    __builtin_amdgcn_s_barrier();
    const unsigned wb = (unsigned)WS_WO + grp * (D_ * D_ * 2) + ct * (5 * 20 * 1024) + q * 5120;
#pragma unroll
    for (int k = 0; k < 5; ++k)
#pragma unroll
      for (int j = 0; j < 5; ++j) W[k * 5 + j] = __builtin_bit_cast(bf16x8, ld16p(wr, vl + k * 1024, wb + j * (20 * 1024)));
    cfence();
#pragma unroll
    for (int i = 0; i < 13; ++i) {
      int u = w + 8 * i;
      u = u < 100 ? u : 99;
      const int t = u / 5, j = u - t * 5;
      glds16(ws + WS_W2 + (unsigned)(ct * (5 * 20 * 1024) + (j * 20 + t) * 1024), vl, lb + L_W2 + u * 1024);
    }
    wait_vm<38>();
    __builtin_amdgcn_s_barrier();
    cfence();
    ST(2);
    u32x2 om0, ot0, om1, ot1;
    attention<0>(smem, xcd, w, om0, ot0);
    __builtin_amdgcn_sched_barrier(0);
    const int tu = jj * 8 + w;
    touch(tu < 120 ? ws + WS_W4 + (unsigned)((tu / 3) * 49152 + 24576 + (tu % 3) * 8192) : x + (unsigned)(xcd * 327680 + (tu < 160 ? tu - 120 : 39) * 8192), l, lb);
    touch(ws + WS_W3 + (unsigned)((tu < 240 ? tu : 239) * 8192), l, lb);
    __builtin_amdgcn_sched_barrier(0);
    attention<1>(smem, xcd, w, om1, ot1);
    {
      const auto s0 = __builtin_amdgcn_permlane16_swap(om0[0], om1[0], false, false);
      const auto s1 = __builtin_amdgcn_permlane16_swap(om0[1], om1[1], false, false);
      const auto t0 = __builtin_amdgcn_permlane16_swap(ot0[0], ot1[0], false, false);
      const auto t1 = __builtin_amdgcn_permlane16_swap(ot0[1], ot1[1], false, false);
      const unsigned Of = (unsigned)WS_O + (xcd * 16 + 2 * w) * 20480 + (jj >> 3) * 5120;
      st16(wr, (g & 1) * 20480 + (g >> 1) * 256 + li * 16, Of + (jj & 7) * 512, u32x4{s0[0], s1[0], s0[1], s1[1]});
      st8(wr, g < 2 ? g * 20480 + li * 8 : OOB, Of + 4096 + (jj & 7) * 128, u32x2{t0[0], t1[0]});
    }
    ST(3);
    wait_vm<4>();
    __builtin_amdgcn_s_barrier();
    cfence();
    ga = grp; gb = grp;
    asm volatile("" : "+s"(ga), "+s"(gb));
  }
  if (ga == 0) {
    CTX;
#pragma unroll
    for (int i = 0; i < 8; ++i) st_empty(wr, v6, (unsigned)WS_U + (B8 + i) * 49152);
    f32x4 cs[2];
#pragma unroll
    for (int i = 0; i < 2; ++i) cs[i] = __builtin_bit_cast(f32x4, ld16p(wr, ko * 32 + i * 16, (unsigned)WS_CS));
    m_phase<5, 5, 4, 1, 3, 0>(W, a, l, lb, wr, 0, q, [&](int i) { return (unsigned)WS_O + g4(i) * 20480 + q * 5120; }, lane, ring, slot2,
                              nohook STPASS(4));
#pragma unroll
    for (int f = 0; f < 25; ++f) W[f] = *(const LAS bf16x8*)(uintptr_t)(lb + L_W2 + (q * 25 + f) * 1024 + vl);
    lds_inc(lb + C_W2);
    lds_wait(lb + C_W2, 4);
    auto stage4 = [&](int f) { glds16(ws + (w4b + 4 * 6144 + (f % 5) * (48 * 1024) + (f / 5) * 1024), vl, lb + L_W + (q * 26 + f) * 1024); };
#pragma unroll
    for (int f = 0; f < 8; ++f) stage4(f);
    const int sr = li - ((q & 2) ? ((q & 1) ? -2 : 2) : ((q & 1) ? -1 : 1));
    const unsigned vg = (unsigned)(((sr >> 4) + 1) * 20480 + g * 256 + (sr & 15) * 16);
    auto sog = [&](int i) { return (unsigned)WS_G + (g4(i) - 1) * 20480 + q * 5120; };
    auto vog = [&](int i) { return (unsigned)(g4(i) * 16 + sr) < (unsigned)S_ ? vg : OOB; };
    {
      StatRd sd = e_stat0(lb, 4, li);
      static_for<0, 4>([&](auto i) {
        constexpr int n = 4 + i.value;
        float sum, sq, mean, var, m1, s1;
        e_stat1(sd, sum, sq, lb, n, li);
        ln_fast(sum, sq, mean, var, m1, s1);
        const float r1 = __builtin_amdgcn_rcpf(s1);
        const f32x2 mv = bfr2(f32x2{(mean - m1) * r1, var * r1 * r1});
        const float s2 = bfr(__builtin_amdgcn_sqrtf(bfr(mv[1] + 1e-5f)));
        float rr = r1 * __builtin_amdgcn_rcpf(s2);
        const float nm = -(m1 + mv[0] * s1) * rr;
        if (w == 0) *(LAS f32x2*)(uintptr_t)(lb + L_LN + (i * 16 + li) * 8) = f32x2{m1, r1};
        f32x4 c0 = cs[0] * nm, c1 = cs[1] * nm;
        asm volatile("" : "+v"(rr), "+v"(c0), "+v"(c1));
        f32x4 lo, hi;
        e_sum<4, 336>(lo, hi, lb, pe + (n & 1) * SLOT, n);
        if constexpr (i.value < 3) sd = e_stat0(lb, n + 1, li);
        if (i == 0) { ST(6); }
        lo = lo * rr + c0; hi = hi * rr + c1;
        st16(wr, vst, (unsigned)WS_G + g4(i) * 20480,
             u32x4{pk2v(gelu2(bfr2(lo2(lo)))), pk2v(gelu2(bfr2(hi2(lo)))), pk2v(gelu2(bfr2(lo2(hi)))), pk2v(gelu2(bfr2(hi2(hi))))});
        if constexpr (i.value < 3) {
#pragma unroll
          for (int f = 0; f < 6; ++f) stage4(8 + 6 * i + f);
        }
        if constexpr (i.value == 2) m_issue<5, 0>(a[0], wr, l, sog(0), vog(0));
      });
    }
    ST(7);
#pragma unroll
    for (int i = 0; i < 2; ++i) cs[i] = __builtin_bit_cast(f32x4, ld16p(wr, k6 * 32 + i * 16, (unsigned)WS_CS + D_ * 4));
    m_phase<5, 5, 4, 0, 3, 1>(W, a, l, lb, wr, 8, q, sog, vog, [&](int i) { return ring(8 + i); }, slot2,
                              [&](int i) { if (i == 0) lds_inc(lb + C_W4); } STPASS(8));
    {
      const unsigned p6 = lb + li * 400 + (3 * q + (g < 3 ? g : 2)) * 32;
      StatRd sd = e_stat0(lb, 12, li);
      static_for<0, 8>([&](auto i) {
        constexpr int n = 12 + i.value;
        if constexpr (i.value < 6) {
#pragma unroll
          for (int j = 0; j < 5; ++j) W[i.value * 5 + j] = __builtin_bit_cast(bf16x8, ld16p(wr, vl + i.value * 1024, w4b + j * (48 * 1024)));
          cfence();
        }
        if constexpr (i.value == 7) m_issue<6, 0>(a[0], wr, l, (unsigned)WS_U + g4(0) * 49152 + w * 6144, vl);
        float sum, sq, mean, var, m, s;
        e_stat1(sd, sum, sq, lb, n, li);
        ln_fast(sum, sq, mean, var, m, s);
        float rinv = __builtin_amdgcn_rcpf(s);
        const float nm = -m * rinv;
        f32x4 c0 = cs[0] * nm, c1 = cs[1] * nm;
        asm volatile("" : "+v"(rinv), "+v"(c0), "+v"(c1));
        f32x4 lo, hi;
        e_sum<4, 400>(lo, hi, lb, p6 + (n & 1) * SLOT, n);
        if constexpr (i.value < 7) sd = e_stat0(lb, n + 1, li);
        if (i == 0) { ST(10); }
        lo = lo * rinv + c0; hi = hi * rinv + c1;
        st16(wr, v6, (unsigned)WS_U + g8(i) * 49152,
             u32x4{pk2v(gelu2(bfr2(lo2(lo)))), pk2v(gelu2(bfr2(hi2(lo)))), pk2v(gelu2(bfr2(lo2(hi)))), pk2v(gelu2(bfr2(hi2(hi))))});
      });
      ST(11);
    }
  }
  if (gb != 0) {
    CTX;
    const rsrc_t xr = __builtin_amdgcn_make_buffer_rsrc((void*)x, 0, S_ * D_ * 2, 0x00020000);
    auto soh = [&](int i) { return (unsigned)WS_H + g4(i) * 20480 + q * 5120; };
    auto sox = [&](int i) { return (unsigned)WS_X1 + g8(i) * 20480 + q * 5120; };
    {
      u32x4 xv[4];
#pragma unroll
      for (int i = 0; i < 4; ++i) xv[i] = ld16p(xr, xo, g4(i) * (16 * D_ * 2));
      cfence();
#pragma unroll
      for (int i = 0; i < 4; ++i) {
        f32x4 lo, hi;
        e_sum<4, 336>(lo, hi, lb, pe + (i & 1) * SLOT, i);
        if (i == 0) { ST(4); }
        st16(wr, vst, (unsigned)WS_H + g4(i) * 20480,
             u32x4{pk2v(unpk(xv[i][0]) + bfr2(lo2(lo))), pk2v(unpk(xv[i][1]) + bfr2(hi2(lo))),
                   pk2v(unpk(xv[i][2]) + bfr2(lo2(hi))), pk2v(unpk(xv[i][3]) + bfr2(hi2(hi)))});
        if (i == 2) m_issue<5, 2>(a[0], wr, l, soh(0), vl);
      }
      ST(5);
    }
    m_phase<5, 5, 4, 2, 3, 1>(W, a, l, lb, wr, 4, q, soh, lane, [&](int i) { return ring(4 + i); }, slot2, nohook STPASS(6));
    {
      u32x4 hv[4];
#pragma unroll
      for (int i = 0; i < 4; ++i) hv[i] = ld16(wr, vft, (unsigned)WS_H + g4(i) * 20480);
      cfence();
      { const int u = wid < 120 ? wid : 119; touch(ws + WS_W4 + (unsigned)((u / 3) * 49152 + (u % 3) * 8192), l, lb); }
      const unsigned w3b = (unsigned)WS_W3 + ct16 * (6 * 20 * 1024) + q * 5120;
      auto load3 = [&](int f) { W[f] = __builtin_bit_cast(bf16x8, ld16p(wr, vl + (f / 6) * 1024, w3b + (f % 6) * (20 * 1024))); };
#pragma unroll
      for (int f = 0; f < 12; ++f) load3(f);
      cfence();
#pragma unroll
      for (int i = 0; i < 4; ++i) {
        const int n = 8 + i;
        f32x4 lo, hi;
        e_sum<4, 336>(lo, hi, lb, pe + (n & 1) * SLOT, n);
        if (i == 0) { ST(8); }
        const f32x2 mr = *(const LAS f32x2*)(uintptr_t)(lb + L_LN + (i * 16 + li) * 8);
        const float m1 = mr[0], r1 = mr[1];
        st16(wr, vst, (unsigned)WS_X1 + g4(i) * 20480,
             u32x4{pk2v(bfr2(bfr2(unpk(hv[i][0]) - m1) * r1) + bfr2(lo2(lo))), pk2v(bfr2(bfr2(unpk(hv[i][1]) - m1) * r1) + bfr2(hi2(lo))),
                   pk2v(bfr2(bfr2(unpk(hv[i][2]) - m1) * r1) + bfr2(lo2(hi))), pk2v(bfr2(bfr2(unpk(hv[i][3]) - m1) * r1) + bfr2(hi2(hi)))});
        if (i < 3) {
#pragma unroll
          for (int f = 0; f < 6; ++f) load3(12 + 6 * i + f);
          cfence();
        }
        if (i == 2) { m_issue<5, 2>(a[0], wr, l, sox(0), vl); m_issue<5, 2>(a[1], wr, l, sox(1), vl); }
      }
      ST(9);
    }
    m_phase<6, 5, 8, 2, 3, 2>(W, a, l, lb, wr, 12, q, sox, lane, [&](int i) { return ring(12 + i); }, slot2, nohook STPASS(10));
    m_issue<6, 0>(a[0], wr, l, (unsigned)WS_U + g4(0) * 49152 + w * 6144, vl);
#pragma unroll
    for (int j = 1; j < 5; ++j) W[25 + j] = __builtin_bit_cast(bf16x8, ld16p(wr, vl + 5 * 1024, w4b + j * (48 * 1024)));
    lds_wait(lb + C_W4, 4);
#pragma unroll
    for (int f = 0; f < 26; ++f) W[f] = *(const LAS bf16x8*)(uintptr_t)(lb + L_W + (q * 26 + f) * 1024 + vl);
    lds_inc(lb + C_WC);
  }
  {
    CTX;
    const rsrc_t orr = __builtin_amdgcn_make_buffer_rsrc(out, 0, S_ * D_ * 2, 0x00020000);
    u32x4 hv[2], xv[2];
#pragma unroll
    for (int i = 0; i < 2; ++i) {
      const unsigned to = g4(2 * i + grp) * 20480;
      hv[i] = ld16(wr, vft, (unsigned)WS_H + to); xv[i] = ld16(wr, vft, (unsigned)WS_X1 + to);
    }
    auto fin = [&](int p, auto si) {
      constexpr int s = si.value;
      f32x4 lo, hi;
      e_sum<8, 336>(lo, hi, lb, pe + (p % 3) * SLOT7, 20 + p);
      const u32x4 y = {pk2v(unpk(hv[s][0]) + bfr2(unpk(xv[s][0]) + bfr2(lo2(lo)))), pk2v(unpk(hv[s][1]) + bfr2(unpk(xv[s][1]) + bfr2(hi2(lo)))),
                       pk2v(unpk(hv[s][2]) + bfr2(unpk(xv[s][2]) + bfr2(lo2(hi)))), pk2v(unpk(hv[s][3]) + bfr2(unpk(xv[s][3]) + bfr2(hi2(hi))))};
      st16r<0>(orr, act ? xo : OOB, g4(p) * (16 * D_ * 2), y);
    };
    m_phase<5, 6, 4, 0, 2, 1>(W, a, l, lb, wr, 20, w, [&](int i) { return (unsigned)WS_U + g4(i) * 49152 + w * 6144; }, lane,
        [&](auto i) { return lb + (i.value == 0 ? C_F + 19 * 4 : i.value == 1 ? C_WC : i.value == 2 ? C_OK : C_F + 20 * 4); },
        [&](auto i) { return (unsigned)((i.value % 3) * SLOT7); },
        [&](auto i) {
          if constexpr (i.value == 1) { if (grp == 0) fin(0, std::integral_constant<int, 0>{}); }
          if constexpr (i.value == 2) { if (grp == 1) fin(1, std::integral_constant<int, 0>{}); }
          if constexpr (i.value == 3) fin(2 + grp, std::integral_constant<int, 1>{});
        } STPASS(12));
    STDUMP2(ws, 2 + grp);
  }
}

extern "C" void kernel_launch(void* const* d_in, const int* in_sizes, int n_in, void* d_out, int out_size,
                              void* d_ws, size_t ws_size, hipStream_t stream) {
  const char* x = (const char*)d_in[0];
  const char* wqkv = (const char*)d_in[1];
  const char* wo = (const char*)d_in[2];
  const char* w1 = (const char*)d_in[4];
  const char* w2 = (const char*)d_in[5];
  const char* w3 = (const char*)d_in[6];
  const char* w4 = (const char*)d_in[7];
  char* ws = (char*)d_ws;
  if (ws_size < WS_END) return;
  constexpr int LQ = 4 * 32768 + 2048;
  (void)hipFuncSetAttribute((const void*)k1_qkv, hipFuncAttributeMaxDynamicSharedMemorySize, LQ);
  (void)hipFuncSetAttribute((const void*)kp_main, hipFuncAttributeMaxDynamicSharedMemorySize, LDS_TOT);
  k1_qkv<<<256, 384, LQ, stream>>>(x, wqkv, wo, w1, w2, w3, w4, ws);
  kp_main<<<256, 512, LDS_TOT, stream>>>(x, ws, (char*)d_out);
}
```

```cpp
#include <hip/hip_runtime.h>
#include <hip/hip_bf16.h>
#include <stdint.h>
#include <type_traits>

typedef __attribute__((ext_vector_type(8))) __bf16 bf16x8;
typedef __attribute__((ext_vector_type(4))) __bf16 bf16x4;
typedef __attribute__((ext_vector_type(2))) __bf16 bf16x2;
typedef __attribute__((ext_vector_type(4))) short s16x4;
typedef __attribute__((ext_vector_type(4))) float f32x4;
typedef __attribute__((ext_vector_type(2))) unsigned u32x2;
typedef __attribute__((ext_vector_type(4))) unsigned u32x4;
#define LAS __attribute__((address_space(3)))
#define DEV __device__ __forceinline__

constexpr int S_ = 2048, D_ = 640, F_ = 1536, H_ = 32;

constexpr size_t QK_HEAD = 128 * 768;
constexpr size_t V_HEAD  = 64 * 1280;
constexpr size_t WS_Q  = 0;
constexpr size_t WS_K  = WS_Q + H_ * QK_HEAD;
constexpr size_t WS_V  = WS_K + H_ * QK_HEAD;
constexpr size_t WS_O  = WS_V + H_ * V_HEAD;
constexpr size_t WS_H  = WS_O + (size_t)S_ * D_ * 2;
constexpr size_t WS_G  = WS_H + (size_t)S_ * D_ * 2;
constexpr size_t WS_X1 = WS_G + (size_t)S_ * D_ * 2;
constexpr size_t WS_U  = WS_X1 + (size_t)S_ * D_ * 2;
constexpr size_t WS_CS = WS_U + (size_t)S_ * F_ * 2;
constexpr size_t WS_W3 = WS_CS + 16384;
constexpr size_t WS_W4 = WS_W3 + (size_t)D_ * F_ * 2;
constexpr size_t WS_END = WS_W4 + (size_t)D_ * F_ * 2;
constexpr unsigned EMPTY = 0xffffffffu;

template <int I, int N, class Fn> DEV void static_for(Fn&& f) {
  if constexpr (I < N) { f(std::integral_constant<int, I>{}); static_for<I + 1, N>(f); }
}
template <int N> DEV void wait_vm() { asm volatile("s_waitcnt vmcnt(%0) lgkmcnt(0)" ::"n"(N) : "memory"); }
DEV void glds16(const char* sbase, unsigned voff, unsigned lds) {
  asm volatile("s_mov_b32 m0, %0\n\ts_nop 0\n\tglobal_load_lds_dwordx4 %1, %2" ::"s"(lds), "v"(voff), "s"(sbase) : "memory");
}
DEV unsigned lds_addr(const void* p) { return (unsigned)(uintptr_t)(LAS const void*)p; }
#define ST(i)
#define STR(i)
#define STDECL
#define STDUMP(o, ws, kid)
#define STARG
#define STPASS(i)
#define STDUMP2(ws, kid)
DEV void wait_lgkm0() { asm volatile("s_waitcnt lgkmcnt(0)" ::: "memory"); }
DEV void cfence() { asm volatile("" ::: "memory"); }
DEV float bfr(float x) { return (float)(__bf16)x; }
DEV unsigned pk2(float a, float b) { bf16x2 v = {(__bf16)a, (__bf16)b}; return __builtin_bit_cast(unsigned, v); }
DEV float bf_lo(unsigned u) { return __builtin_bit_cast(float, u << 16); }
DEV float bf_hi(unsigned u) { return __builtin_bit_cast(float, u & 0xffff0000u); }
DEV f32x4 mfma16(bf16x8 a, bf16x8 b, f32x4 c) { return __builtin_amdgcn_mfma_f32_16x16x32_bf16(a, b, c, 0, 0, 0); }
DEV unsigned ft_off(int row, int k, int KS) {
  return (unsigned)(((row >> 4) * KS + (k >> 5)) * 1024 + ((row & 15) + 16 * ((k >> 3) & 3)) * 16 + (k & 7) * 2);
}

DEV void ln_stats(float sum, float sq, float& mean, float& var, float& m, float& s) {
  mean = sum * (1.0f / D_);
  var = fmaxf(sq * (1.0f / D_) - mean * mean, 0.f);
  m = bfr(mean);
  s = bfr(sqrtf(bfr(bfr(var) + 1e-5f)));
}

DEV void dot_sum(float& s, bf16x8 f) {
  const bf16x2 one2 = {(__bf16)1.0f, (__bf16)1.0f};
#pragma unroll
  for (int e = 0; e < 4; ++e) { bf16x2 v = {f[2 * e], f[2 * e + 1]}; s = __builtin_amdgcn_fdot2_f32_bf16(v, one2, s, false); }
}
DEV void dot_sq(float& s, bf16x8 f) {
#pragma unroll
  for (int e = 0; e < 4; ++e) { bf16x2 v = {f[2 * e], f[2 * e + 1]}; s = __builtin_amdgcn_fdot2_f32_bf16(v, v, s, false); }
}
DEV unsigned f2u(float v) { return __builtin_bit_cast(unsigned, v); }
DEV float u2f(unsigned v) { return __builtin_bit_cast(float, v); }
DEV float red_g(float v) {
  const auto r = __builtin_amdgcn_permlane16_swap(f2u(v), f2u(v), false, false);
  v = u2f(r[0]) + u2f(r[1]);
  const auto s = __builtin_amdgcn_permlane32_swap(f2u(v), f2u(v), false, false);
  return u2f(s[0]) + u2f(s[1]);
}
DEV float max_g(float v) {
  const auto r = __builtin_amdgcn_permlane16_swap(f2u(v), f2u(v), false, false);
  v = fmaxf(u2f(r[0]), u2f(r[1]));
  const auto s = __builtin_amdgcn_permlane32_swap(f2u(v), f2u(v), false, false);
  return fmaxf(u2f(s[0]), u2f(s[1]));
}

DEV unsigned wrm_voff(int l, int q, int ldw) {
  return (unsigned)((l >> 4) * ldw + ((((l & 15) >> 1) ^ ((l >> 4) | (q << 2))) * 32) + (l & 1) * 16);
}
DEV bf16x8 wrm_frag(const char* p) {
  s16x4 lo = __builtin_amdgcn_ds_read_tr16_b64_v4i16((LAS s16x4*)(p));
  s16x4 hi = __builtin_amdgcn_ds_read_tr16_b64_v4i16((LAS s16x4*)(p + 1024));
  typedef __attribute__((ext_vector_type(8))) short s16x8;
  s16x8 v = __builtin_shufflevector(lo, hi, 0, 1, 2, 3, 4, 5, 6, 7);
  return __builtin_bit_cast(bf16x8, v);
}
DEV unsigned wrm_lane(int l, int jhi, int jj) {
  const int g = l >> 4, i = l & 15;
  return (unsigned)((8 * g + (i >> 2)) * 256 + ((((jhi ^ (g & 1)) << 2) | (jj ^ (i >> 2))) * 32) + (i & 3) * 8);
}

struct WJob { const char* src; char* dst; const char *t0, *t1, *t2; int ldw, wks, r4, tj; };
DEV WJob make_job(int b, bool live, int tj, const char* wo, const char* w1, const char* w2, const char* w3, const char* w4, char* ws) {
  const char* wsrc; char* wdst; int ldw = D_ * 2, wks = 20, kt, nt;
  if (b < 120) { wsrc = w3; wdst = ws + WS_W3; ldw = F_ * 2; kt = b / 12; nt = b - kt * 12; }
  else { const int u = b - 120; wsrc = w4; wdst = ws + WS_W4; wks = 48; kt = u / 5; nt = u - kt * 5; }
  WJob j;
  j.src = wsrc + nt * 256;
  j.r4 = kt * 16;
  j.dst = live ? wdst + (size_t)(nt * 8 * wks + kt * 2) * 1024 : nullptr;
  j.ldw = ldw; j.wks = wks;
  j.t0 = wo; j.t1 = w1; j.t2 = w2; j.tj = tj;
  return j;
}

template <bool SWAP>
struct GemmQ {
  f32x4 acc[4][4];
  STDECL;
  static constexpr int NG = 4, D = 3, NGRP = 10, SB = 32768, XCH = NG * SB;
  DEV bool run(char* smem, const char* Ab, const char* Wb, int k0, const WJob& job) {
    STR(0); ST(1);
    constexpr int LDA = D_ * 2, LDW = 3 * D_ * 2;
    const int tid = threadIdx.x, l = tid & 63;
    const int wave = __builtin_amdgcn_readfirstlane(tid >> 6);
    if (wave >= 4) {
      const bool isw = wave == 5;
      unsigned vo[2];
#pragma unroll
      for (int q = 0; q < 2; ++q)
        vo[q] = isw ? wrm_voff(l, q, LDW) : (unsigned)((l >> 3) * LDA + (((l & 7) ^ (q * 4 + (l >> 4))) * 16));
      const unsigned vx[4] = {vo[0], isw ? vo[0] : vo[1], isw ? vo[1] : vo[0], vo[1]};
      const unsigned dst0 = lds_addr(smem) + (isw ? 16384 : 0);
      const char* src0 = isw ? Wb : Ab;
      const int gstr = isw ? 64 * LDW : 128, istr = isw ? 4 * LDW : 8 * LDA;
      auto issue = [&](int grp) {
        int kg = grp + k0;
        kg = kg >= NGRP ? kg - NGRP : kg;
        const char* sp = src0 + (size_t)kg * gstr;
#pragma unroll
        for (int i = 0; i < 16; ++i) glds16(sp + (size_t)i * istr, vx[i & 3], dst0 + (grp % NG) * SB + i * 1024);
      };
      static_for<0, D>([&](auto gi) { issue(gi.value); });
      static_for<0, NGRP>([&](auto gi) {
        constexpr int grp = gi.value;
        constexpr int issued = grp + D < NGRP ? grp + D : NGRP;
        wait_vm<16 * (issued - grp - 1) + (grp > NGRP - D ? 16 : 0)>();
        __builtin_amdgcn_s_barrier();
        cfence();
        if constexpr (grp + D < NGRP) issue(grp + D);
        if constexpr (grp == NGRP - D) {
          if (job.dst) {
#pragma unroll
            for (int i = 0; i < 16; ++i)
              glds16(job.src + (size_t)(job.r4 + i) * 4 * job.ldw, wrm_voff(l, (i >> 1) & 1, job.ldw), dst0 + ((grp - 1) % NG) * SB + i * 1024);
          } else {
#pragma unroll
            for (int i = 0; i < 16; ++i) {
              const int u = (i >> 2) * 28 + job.tj;
              const char* p = ((i & 3) == 0 ? job.t0 : (i & 3) == 1 ? job.t1 : job.t2) + (u < 100 && (i & 3) < 3 ? u : 99) * 8192;
              asm volatile("s_mov_b32 m0, %0\n\ts_nop 0\n\tglobal_load_lds_dword %1, %2" ::"s"(dst0 + ((grp - 1) % NG) * SB), "v"(l * 128), "s"(p) : "memory");
            }
          }
        }
      });
      __builtin_amdgcn_s_barrier();
      wait_vm<0>();
      if (job.dst) {
        const char* tb = smem + ((NGRP - D - 1) % NG) * SB + (isw ? 16384 : 0);
#pragma unroll
        for (int kk = 0; kk < 2; ++kk)
#pragma unroll
          for (int j = 0; j < 8; ++j) {
            const bf16x8 f = wrm_frag(tb + kk * 8192 + wrm_lane(l, j >> 2, j & 3));
            *(bf16x8*)(job.dst + (size_t)(j * job.wks + kk) * 1024 + l * 16) = f;
          }
      }
      return false;
    }
    const int wm = wave >> 1, wn = wave & 1, g = l >> 4;
    float rs[2] = {0.f, 0.f}, rq[2] = {0.f, 0.f}, cd[2] = {0.f, 0.f};
#pragma unroll
    for (int i = 0; i < 4; ++i)
#pragma unroll
      for (int j = 0; j < 4; ++j) acc[i][j] = f32x4{0.f, 0.f, 0.f, 0.f};
    const unsigned a0 = (unsigned)((wm * 64 + (l & 15)) * 128 + ((g ^ ((l & 15) >> 1)) * 16));
    const char* al[2][2] = {{smem + a0 + wn * 4096, smem + a0 + 4096 - wn * 4096},
                            {smem + (a0 ^ 64u) + wn * 4096, smem + (a0 ^ 64u) + 4096 - wn * 4096}};
    const char* wl[4];
#pragma unroll
    for (int j = 0; j < 4; ++j) wl[j] = smem + 16384 + wrm_lane(l, wn, j ^ (2 * wm));
    bf16x8 af[2][4], wf[2][4];
    auto ldfrag = [&](auto ti) {
      constexpr int t = ti.value, kk = t & 1, so = ((t >> 1) % NG) * SB;
#pragma unroll
      for (int i = 0; i < 4; ++i) af[kk][i] = *(const bf16x8*)(al[kk][i >> 1] + so + (i & 1) * 2048);
#pragma unroll
      for (int j = 0; j < 4; ++j) wf[kk][j] = wrm_frag(wl[j] + so + kk * 8192);
    };
    __builtin_amdgcn_s_barrier();
    cfence();
    ST(3);
    ldfrag(std::integral_constant<int, 0>{});
    static_for<0, 2 * NGRP>([&](auto ti) {
      constexpr int t = ti.value, kk = t & 1;
      if constexpr (t + 1 < 2 * NGRP) {
        if constexpr (kk == 1) {
          wait_lgkm0();
          __builtin_amdgcn_s_barrier();
          cfence();
          ST(3 + (t + 1) / 2);
        }
        ldfrag(std::integral_constant<int, t + 1>{});
      }
      __builtin_amdgcn_sched_barrier(0);
#pragma unroll
      for (int i = 0; i < 4; ++i)
#pragma unroll
        for (int j = 0; j < 4; ++j)
          acc[i][j] = SWAP ? mfma16(wf[kk][j], af[kk][i], acc[i][j]) : mfma16(af[kk][i], wf[kk][j], acc[i][j]);
#pragma unroll
      for (int i = 0; i < 2; ++i) { dot_sum(rs[i], af[kk][i]); dot_sq(rq[i], af[kk][i]); dot_sum(cd[i], wf[kk][i]); }
#pragma unroll
      for (int q = 0; q < 8; ++q) {
        __builtin_amdgcn_sched_group_barrier(0x8, 2, 0);
        __builtin_amdgcn_sched_group_barrier(0x2, 3, 0);
      }
      asm volatile("" : "+v"(rs[0]), "+v"(rs[1]), "+v"(rq[0]), "+v"(rq[1]), "+v"(cd[0]), "+v"(cd[1]));
      __builtin_amdgcn_sched_barrier(0);
    });
    ST(21);
    float2* xs = (float2*)(smem + XCH);
    float* xc = (float*)(smem + XCH + 1024);
#pragma unroll
    for (int i = 0; i < 2; ++i) {
      const float a = red_g(rs[i]), b = red_g(rq[i]), c = red_g(cd[i]);
      float mean, var, m, sd;
      ln_stats(a, b, mean, var, m, sd);
      const float rinv = 1.0f / sd;
      if (l < 16) { xs[(wm * 4 + wn * 2 + i) * 16 + l] = float2{rinv, -m * rinv}; xc[(wn * 4 + wm * 2 + i) * 16 + l] = c; }
    }
    wait_lgkm0();
    __builtin_amdgcn_s_barrier();
    cfence();
    return true;
  }
};

template <bool ISV>
DEV void qkv_body(char* smem, const char* x, const char* wqkv, char* ws, int rt, int ct, int k0, const WJob& job) {
  using GQ = GemmQ<!ISV>;
  GQ gm;
  const int row0 = rt * 128, n0 = ct * 128;
  if (!gm.run(smem, x + (size_t)row0 * (D_ * 2), wqkv + n0 * 2, k0, job)) return;
  const int tid = threadIdx.x, l = tid & 63, wave = tid >> 6, wm = wave >> 1, wn = wave & 1, g = l >> 4;
  const float2* xs = (const float2*)(smem + GQ::XCH);
  const float* xc = (const float*)(smem + GQ::XCH + 1024);
  if constexpr (!ISV) {
    f32x4 cs[4];
    unsigned po[4];
#pragma unroll
    for (int j = 0; j < 4; ++j) {
      const int ja = wn * 4 + (j ^ (2 * wm));
      cs[j] = *(const f32x4*)(xc + ja * 16 + 4 * g);
      const int c = (n0 + ja * 16 + 4 * g) % D_, head = c / 20, d0 = c - head * 20;
      po[j] = (unsigned)(head * (int)QK_HEAD + (d0 >> 3) * 256 + (d0 & 7) * 2) | (d0 == 16 ? 1u : 0u);
    }
    char* dstb = ws + (ct < 5 ? WS_Q : WS_K) + (size_t)(row0 >> 4) * 768 + (l & 15) * 16;
#pragma unroll
    for (int i = 0; i < 4; ++i) {
      const int ia = wm * 4 + (i ^ (2 * wn));
      const float2 st = xs[ia * 16 + (l & 15)];
#pragma unroll
      for (int j = 0; j < 4; ++j) {
        const f32x4 y = gm.acc[i][j] * st.x + cs[j] * st.y;
        char* p = dstb + ia * 768 + (po[j] & ~1u);
        if (po[j] & 1u) *(u32x4*)p = u32x4{pk2(y[0], y[1]), pk2(y[2], y[3]), 0u, 0u};
        else *(u32x2*)p = u32x2{pk2(y[0], y[1]), pk2(y[2], y[3])};
      }
    }
  } else {
    float cd[4];
    unsigned po[4];
#pragma unroll
    for (int j = 0; j < 4; ++j) {
      const int ja = wn * 4 + (j ^ (2 * wm));
      cd[j] = xc[ja * 16 + (l & 15)];
      const int c = n0 - 2 * D_ + ja * 16 + (l & 15), head = c / 20, d = c - head * 20;
      po[j] = (unsigned)(head * (int)V_HEAD + (d < 16 ? (g * 16 + d) * 16 : 1024 + (g * 4 + d - 16) * 16));
    }
    char* dstb = ws + WS_V + (size_t)(row0 >> 5) * 1280;
#pragma unroll
    for (int i = 0; i < 4; ++i) {
      const int ia = wm * 4 + (i ^ (2 * wn));
      const f32x4 s01 = *(const f32x4*)(xs + ia * 16 + 4 * g), s23 = *(const f32x4*)(xs + ia * 16 + 4 * g + 2);
      const f32x4 rr = {s01[0], s01[2], s23[0], s23[2]}, nm = {s01[1], s01[3], s23[1], s23[3]};
#pragma unroll
      for (int j = 0; j < 4; ++j) {
        const f32x4 y = gm.acc[i][j] * rr + nm * cd[j];
        *(u32x2*)(dstb + (ia >> 1) * 1280 + (i & 1) * 8 + po[j]) = u32x2{pk2(y[0], y[1]), pk2(y[2], y[3])};
      }
    }
  }
  STDUMP(gm, ws, 1);
}

DEV void colsum_job(char* smem, const char* w1, const char* w3, float* cs, int blk) {
  const int tid = threadIdx.x, c = tid % 17, rg = tid / 17;
  const int gc = blk * 17 + c;
  const char* base = gc < 80 ? w1 + gc * 16 : w3 + (gc - 80) * 16;
  const int ld = gc < 80 ? D_ * 2 : F_ * 2;
  float a[8] = {0.f, 0.f, 0.f, 0.f, 0.f, 0.f, 0.f, 0.f};
  if (rg < 20) {
#pragma unroll 16
    for (int r = rg; r < D_; r += 20) {
      u32x4 v = *(const u32x4*)(base + (size_t)r * ld);
#pragma unroll
      for (int e = 0; e < 4; ++e) { a[2 * e] += bf_lo(v[e]); a[2 * e + 1] += bf_hi(v[e]); }
    }
  }
  float* sm = (float*)smem;
  if (rg < 20) {
#pragma unroll
    for (int e = 0; e < 8; ++e) sm[rg * 136 + c * 8 + e] = a[e];
  }
  __syncthreads();
  if (tid < 136) {
    float t = 0.f;
#pragma unroll
    for (int r = 0; r < 20; ++r) t += sm[r * 136 + tid];
    cs[blk * 136 + tid] = t;
  }
}

__global__ __launch_bounds__(384) void k1_qkv(const char* x, const char* wqkv, const char* wo, const char* w1, const char* w2,
                                              const char* w3, const char* w4, char* ws) {
  extern __shared__ __attribute__((aligned(16))) char smem[];
  const int b = blockIdx.x, xcd = b & 7, j = b >> 3, r = j & 3, c = j >> 2;
  {
    const u32x4 e = {EMPTY, EMPTY, EMPTY, EMPTY};
    const int t = threadIdx.x, bt = b >> 4;
    *(u32x4*)(ws + WS_O + b * 10240 + t * 16) = e;
    if (t < 256) *(u32x4*)(ws + WS_O + b * 10240 + 6144 + t * 16) = e;
    else if (t < 336) *(u32x4*)(ws + WS_G + ((bt >> 1) * 16 + (bt & 1) * 15) * 20480 + (b & 15) * 1280 + (t - 256) * 16) = e;
  }
  if ((xcd & 1) && c == 7) { colsum_job(smem, w1, w3, (float*)(ws + WS_CS), (xcd >> 1) * 4 + r); return; }
  const int rt = (xcd >> 1) * 4 + r, ct = (xcd & 1) * 8 + c;
  const int wave = __builtin_amdgcn_readfirstlane(threadIdx.x >> 6);
  const WJob job = make_job(rt * 15 + ct, wave == 4, j < 28 ? j : 99, wo, w1, w2, w3, w4, ws);
  const int k0 = (3 * r + 4 * c) % 10;
  if (ct >= 10) qkv_body<true>(smem, x, wqkv, ws, rt, ct, k0, job);
  else qkv_body<false>(smem, x, wqkv, ws, rt, ct, k0, job);
}

typedef __attribute__((ext_vector_type(2))) float f32x2;
typedef unsigned long long u64;
DEV f32x2 unpk(unsigned u) { return f32x2{bf_lo(u), bf_hi(u)}; }
DEV unsigned pk2v(f32x2 v) { return pk2(v[0], v[1]); }
DEV f32x2 bfr2(f32x2 v) { unsigned u = pk2v(v); asm("" : "+v"(u)); return unpk(u); }
DEV void ln_fast(float sum, float sq, float& mean, float& var, float& m, float& s) {
  mean = sum * (1.0f / D_);
  var = fmaxf(sq * (1.0f / D_) - mean * mean, 0.f);
  const f32x2 r = bfr2(f32x2{mean, var});
  m = r[0];
  s = bfr(__builtin_amdgcn_sqrtf(bfr(r[1] + 1e-5f)));
}
DEV f32x2 gelu2(f32x2 x) {
  const float c0 = -2.0f * 0.7978845608028654f * 1.4426950408889634f, c1 = c0 * 0.044715f;
  const f32x2 z = x * (x * x * c1 + c0);
  const f32x2 d = f32x2{__builtin_amdgcn_exp2f(z[0]), __builtin_amdgcn_exp2f(z[1])} + 1.0f;
  return x * f32x2{__builtin_amdgcn_rcpf(d[0]), __builtin_amdgcn_rcpf(d[1])};
}
DEV f32x2 lo2(f32x4 v) { return f32x2{v[0], v[1]}; }
DEV f32x2 hi2(f32x4 v) { return f32x2{v[2], v[3]}; }

typedef __amdgpu_buffer_rsrc_t rsrc_t;
constexpr unsigned OOB = 0x40000000u;
DEV unsigned full(unsigned v) { return v < EMPTY ? v : EMPTY - 1; }
DEV void st8(rsrc_t r, unsigned v, unsigned s, u32x2 d) { __builtin_amdgcn_raw_buffer_store_b64(u32x2{full(d[0]), full(d[1])}, r, v, s, 16); }
template <int AUX> DEV void st16r(rsrc_t r, unsigned v, unsigned s, u32x4 d) {
  asm("" : "+v"(d));
  __builtin_amdgcn_raw_buffer_store_b128(d, r, v, s, AUX);
  asm volatile("s_nop 1" ::"v"(d) : "memory");
}
DEV void st16(rsrc_t r, unsigned v, unsigned s, u32x4 d) { st16r<16>(r, v, s, u32x4{full(d[0]), full(d[1]), full(d[2]), full(d[3])}); }
DEV void st_empty(rsrc_t r, unsigned v, unsigned s) { st16r<16>(r, v, s, u32x4{EMPTY, EMPTY, EMPTY, EMPTY}); }
DEV u32x4 ld16(rsrc_t r, unsigned v, unsigned s) { return __builtin_amdgcn_raw_buffer_load_b128(r, v, s, 16); }
DEV u32x2 ld8(rsrc_t r, unsigned v, unsigned s) { return __builtin_amdgcn_raw_buffer_load_b64(r, v, s, 16); }
DEV u32x4 ld16p(rsrc_t r, unsigned v, unsigned s) { return __builtin_amdgcn_raw_buffer_load_b128(r, v, s, 0); }
DEV void vm0() { __builtin_amdgcn_s_waitcnt(0x0f70); cfence(); }

constexpr int LDS_TOT = 163840;
constexpr int AT_Q = 0, AT_K = AT_Q + 16 * 768, AT_V = AT_K + 32 * 768;
constexpr int SLOT = 25600, SLOT7 = 43008;
constexpr int L_W = 51200, L_W2 = 57344;
constexpr int L_SX = 159744;
constexpr int L_SY = 161792;
constexpr int C_P = L_SY, C_S = L_SY + 128, C_F = L_SY + 256, C_W2 = L_SY + 384, C_W4 = L_SY + 388, C_WC = L_SY + 392, C_OK = L_SY + 396, C_WL = L_SY + 400;
constexpr int L_LN = L_SY + 512;
constexpr int L_DUM = L_SY + 1024;
DEV void touch(const char* p, int l, unsigned lb) {
  asm volatile("s_mov_b32 m0, %0\n\ts_nop 0\n\tglobal_load_lds_dword %1, %2" ::"s"(lb + L_DUM + 512), "v"(l * 128), "s"(p) : "memory");
}
DEV void lds_inc(unsigned a) {
  asm volatile("s_mov_b64 exec, 1\n\tds_add_u32 %0, %1\n\ts_mov_b64 exec, -1" ::"v"(a), "v"(1u) : "memory");
}
DEV void lds_wait(unsigned a, unsigned target) {
  for (int it = 0; it < (1 << 22); ++it) {
    const unsigned v = __builtin_amdgcn_readfirstlane(*(volatile LAS unsigned*)(uintptr_t)a);
    if (v >= target) break;
  }
  cfence();
}
DEV f32x4 lds_ld4(unsigned a) { return *(const LAS f32x4*)(uintptr_t)a; }
DEV bf16x8 tr_frag(unsigned a) {
  typedef __attribute__((ext_vector_type(8))) short s16x8;
  const s16x4 lo = __builtin_amdgcn_ds_read_tr16_b64_v4i16((LAS s16x4*)(uintptr_t)a);
  const s16x4 hi = __builtin_amdgcn_ds_read_tr16_b64_v4i16((LAS s16x4*)(uintptr_t)(a + 640));
  return __builtin_bit_cast(bf16x8, __builtin_shufflevector(lo, hi, 0, 1, 2, 3, 4, 5, 6, 7));
}
DEV unsigned max3u(unsigned a, unsigned b, unsigned c) { a = a > b ? a : b; return a > c ? a : c; }

template <int KW, int MODE>
DEV void m_issue(u32x4 (&a)[6], rsrc_t wr, int l, unsigned s, unsigned v) {
#pragma unroll
  for (int k = 0; k < (MODE == 1 ? KW - 1 : KW); ++k) a[k] = ld16(wr, v + k * 1024, s);
  if constexpr (MODE == 1) {
    const unsigned t = (l >> 4) * 256 + (l & 15) * 8 + 4096;
    const u32x2 t0 = ld8(wr, t, s), t1 = ld8(wr, t + 128, s);
    a[KW - 1] = u32x4{t0[0], t0[1], t1[0], t1[1]};
  }
  cfence();
}
template <int NT, int KW, int NTILE, int MODE, int NB, int PRE, class TS, class TV, class TG, class TL, class TH>
DEV void m_phase(const bf16x8 (&W)[30], u32x4 (&a)[3][6], int l, unsigned lb, rsrc_t wr, int n0, int part, TS&& so, TV&& vo, TG&& guard,
                 TL&& slot, TH&& hook STARG) {
  constexpr int RS = NT * 64 + 16;
  const int g = l >> 4, li = l & 15;
  const unsigned pw = lb + part * (16 * RS) + li * RS + g * 16;
  const bool dg = g == (li >> 2);
  const unsigned sx = dg ? lb + L_SX + li * 32 + part * 8 : lb + L_DUM + l * 8, sm = dg ? 512u : 0u;
  static_for<PRE, NB - 1>([&](auto i) { m_issue<KW, MODE>(a[i.value], wr, l, so(i), vo(i)); });
  static_for<0, NTILE>([&](auto i) {
    constexpr int B = i.value % NB;
    const int n = n0 + i;
    auto bad = [&]() {
      unsigned m = 0;
#pragma unroll
      for (int k = 0; k < KW; ++k) m = max3u(max3u(m, a[B][k][0], a[B][k][1]), a[B][k][2], a[B][k][3]);
      return __builtin_amdgcn_ballot_w64(m == EMPTY) != 0;
    };
    if (bad()) {
      int it = 0;
#pragma unroll 1
      do { m_issue<KW, MODE>(a[B], wr, l, so(i), vo(i)); vm0(); } while (bad() && ++it < (1 << 16));
    }
    cfence();
    if (i == 0) { ST(STI); }
    const f32x4 z4 = {0.f, 0.f, 0.f, 0.f};
    f32x4 sa = z4, qa = z4;
    if constexpr (MODE == 2) {
      const u32x4 one = {0x3F803F80u, 0x3F803F80u, 0x3F803F80u, 0x3F803F80u};
#pragma unroll
      for (int k = 0; k < KW; ++k) {
        const bf16x8 f = __builtin_bit_cast(bf16x8, a[B][k]);
        qa = mfma16(f, f, qa); sa = mfma16(__builtin_bit_cast(bf16x8, one), f, sa);
      }
      __builtin_amdgcn_sched_barrier(0);
    }
    if constexpr (i.value + NB - 1 < NTILE) {
      constexpr std::integral_constant<int, i.value + NB - 1> j{};
      m_issue<KW, MODE>(a[j.value % NB], wr, l, so(j), vo(j));
    }
    const unsigned ga = guard(i);
    const unsigned gv = *(volatile LAS unsigned*)(uintptr_t)ga;
    __builtin_amdgcn_sched_barrier(0);
    f32x4 acc[NT];
#pragma unroll
    for (int j = 0; j < NT; ++j) acc[j] = mfma16(W[j], __builtin_bit_cast(bf16x8, a[B][0]), z4);
    if constexpr (MODE == 2) {
      const float q01 = (li & 1) ? qa[1] : qa[0], q23 = (li & 1) ? qa[3] : qa[2];
      *(LAS f32x2*)(uintptr_t)(sx + (i.value & 3) * sm) = f32x2{sa[0], (li & 2) ? q23 : q01};
      lds_inc(lb + C_S + n * 4);
      __builtin_amdgcn_sched_barrier(0);
    }
#pragma unroll
    for (int k = 1; k < KW; ++k)
#pragma unroll
      for (int j = 0; j < NT; ++j) acc[j] = mfma16(W[k * NT + j], __builtin_bit_cast(bf16x8, a[B][k]), acc[j]);
    __builtin_amdgcn_sched_barrier(0);
    if (__builtin_amdgcn_readfirstlane(gv) < 4u) lds_wait(ga, 4);
    const unsigned sl = slot(i);
#pragma unroll
    for (int j = 0; j < NT; ++j) *(LAS f32x4*)(uintptr_t)(pw + sl + j * 64) = acc[j];
    lds_inc(lb + C_P + n * 4);
    hook(i);
  });
  ST(STI + 1);
}

DEV unsigned lds_cnt(unsigned a) { const unsigned v = *(volatile LAS unsigned*)(uintptr_t)a; cfence(); return v; }
template <int NP, int RS>
DEV void e_sum(f32x4& lo, f32x4& hi, unsigned lb, unsigned a, int n) {
  f32x4 p[NP][2];
#pragma unroll 1
  for (int it = 0;; ++it) {
    const unsigned c = lds_cnt(lb + C_P + n * 4);
#pragma unroll
    for (int q = 0; q < NP; ++q) { p[q][0] = lds_ld4(a + q * (16 * RS)); p[q][1] = lds_ld4(a + q * (16 * RS) + 16); }
    if (__builtin_amdgcn_readfirstlane(c) >= (unsigned)NP || it) break;
    lds_wait(lb + C_P + n * 4, NP);
  }
  lds_inc(lb + C_F + n * 4);
#pragma unroll
  for (int s = 1; s < NP; s *= 2)
#pragma unroll
    for (int q = 0; q < NP; q += 2 * s) { p[q][0] += p[q + s][0]; p[q][1] += p[q + s][1]; }
  lo = p[0][0]; hi = p[0][1];
}
struct StatRd { unsigned c; f32x4 a, b; };
DEV StatRd e_stat0(unsigned lb, int n, int li) {
  StatRd r;
  r.c = lds_cnt(lb + C_S + n * 4);
  r.a = lds_ld4(lb + L_SX + (n & 3) * 512 + li * 32); r.b = lds_ld4(lb + L_SX + (n & 3) * 512 + li * 32 + 16);
  cfence();
  return r;
}
DEV void e_stat1(StatRd r, float& sum, float& sq, unsigned lb, int n, int li) {
  if (__builtin_amdgcn_readfirstlane(r.c) < 4u) { lds_wait(lb + C_S + n * 4, 4); r = e_stat0(lb, n, li); }
  const f32x4 t = r.a + r.b;
  sum = t[0] + t[2]; sq = t[1] + t[3];
}

template <int I, class TH>
DEV void attention(char* smem, int xcd, int w, u32x2& om, u32x2& ot, TH&& hook) {
  const int l = threadIdx.x & 63, g = l >> 4, li = l & 15;
  u32x4 qv = *(const u32x4*)(smem + AT_Q + (2 * w + I) * 768 + (g < 3 ? g : 2) * 256 + li * 16);
  if (g == 3) qv = u32x4{0u, 0u, 0u, 0u};
  const bf16x8 qf = __builtin_bit_cast(bf16x8, qv);
  const char* Ks = smem + AT_K + (2 * w + I) * 768 + (g < 3 ? g : 2) * 256 + li * 16;
  const int kbase = xcd * 256 + 32 * w + 16 * I - 256;
  f32x4 s0[17];
  const f32x4 z4 = {0.f, 0.f, 0.f, 0.f};
#pragma unroll
  for (int c = 0; c < 17; ++c) s0[c] = mfma16(*(const bf16x8*)(Ks + c * 768), qf, z4);
  const float NEG = -__builtin_inff();
  hook(std::integral_constant<int, 5 * I>{});
#pragma unroll
  for (int r = 0; r < 4; ++r) {
    const int kj = 4 * g + r;
    if (kj <= li) s0[0][r] = NEG;
    if (kj > li) s0[16][r] = NEG;
  }
  if (kbase < 0) {
#pragma unroll
    for (int c = 0; c < 17; ++c)
      if (kbase + 16 * c < 0) s0[c] = f32x4{NEG, NEG, NEG, NEG};
  }
  float m0 = NEG, m1 = NEG;
#pragma unroll
  for (int c = 0; c < 17; ++c) {
    m0 = fmaxf(fmaxf(m0, s0[c][0]), s0[c][1]);
    m1 = fmaxf(fmaxf(m1, s0[c][2]), s0[c][3]);
  }
  m0 = max_g(fmaxf(m0, m1));
  const float C2 = 0.22360679774997896f * 1.4426950408889634f;
  const float b0 = -m0 * C2;
  u32x2 p0[18];
  static_for<0, 17>([&](auto ci) {
    constexpr int c = ci.value;
    if constexpr (c % 5 == 0) hook(std::integral_constant<int, 5 * I + 1 + c / 5>{});
    const f32x4 e = s0[c] * C2 + b0;
    p0[c + I] = u32x2{pk2(__builtin_amdgcn_exp2f(e[0]), __builtin_amdgcn_exp2f(e[1])), pk2(__builtin_amdgcn_exp2f(e[2]), __builtin_amdgcn_exp2f(e[3]))};
  });
  p0[I ? 0 : 17] = u32x2{0u, 0u};
  const char* Vs0 = smem + AT_V + w * 1280 + (g * 16 + li) * 16;
  const char* Vs1 = smem + AT_V + w * 1280 + 1024 + (g * 4 + (li & 3)) * 16;
  f32x4 o0 = z4, o1 = z4;
#pragma unroll
  for (int s = 0; s < 9; ++s) {
    bf16x8 v0 = *(const bf16x8*)(Vs0 + s * 1280);
    u32x4 v1u = *(const u32x4*)(Vs1 + s * 1280);
    if (li == 4) v1u = u32x4{0x3F803F80u, 0x3F803F80u, 0x3F803F80u, 0x3F803F80u};
    const u32x4 pa = {p0[2 * s][0], p0[2 * s][1], p0[2 * s + 1][0], p0[2 * s + 1][1]};
    const bf16x8 pf = __builtin_bit_cast(bf16x8, pa);
    o0 = mfma16(v0, pf, o0); o1 = mfma16(__builtin_bit_cast(bf16x8, v1u), pf, o1);
  }
  const float r0 = 1.0f / __shfl(o1[0], 16 + li);
  om = u32x2{pk2(o0[0] * r0, o0[1] * r0), pk2(o0[2] * r0, o0[3] * r0)};
  ot = u32x2{pk2(o1[0] * r0, o1[1] * r0), pk2(o1[2] * r0, o1[3] * r0)};
}

#define CTX \
  int tid_ = threadIdx.x, b_ = blockIdx.x; \
  asm volatile("" : "+v"(tid_)); asm volatile("" : "+s"(b_)); \
  const int xcd = b_ & 7, jj = b_ >> 3, l = tid_ & 63, g = l >> 4, li = l & 15; \
  const int w = __builtin_amdgcn_readfirstlane(tid_ >> 6), q = w & 3, grp = w >> 2; \
  const int ct = jj & 7, ct16 = jj & 15; \
  const unsigned vl = l * 16; \
  const int G4 = (xcd * 4 + (jj >> 3)) * 4, dir = (jj >> 3) & 1; \
  const int B8 = (xcd * 2 + (jj >> 4)) * 8; \
  auto g4 = [&](int i) { return G4 + (dir ? 3 - i : i); }; \
  auto g8 = [&](int i) { return B8 + ((i & 1) ? 7 - (i >> 1) : (i >> 1)); }; \
  auto lane = [&](int) { return vl; }; \
  auto ring = [&](int n) { return lb + (n >= 2 ? C_F + (n - 2) * 4 : C_OK); }; \
  auto slot2 = [&](int i) { return (unsigned)((i & 1) * SLOT); }; \
  const int wid = jj * 4 + q; \
  auto nohook = [](int) {}; \
  const int nu = q < 2 ? 3 : 2, un = (q < 2 ? 3 * q : 2 * q + 2) + (g < nu ? g : nu - 1); \
  const bool act = g < nu; \
  const int ko = ct * 10 + un; \
  const unsigned vft = (ko >> 2) * 1024 + (ko & 3) * 256 + li * 16, vst = act ? vft : OOB; \
  const unsigned pe = lb + li * 336 + un * 32; \
  const unsigned xo = li * (D_ * 2) + ko * 16; \
  const int k6 = ct16 * 12 + 3 * q + (g < 3 ? g : 2); \
  const unsigned v6 = g < 3 ? (unsigned)((k6 >> 2) * 1024 + (k6 & 3) * 256 + li * 16) : OOB; \
  const unsigned trl = lb + L_W2 + (8 * g + (li >> 2)) * 160 + (li & 3) * 8; \
  const unsigned w4b = (unsigned)WS_W4 + ct * (5 * 48 * 1024) + w * 6144; \
  (void)0

__global__ __launch_bounds__(512) void kp_main(const char* x, const char* wo, const char* w1, const char* w2, char* ws, char* out) {
  extern __shared__ __attribute__((aligned(16))) char smem[];
  STDECL;
  STR(0); ST(1);
  const unsigned lb = lds_addr(smem);
  const rsrc_t wr = __builtin_amdgcn_make_buffer_rsrc(ws, 0, (int)WS_END, 0x00020000);
  bf16x8 W[30];
  u32x4 a[3][6];
  int ga, gb;
  {
    CTX;
#pragma unroll
    for (int i = 0; i < 4; ++i) {
      st_empty(wr, vst, (unsigned)(grp ? WS_H : WS_G) + (G4 + i) * 20480);
      st_empty(wr, grp ? vst : OOB, (unsigned)WS_X1 + (G4 + i) * 20480);
    }
    const unsigned qo = (unsigned)WS_Q + jj * (unsigned)QK_HEAD + xcd * 16 * 768;
    const unsigned ko0 = (unsigned)WS_K + jj * (unsigned)QK_HEAD, vo0 = (unsigned)WS_V + jj * (unsigned)V_HEAD;
    const int koff0 = (xcd * 16 - 16) * 768, voff0 = (xcd * 8 - 8) * 1280;
#pragma unroll
    for (int i = 0; i < 7; ++i) {
      const int p = w + 8 * i, ok = koff0 + (p - 12) * 1024, ov = voff0 + (p - 36) * 1024;
      const unsigned src = p < 12 ? qo + p * 1024 : p < 36 ? ko0 + (ok < 0 ? 0 : ok) : vo0 + (ov < 0 ? 0 : ov);
      glds16(ws + src, vl, lb + AT_Q + p * 1024);
    }
    *(LAS unsigned*)(uintptr_t)(lb + L_SY + (tid_ & 127) * 4) = (tid_ & 127) == (C_OK - L_SY) / 4 ? 4u : 0u;
    auto rowoff = [&](int i, int mode) {
      const int c = 64 * i + l, r = (c * 205) >> 11, cc = c - 10 * r;
      const int row = mode == 0 ? r : mode == 1 ? r + 4 * (r >> 4) : (r >> 2) * 20 + 16 + (r & 3);
      return (unsigned)(row * (D_ * 2) + cc * 16);
    };
    const char* wsrc = (grp ? w1 : wo) + (unsigned)(q * 160 * (D_ * 2) + ct * 160);
    auto stage = [&](int t, int slot) {
#pragma unroll
      for (int i = 0; i < 5; ++i)
        glds16(wsrc + (unsigned)((grp ? 32 * t : t < 4 ? 40 * t : 0) * (D_ * 2)), grp ? rowoff(i, 0) : rowoff(i, t < 4 ? 1 : 2), lb + L_W2 + slot * 5120 + i * 1024);
    };
    auto stage2 = [&](int t) {
#pragma unroll
      for (int i = 0; i < 5; ++i) glds16(w2 + (unsigned)(32 * t * (D_ * 2) + ct * 160), rowoff(i, 0), lb + L_W2 + t * 5120 + i * 1024);
    };
    auto fetch = [&](int t, int slot) {
#pragma unroll
      for (int j = 0; j < 5; ++j) W[t * 5 + j] = tr_frag(trl + slot * 5120 + j * 32);
    };
    stage(0, 2 * w); stage(1, 2 * w + 1);
    if (!grp) stage(4, 16 + q);
    wait_vm<0>();
    __builtin_amdgcn_s_barrier();
    cfence();
    ST(2);
    const int tu = jj * 8 + w;
    auto feed = [&](auto hi) {
      constexpr int h = hi.value;
      __builtin_amdgcn_sched_barrier(0);
      if constexpr (h == 0) {
        fetch(0, 2 * w); fetch(1, 2 * w + 1);
        if (!grp) fetch(4, 16 + q);
        wait_lgkm0();
        __builtin_amdgcn_s_barrier();
        if (grp) stage(4, 16 + q);
        stage(2, 2 * w); stage(3, 2 * w + 1);
        touch(tu < 40 ? x + (unsigned)(xcd * 327680 + tu * 8192) : ws + WS_W3 + (unsigned)((tu - 40) * 8192), l, lb);
      }
      if constexpr (h == 2) {
        wait_vm<1>();
        fetch(2, 2 * w); fetch(3, 2 * w + 1);
        if (grp) fetch(4, 16 + q);
        wait_lgkm0();
        stage2(2 * w);
      }
      if constexpr (h == 4) stage2(2 * w + 1);
      if constexpr (h == 5) { if (grp) stage2(16 + q); }
      if constexpr (h == 6) {
        const int u = tu < 144 ? tu - 24 : 119;
        touch(tu < 24 ? ws + WS_W3 + (unsigned)((216 + tu) * 8192) : ws + WS_W4 + (unsigned)((u / 3) * 49152 + 24576 + (u % 3) * 8192), l, lb);
      }
      cfence();
      __builtin_amdgcn_sched_barrier(0);
    };
    u32x2 om0, ot0, om1, ot1;
    attention<0>(smem, xcd, w, om0, ot0, feed);
    __builtin_amdgcn_sched_barrier(0);
    attention<1>(smem, xcd, w, om1, ot1, feed);
    {
      const auto s0 = __builtin_amdgcn_permlane16_swap(om0[0], om1[0], false, false);
      const auto s1 = __builtin_amdgcn_permlane16_swap(om0[1], om1[1], false, false);
      const auto t0 = __builtin_amdgcn_permlane16_swap(ot0[0], ot1[0], false, false);
      const auto t1 = __builtin_amdgcn_permlane16_swap(ot0[1], ot1[1], false, false);
      const unsigned Of = (unsigned)WS_O + (xcd * 16 + 2 * w) * 20480 + (jj >> 3) * 5120;
      st16(wr, (g & 1) * 20480 + (g >> 1) * 256 + li * 16, Of + (jj & 7) * 512, u32x4{s0[0], s1[0], s0[1], s1[1]});
      st8(wr, g < 2 ? g * 20480 + li * 8 : OOB, Of + 4096 + (jj & 7) * 128, u32x2{t0[0], t1[0]});
    }
    ST(3);
    wait_vm<3>();
    __builtin_amdgcn_s_barrier();
    cfence();
    ga = grp; gb = grp;
    asm volatile("" : "+s"(ga), "+s"(gb));
  }
  if (ga == 0) {
    CTX;
#pragma unroll
    for (int i = 0; i < 8; ++i) st_empty(wr, v6, (unsigned)WS_U + (B8 + i) * 49152);
    f32x4 cs[2];
#pragma unroll
    for (int i = 0; i < 2; ++i) cs[i] = __builtin_bit_cast(f32x4, ld16p(wr, ko * 32 + i * 16, (unsigned)WS_CS));
    m_phase<5, 5, 4, 1, 3, 0>(W, a, l, lb, wr, 0, q, [&](int i) { return (unsigned)WS_O + g4(i) * 20480 + q * 5120; }, lane, ring, slot2,
                              nohook STPASS(4));
#pragma unroll
    for (int f = 0; f < 25; ++f) W[f] = tr_frag(trl + (q * 5 + f / 5) * 5120 + (f % 5) * 32);
    lds_inc(lb + C_W2);
    lds_wait(lb + C_W2, 4);
    auto stage4 = [&](int f) { glds16(ws + (w4b + 4 * 6144 + (f % 5) * (48 * 1024) + (f / 5) * 1024), vl, lb + L_W + (q * 26 + f) * 1024); };
#pragma unroll
    for (int f = 0; f < 8; ++f) stage4(f);
    const int sr = li - ((q & 2) ? ((q & 1) ? -2 : 2) : ((q & 1) ? -1 : 1));
    const unsigned vg = (unsigned)(((sr >> 4) + 1) * 20480 + g * 256 + (sr & 15) * 16);
    auto sog = [&](int i) { return (unsigned)WS_G + (g4(i) - 1) * 20480 + q * 5120; };
    auto vog = [&](int i) { return (unsigned)(g4(i) * 16 + sr) < (unsigned)S_ ? vg : OOB; };
    {
      StatRd sd = e_stat0(lb, 4, li);
      static_for<0, 4>([&](auto i) {
        constexpr int n = 4 + i.value;
        float sum, sq, mean, var, m1, s1;
        e_stat1(sd, sum, sq, lb, n, li);
        ln_fast(sum, sq, mean, var, m1, s1);
        const float r1 = __builtin_amdgcn_rcpf(s1);
        const f32x2 mv = bfr2(f32x2{(mean - m1) * r1, var * r1 * r1});
        const float s2 = bfr(__builtin_amdgcn_sqrtf(bfr(mv[1] + 1e-5f)));
        float rr = r1 * __builtin_amdgcn_rcpf(s2);
        const float nm = -(m1 + mv[0] * s1) * rr;
        if (w == 0) *(LAS f32x2*)(uintptr_t)(lb + L_LN + (i * 16 + li) * 8) = f32x2{m1, r1};
        f32x4 c0 = cs[0] * nm, c1 = cs[1] * nm;
        asm volatile("" : "+v"(rr), "+v"(c0), "+v"(c1));
        f32x4 lo, hi;
        e_sum<4, 336>(lo, hi, lb, pe + (n & 1) * SLOT, n);
        if constexpr (i.value < 3) sd = e_stat0(lb, n + 1, li);
        if (i == 0) { ST(6); }
        ST(14 + 2 * i.value);
        lo = lo * rr + c0; hi = hi * rr + c1;
        st16(wr, vst, (unsigned)WS_G + g4(i) * 20480,
             u32x4{pk2v(gelu2(bfr2(lo2(lo)))), pk2v(gelu2(bfr2(hi2(lo)))), pk2v(gelu2(bfr2(lo2(hi)))), pk2v(gelu2(bfr2(hi2(hi))))});
        if constexpr (i.value < 3) {
#pragma unroll
          for (int f = 0; f < 6; ++f) stage4(8 + 6 * i + f);
        }
        if constexpr (i.value == 2) m_issue<5, 0>(a[0], wr, l, sog(0), vog(0));
        ST(15 + 2 * i.value);
      });
    }
    ST(7);
#pragma unroll
    for (int i = 0; i < 2; ++i) cs[i] = __builtin_bit_cast(f32x4, ld16p(wr, k6 * 32 + i * 16, (unsigned)WS_CS + D_ * 4));
    m_phase<5, 5, 4, 0, 3, 1>(W, a, l, lb, wr, 8, q, sog, vog, [&](int i) { return ring(8 + i); }, slot2,
                              [&](int i) { if (i == 0) lds_inc(lb + C_W4); } STPASS(8));
    {
      const unsigned p6 = lb + li * 400 + (3 * q + (g < 3 ? g : 2)) * 32;
      StatRd sd = e_stat0(lb, 12, li);
      static_for<0, 8>([&](auto i) {
        constexpr int n = 12 + i.value;
        if constexpr (i.value < 6) {
#pragma unroll
          for (int j = 0; j < 5; ++j) W[i.value * 5 + j] = __builtin_bit_cast(bf16x8, ld16p(wr, vl + i.value * 1024, w4b + j * (48 * 1024)));
          cfence();
        }
        if constexpr (i.value == 7) m_issue<6, 0>(a[0], wr, l, (unsigned)WS_U + g4(0) * 49152 + w * 6144, vl);
        float sum, sq, mean, var, m, s;
        e_stat1(sd, sum, sq, lb, n, li);
        ln_fast(sum, sq, mean, var, m, s);
        float rinv = __builtin_amdgcn_rcpf(s);
        const float nm = -m * rinv;
        f32x4 c0 = cs[0] * nm, c1 = cs[1] * nm;
        asm volatile("" : "+v"(rinv), "+v"(c0), "+v"(c1));
        f32x4 lo, hi;
        e_sum<4, 400>(lo, hi, lb, p6 + (n & 1) * SLOT, n);
        if constexpr (i.value < 7) sd = e_stat0(lb, n + 1, li);
        if (i == 0) { ST(10); }
        lo = lo * rinv + c0; hi = hi * rinv + c1;
        st16(wr, v6, (unsigned)WS_U + g8(i) * 49152,
             u32x4{pk2v(gelu2(bfr2(lo2(lo)))), pk2v(gelu2(bfr2(hi2(lo)))), pk2v(gelu2(bfr2(lo2(hi)))), pk2v(gelu2(bfr2(hi2(hi))))});
      });
      ST(11);
    }
  }
  if (gb != 0) {
    CTX;
    const rsrc_t xr = __builtin_amdgcn_make_buffer_rsrc((void*)x, 0, S_ * D_ * 2, 0x00020000);
    auto soh = [&](int i) { return (unsigned)WS_H + g4(i) * 20480 + q * 5120; };
    auto sox = [&](int i) { return (unsigned)WS_X1 + g8(i) * 20480 + q * 5120; };
    {
      u32x4 xv[4];
#pragma unroll
      for (int i = 0; i < 4; ++i) xv[i] = ld16p(xr, xo, g4(i) * (16 * D_ * 2));
      cfence();
#pragma unroll
      for (int i = 0; i < 4; ++i) {
        f32x4 lo, hi;
        e_sum<4, 336>(lo, hi, lb, pe + (i & 1) * SLOT, i);
        if (i == 0) { ST(4); }
        st16(wr, vst, (unsigned)WS_H + g4(i) * 20480,
             u32x4{pk2v(unpk(xv[i][0]) + bfr2(lo2(lo))), pk2v(unpk(xv[i][1]) + bfr2(hi2(lo))),
                   pk2v(unpk(xv[i][2]) + bfr2(lo2(hi))), pk2v(unpk(xv[i][3]) + bfr2(hi2(hi)))});
        if (i == 2) m_issue<5, 2>(a[0], wr, l, soh(0), vl);
      }
      ST(5);
    }
    m_phase<5, 5, 4, 2, 3, 1>(W, a, l, lb, wr, 4, q, soh, lane, [&](int i) { return ring(4 + i); }, slot2, nohook STPASS(6));
    {
      u32x4 hv[4];
#pragma unroll
      for (int i = 0; i < 4; ++i) hv[i] = ld16(wr, vft, (unsigned)WS_H + g4(i) * 20480);
      cfence();
      { const int u = wid < 120 ? wid : 119; touch(ws + WS_W4 + (unsigned)((u / 3) * 49152 + (u % 3) * 8192), l, lb); }
      const unsigned w3b = (unsigned)WS_W3 + ct16 * (6 * 20 * 1024) + q * 5120;
      auto load3 = [&](int f) { W[f] = __builtin_bit_cast(bf16x8, ld16p(wr, vl + (f / 6) * 1024, w3b + (f % 6) * (20 * 1024))); };
#pragma unroll
      for (int f = 0; f < 12; ++f) load3(f);
      cfence();
#pragma unroll
      for (int i = 0; i < 4; ++i) {
        const int n = 8 + i;
        f32x4 lo, hi;
        e_sum<4, 336>(lo, hi, lb, pe + (n & 1) * SLOT, n);
        if (i == 0) { ST(8); }
        const f32x2 mr = *(const LAS f32x2*)(uintptr_t)(lb + L_LN + (i * 16 + li) * 8);
        const float m1 = mr[0], r1 = mr[1];
        st16(wr, vst, (unsigned)WS_X1 + g4(i) * 20480,
             u32x4{pk2v(bfr2(bfr2(unpk(hv[i][0]) - m1) * r1) + bfr2(lo2(lo))), pk2v(bfr2(bfr2(unpk(hv[i][1]) - m1) * r1) + bfr2(hi2(lo))),
                   pk2v(bfr2(bfr2(unpk(hv[i][2]) - m1) * r1) + bfr2(lo2(hi))), pk2v(bfr2(bfr2(unpk(hv[i][3]) - m1) * r1) + bfr2(hi2(hi)))});
        if (i < 3) {
#pragma unroll
          for (int f = 0; f < 6; ++f) load3(12 + 6 * i + f);
          cfence();
        }
        if (i == 2) { m_issue<5, 2>(a[0], wr, l, sox(0), vl); m_issue<5, 2>(a[1], wr, l, sox(1), vl); }
      }
      ST(9);
    }
    m_phase<6, 5, 8, 2, 3, 2>(W, a, l, lb, wr, 12, q, sox, lane, [&](int i) { return ring(12 + i); }, slot2, nohook STPASS(10));
    m_issue<6, 0>(a[0], wr, l, (unsigned)WS_U + g4(0) * 49152 + w * 6144, vl);
#pragma unroll
    for (int j = 1; j < 5; ++j) W[25 + j] = __builtin_bit_cast(bf16x8, ld16p(wr, vl + 5 * 1024, w4b + j * (48 * 1024)));
    lds_wait(lb + C_W4, 4);
#pragma unroll
    for (int f = 0; f < 26; ++f) W[f] = *(const LAS bf16x8*)(uintptr_t)(lb + L_W + (q * 26 + f) * 1024 + vl);
    lds_inc(lb + C_WC);
  }
  {
    CTX;
    const rsrc_t orr = __builtin_amdgcn_make_buffer_rsrc(out, 0, S_ * D_ * 2, 0x00020000);
    u32x4 hv[2], xv[2];
#pragma unroll
    for (int i = 0; i < 2; ++i) {
      const unsigned to = g4(2 * i + grp) * 20480;
      hv[i] = ld16(wr, vft, (unsigned)WS_H + to); xv[i] = ld16(wr, vft, (unsigned)WS_X1 + to);
    }
    auto fin = [&](int p, auto si) {
      constexpr int s = si.value;
      f32x4 lo, hi;
      e_sum<8, 336>(lo, hi, lb, pe + (p % 3) * SLOT7, 20 + p);
      const u32x4 y = {pk2v(unpk(hv[s][0]) + bfr2(unpk(xv[s][0]) + bfr2(lo2(lo)))), pk2v(unpk(hv[s][1]) + bfr2(unpk(xv[s][1]) + bfr2(hi2(lo)))),
                       pk2v(unpk(hv[s][2]) + bfr2(unpk(xv[s][2]) + bfr2(lo2(hi)))), pk2v(unpk(hv[s][3]) + bfr2(unpk(xv[s][3]) + bfr2(hi2(hi))))};
      st16r<0>(orr, act ? xo : OOB, g4(p) * (16 * D_ * 2), y);
    };
    m_phase<5, 6, 4, 0, 2, 1>(W, a, l, lb, wr, 20, w, [&](int i) { return (unsigned)WS_U + g4(i) * 49152 + w * 6144; }, lane,
        [&](auto i) { return lb + (i.value == 0 ? C_F + 19 * 4 : i.value == 1 ? C_WC : i.value == 2 ? C_OK : C_F + 20 * 4); },
        [&](auto i) { return (unsigned)((i.value % 3) * SLOT7); },
        [&](auto i) {
          if constexpr (i.value == 1) { if (grp == 0) fin(0, std::integral_constant<int, 0>{}); }
          if constexpr (i.value == 2) { if (grp == 1) fin(1, std::integral_constant<int, 0>{}); }
          if constexpr (i.value == 3) fin(2 + grp, std::integral_constant<int, 1>{});
        } STPASS(12));
    STDUMP2(ws, 2 + grp);
  }
}

extern "C" void kernel_launch(void* const* d_in, const int* in_sizes, int n_in, void* d_out, int out_size,
                              void* d_ws, size_t ws_size, hipStream_t stream) {
  const char* x = (const char*)d_in[0];
  const char* wqkv = (const char*)d_in[1];
  const char* wo = (const char*)d_in[2];
  const char* w1 = (const char*)d_in[4];
  const char* w2 = (const char*)d_in[5];
  const char* w3 = (const char*)d_in[6];
  const char* w4 = (const char*)d_in[7];
  char* ws = (char*)d_ws;
  if (ws_size < WS_END) return;
  constexpr int LQ = 4 * 32768 + 2048;
  (void)hipFuncSetAttribute((const void*)k1_qkv, hipFuncAttributeMaxDynamicSharedMemorySize, LQ);
  (void)hipFuncSetAttribute((const void*)kp_main, hipFuncAttributeMaxDynamicSharedMemorySize, LDS_TOT);
  k1_qkv<<<256, 384, LQ, stream>>>(x, wqkv, wo, w1, w2, w3, w4, ws);
  kp_main<<<256, 512, LDS_TOT, stream>>>(x, wo, w1, w2, ws, (char*)d_out);
}
```

```cpp
#include <hip/hip_runtime.h>
#include <hip/hip_bf16.h>
#include <stdint.h>
#include <type_traits>

typedef __attribute__((ext_vector_type(8))) __bf16 bf16x8;
typedef __attribute__((ext_vector_type(4))) __bf16 bf16x4;
typedef __attribute__((ext_vector_type(2))) __bf16 bf16x2;
typedef __attribute__((ext_vector_type(4))) short s16x4;
typedef __attribute__((ext_vector_type(4))) float f32x4;
typedef __attribute__((ext_vector_type(2))) unsigned u32x2;
typedef __attribute__((ext_vector_type(4))) unsigned u32x4;
#define LAS __attribute__((address_space(3)))
#define DEV __device__ __forceinline__

constexpr int S_ = 2048, D_ = 640, F_ = 1536, H_ = 32;

constexpr size_t QK_HEAD = 128 * 768;
constexpr size_t V_HEAD  = 64 * 1280;
constexpr size_t WS_Q  = 0;
constexpr size_t WS_K  = WS_Q + H_ * QK_HEAD;
constexpr size_t WS_V  = WS_K + H_ * QK_HEAD;
constexpr size_t WS_O  = WS_V + H_ * V_HEAD;
constexpr size_t WS_H  = WS_O + (size_t)S_ * D_ * 2;
constexpr size_t WS_G  = WS_H + (size_t)S_ * D_ * 2;
constexpr size_t WS_X1 = WS_G + (size_t)S_ * D_ * 2;
constexpr size_t WS_U  = WS_X1 + (size_t)S_ * D_ * 2;
constexpr size_t WS_CS = WS_U + (size_t)S_ * F_ * 2;
constexpr size_t WS_WO = WS_CS + 16384;
constexpr size_t WS_W1 = WS_WO + (size_t)D_ * D_ * 2;
constexpr size_t WS_W2 = WS_W1 + (size_t)D_ * D_ * 2;
constexpr size_t WS_W3 = WS_W2 + (size_t)D_ * D_ * 2;
constexpr size_t WS_W4 = WS_W3 + (size_t)D_ * F_ * 2;
constexpr size_t WS_END = WS_W4 + (size_t)D_ * F_ * 2;
constexpr unsigned EMPTY = 0xffffffffu;

template <int I, int N, class Fn> DEV void static_for(Fn&& f) {
  if constexpr (I < N) { f(std::integral_constant<int, I>{}); static_for<I + 1, N>(f); }
}
template <int N> DEV void wait_vm() { asm volatile("s_waitcnt vmcnt(%0) lgkmcnt(0)" ::"n"(N) : "memory"); }
DEV void glds16(const char* sbase, unsigned voff, unsigned lds) {
  asm volatile("s_mov_b32 m0, %0\n\ts_nop 0\n\tglobal_load_lds_dwordx4 %1, %2" ::"s"(lds), "v"(voff), "s"(sbase) : "memory");
}
DEV unsigned lds_addr(const void* p) { return (unsigned)(uintptr_t)(LAS const void*)p; }
#define ST(i)
#define STR(i)
#define STDECL
#define STDUMP(o, ws, kid)
#define STARG
#define STPASS(i)
#define STDUMP2(ws, kid)
DEV void wait_lgkm0() { asm volatile("s_waitcnt lgkmcnt(0)" ::: "memory"); }
DEV void cfence() { asm volatile("" ::: "memory"); }
DEV float bfr(float x) { return (float)(__bf16)x; }
DEV unsigned pk2(float a, float b) { bf16x2 v = {(__bf16)a, (__bf16)b}; return __builtin_bit_cast(unsigned, v); }
DEV float bf_lo(unsigned u) { return __builtin_bit_cast(float, u << 16); }
DEV float bf_hi(unsigned u) { return __builtin_bit_cast(float, u & 0xffff0000u); }
DEV f32x4 mfma16(bf16x8 a, bf16x8 b, f32x4 c) { return __builtin_amdgcn_mfma_f32_16x16x32_bf16(a, b, c, 0, 0, 0); }
DEV unsigned ft_off(int row, int k, int KS) {
  return (unsigned)(((row >> 4) * KS + (k >> 5)) * 1024 + ((row & 15) + 16 * ((k >> 3) & 3)) * 16 + (k & 7) * 2);
}

DEV void ln_stats(float sum, float sq, float& mean, float& var, float& m, float& s) {
  mean = sum * (1.0f / D_);
  var = fmaxf(sq * (1.0f / D_) - mean * mean, 0.f);
  m = bfr(mean);
  s = bfr(sqrtf(bfr(bfr(var) + 1e-5f)));
}

DEV void dot_sum(float& s, bf16x8 f) {
  const bf16x2 one2 = {(__bf16)1.0f, (__bf16)1.0f};
#pragma unroll
  for (int e = 0; e < 4; ++e) { bf16x2 v = {f[2 * e], f[2 * e + 1]}; s = __builtin_amdgcn_fdot2_f32_bf16(v, one2, s, false); }
}
DEV void dot_sq(float& s, bf16x8 f) {
#pragma unroll
  for (int e = 0; e < 4; ++e) { bf16x2 v = {f[2 * e], f[2 * e + 1]}; s = __builtin_amdgcn_fdot2_f32_bf16(v, v, s, false); }
}
DEV unsigned f2u(float v) { return __builtin_bit_cast(unsigned, v); }
DEV float u2f(unsigned v) { return __builtin_bit_cast(float, v); }
DEV float red_g(float v) {
  const auto r = __builtin_amdgcn_permlane16_swap(f2u(v), f2u(v), false, false);
  v = u2f(r[0]) + u2f(r[1]);
  const auto s = __builtin_amdgcn_permlane32_swap(f2u(v), f2u(v), false, false);
  return u2f(s[0]) + u2f(s[1]);
}
DEV float max_g(float v) {
  const auto r = __builtin_amdgcn_permlane16_swap(f2u(v), f2u(v), false, false);
  v = fmaxf(u2f(r[0]), u2f(r[1]));
  const auto s = __builtin_amdgcn_permlane32_swap(f2u(v), f2u(v), false, false);
  return fmaxf(u2f(s[0]), u2f(s[1]));
}

DEV unsigned wrm_voff(int l, int q, int ldw) {
  return (unsigned)((l >> 4) * ldw + ((((l & 15) >> 1) ^ ((l >> 4) | (q << 2))) * 32) + (l & 1) * 16);
}
DEV bf16x8 wrm_frag(const char* p) {
  s16x4 lo = __builtin_amdgcn_ds_read_tr16_b64_v4i16((LAS s16x4*)(p));
  s16x4 hi = __builtin_amdgcn_ds_read_tr16_b64_v4i16((LAS s16x4*)(p + 1024));
  typedef __attribute__((ext_vector_type(8))) short s16x8;
  s16x8 v = __builtin_shufflevector(lo, hi, 0, 1, 2, 3, 4, 5, 6, 7);
  return __builtin_bit_cast(bf16x8, v);
}
DEV unsigned wrm_lane(int l, int jhi, int jj) {
  const int g = l >> 4, i = l & 15;
  return (unsigned)((8 * g + (i >> 2)) * 256 + ((((jhi ^ (g & 1)) << 2) | (jj ^ (i >> 2))) * 32) + (i & 3) * 8);
}

struct WJob { const char* src; char* dst; const char* esrc; char* ws; unsigned edst; int ldw, wks, r4, tj, er4, perm; };
DEV WJob make_job(int b, int hk, int tj, const char* wo, const char* w1, const char* w2, const char* w3, const char* w4, char* ws) {
  const char* wsrc; char* wdst; int ldw = D_ * 2, wks = 20, kt, nt;
  if (b < 120) { wsrc = w3; wdst = ws + WS_W3; ldw = F_ * 2; kt = b / 12; nt = b - kt * 12; }
  else { const int u = b - 120; wsrc = w4; wdst = ws + WS_W4; wks = 48; kt = u / 5; nt = u - kt * 5; }
  WJob j;
  j.src = wsrc + nt * 256;
  j.r4 = kt * 16;
  j.dst = hk == 0 ? wdst + (size_t)(nt * 8 * wks + kt * 2) * 1024 : nullptr;
  j.ldw = ldw; j.wks = wks;
  j.ws = ws; j.tj = tj;
  const int e = b < 150 ? b : 149, mi = e / 50, u = e - mi * 50, ekt = u / 5, ent = u - ekt * 5;
  j.esrc = (mi == 0 ? wo : (mi == 1 ? w1 : w2)) + ent * 256;
  j.er4 = ekt * 16 + 8 * hk; j.perm = mi == 0;
  j.edst = b < 150 ? (unsigned)WS_WO + mi * (D_ * D_ * 2) + (ent * 8 * 20 + ekt * 2 + hk) * 1024 : 0x40000000u;
  return j;
}

template <bool SWAP>
struct GemmQ {
  f32x4 acc[4][4];
  STDECL;
  static constexpr int NG = 4, D = 3, NGRP = 10, SB = 32768, XCH = NG * SB;
  DEV bool run(char* smem, const char* Ab, const char* Wb, int k0, const WJob& job) {
    STR(0); ST(1);
    constexpr int LDA = D_ * 2, LDW = 3 * D_ * 2;
    const int tid = threadIdx.x, l = tid & 63;
    const int wave = __builtin_amdgcn_readfirstlane(tid >> 6);
    if (wave >= 4) {
      const bool isw = wave == 5;
      unsigned vo[2];
#pragma unroll
      for (int q = 0; q < 2; ++q)
        vo[q] = isw ? wrm_voff(l, q, LDW) : (unsigned)((l >> 3) * LDA + (((l & 7) ^ (q * 4 + (l >> 4))) * 16));
      const unsigned vx[4] = {vo[0], isw ? vo[0] : vo[1], isw ? vo[1] : vo[0], vo[1]};
      const unsigned dst0 = lds_addr(smem) + (isw ? 16384 : 0);
      const char* src0 = isw ? Wb : Ab;
      const int gstr = isw ? 64 * LDW : 128, istr = isw ? 4 * LDW : 8 * LDA;
      auto issue = [&](int grp) {
        int kg = grp + k0;
        kg = kg >= NGRP ? kg - NGRP : kg;
        const char* sp = src0 + (size_t)kg * gstr;
#pragma unroll
        for (int i = 0; i < 16; ++i) glds16(sp + (size_t)i * istr, vx[i & 3], dst0 + (grp % NG) * SB + i * 1024);
      };
      static_for<0, D>([&](auto gi) { issue(gi.value); });
      const unsigned eb = XCH + 2048 + (isw ? 8192 : 0);
#pragma unroll
      for (int i = 0; i < 8; ++i) {
        int r4 = job.er4 + i;
        const int kq = r4 / 40, rr = r4 - kq * 40;
        if (job.perm) r4 = rr < 32 ? (8 * kq + (rr >> 2)) * 5 + (rr & 3) : (8 * kq + rr - 32) * 5 + 4;
        glds16(job.esrc + (size_t)r4 * 4 * LDA, wrm_voff(l, (i >> 1) & 1, LDA), lds_addr(smem) + eb + i * 1024);
      }
      static_for<0, NGRP>([&](auto gi) {
        constexpr int grp = gi.value;
        constexpr int issued = grp + D < NGRP ? grp + D : NGRP;
        wait_vm<16 * (issued - grp - 1) + (grp > NGRP - D ? 16 : 0) + (grp < D || (grp > D && grp <= 2 * D) ? 8 : 0)>();
        __builtin_amdgcn_s_barrier();
        cfence();
        if constexpr (grp + D < NGRP) issue(grp + D);
        if constexpr (grp == D) {
          const __amdgpu_buffer_rsrc_t wr = __builtin_amdgcn_make_buffer_rsrc(job.ws, 0, (int)WS_END, 0x00020000);
#pragma unroll
          for (int j = 0; j < 8; ++j) {
            const bf16x8 f = wrm_frag(smem + eb + wrm_lane(l, j >> 2, j & 3));
            __builtin_amdgcn_raw_buffer_store_b128(__builtin_bit_cast(u32x4, f), wr, job.edst + j * 20480 + l * 16, 0, 16);
          }
          cfence();
        }
        if constexpr (grp == NGRP - D) {
          if (job.dst) {
#pragma unroll
            for (int i = 0; i < 16; ++i)
              glds16(job.src + (size_t)(job.r4 + i) * 4 * job.ldw, wrm_voff(l, (i >> 1) & 1, job.ldw), dst0 + ((grp - 1) % NG) * SB + i * 1024);
          } else {
#pragma unroll
            for (int i = 0; i < 16; ++i) {
              const int u = i * 28 + job.tj;
              const char* p = job.ws + WS_WO + (u < 300 ? u : 299) * 8192;
              asm volatile("s_mov_b32 m0, %0\n\ts_nop 0\n\tglobal_load_lds_dword %1, %2" ::"s"(dst0 + ((grp - 1) % NG) * SB), "v"(l * 128), "s"(p) : "memory");
            }
          }
        }
      });
      __builtin_amdgcn_s_barrier();
      wait_vm<0>();
      if (job.dst) {
        const char* tb = smem + ((NGRP - D - 1) % NG) * SB + (isw ? 16384 : 0);
#pragma unroll
        for (int kk = 0; kk < 2; ++kk)
#pragma unroll
          for (int j = 0; j < 8; ++j) {
            const bf16x8 f = wrm_frag(tb + kk * 8192 + wrm_lane(l, j >> 2, j & 3));
            *(bf16x8*)(job.dst + (size_t)(j * job.wks + kk) * 1024 + l * 16) = f;
          }
      }
      return false;
    }
    const int wm = wave >> 1, wn = wave & 1, g = l >> 4;
    float rs[2] = {0.f, 0.f}, rq[2] = {0.f, 0.f}, cd[2] = {0.f, 0.f};
#pragma unroll
    for (int i = 0; i < 4; ++i)
#pragma unroll
      for (int j = 0; j < 4; ++j) acc[i][j] = f32x4{0.f, 0.f, 0.f, 0.f};
    const unsigned a0 = (unsigned)((wm * 64 + (l & 15)) * 128 + ((g ^ ((l & 15) >> 1)) * 16));
    const char* al[2][2] = {{smem + a0 + wn * 4096, smem + a0 + 4096 - wn * 4096},
                            {smem + (a0 ^ 64u) + wn * 4096, smem + (a0 ^ 64u) + 4096 - wn * 4096}};
    const char* wl[4];
#pragma unroll
    for (int j = 0; j < 4; ++j) wl[j] = smem + 16384 + wrm_lane(l, wn, j ^ (2 * wm));
    bf16x8 af[2][4], wf[2][4];
    auto ldfrag = [&](auto ti) {
      constexpr int t = ti.value, kk = t & 1, so = ((t >> 1) % NG) * SB;
#pragma unroll
      for (int i = 0; i < 4; ++i) af[kk][i] = *(const bf16x8*)(al[kk][i >> 1] + so + (i & 1) * 2048);
#pragma unroll
      for (int j = 0; j < 4; ++j) wf[kk][j] = wrm_frag(wl[j] + so + kk * 8192);
    };
    __builtin_amdgcn_s_barrier();
    cfence();
    ST(3);
    ldfrag(std::integral_constant<int, 0>{});
    static_for<0, 2 * NGRP>([&](auto ti) {
      constexpr int t = ti.value, kk = t & 1;
      if constexpr (t + 1 < 2 * NGRP) {
        if constexpr (kk == 1) {
          wait_lgkm0();
          __builtin_amdgcn_s_barrier();
          cfence();
          ST(3 + (t + 1) / 2);
        }
        ldfrag(std::integral_constant<int, t + 1>{});
      }
      __builtin_amdgcn_sched_barrier(0);
#pragma unroll
      for (int i = 0; i < 4; ++i)
#pragma unroll
        for (int j = 0; j < 4; ++j)
          acc[i][j] = SWAP ? mfma16(wf[kk][j], af[kk][i], acc[i][j]) : mfma16(af[kk][i], wf[kk][j], acc[i][j]);
#pragma unroll
      for (int i = 0; i < 2; ++i) { dot_sum(rs[i], af[kk][i]); dot_sq(rq[i], af[kk][i]); dot_sum(cd[i], wf[kk][i]); }
#pragma unroll
      for (int q = 0; q < 8; ++q) {
        __builtin_amdgcn_sched_group_barrier(0x8, 2, 0);
        __builtin_amdgcn_sched_group_barrier(0x2, 3, 0);
      }
      asm volatile("" : "+v"(rs[0]), "+v"(rs[1]), "+v"(rq[0]), "+v"(rq[1]), "+v"(cd[0]), "+v"(cd[1]));
      __builtin_amdgcn_sched_barrier(0);
    });
    ST(21);
    float2* xs = (float2*)(smem + XCH);
    float* xc = (float*)(smem + XCH + 1024);
#pragma unroll
    for (int i = 0; i < 2; ++i) {
      const float a = red_g(rs[i]), b = red_g(rq[i]), c = red_g(cd[i]);
      float mean, var, m, sd;
      ln_stats(a, b, mean, var, m, sd);
      const float rinv = 1.0f / sd;
      if (l < 16) { xs[(wm * 4 + wn * 2 + i) * 16 + l] = float2{rinv, -m * rinv}; xc[(wn * 4 + wm * 2 + i) * 16 + l] = c; }
    }
    wait_lgkm0();
    __builtin_amdgcn_s_barrier();
    cfence();
    return true;
  }
};

template <bool ISV>
DEV void qkv_body(char* smem, const char* x, const char* wqkv, char* ws, int rt, int ct, int k0, const WJob& job) {
  using GQ = GemmQ<!ISV>;
  GQ gm;
  const int row0 = rt * 128, n0 = ct * 128;
  if (!gm.run(smem, x + (size_t)row0 * (D_ * 2), wqkv + n0 * 2, k0, job)) return;
  const int tid = threadIdx.x, l = tid & 63, wave = tid >> 6, wm = wave >> 1, wn = wave & 1, g = l >> 4;
  const float2* xs = (const float2*)(smem + GQ::XCH);
  const float* xc = (const float*)(smem + GQ::XCH + 1024);
  if constexpr (!ISV) {
    f32x4 cs[4];
    unsigned po[4];
#pragma unroll
    for (int j = 0; j < 4; ++j) {
      const int ja = wn * 4 + (j ^ (2 * wm));
      cs[j] = *(const f32x4*)(xc + ja * 16 + 4 * g);
      const int c = (n0 + ja * 16 + 4 * g) % D_, head = c / 20, d0 = c - head * 20;
      po[j] = (unsigned)(head * (int)QK_HEAD + (d0 >> 3) * 256 + (d0 & 7) * 2) | (d0 == 16 ? 1u : 0u);
    }
    char* dstb = ws + (ct < 5 ? WS_Q : WS_K) + (size_t)(row0 >> 4) * 768 + (l & 15) * 16;
#pragma unroll
    for (int i = 0; i < 4; ++i) {
      const int ia = wm * 4 + (i ^ (2 * wn));
      const float2 st = xs[ia * 16 + (l & 15)];
#pragma unroll
      for (int j = 0; j < 4; ++j) {
        const f32x4 y = gm.acc[i][j] * st.x + cs[j] * st.y;
        char* p = dstb + ia * 768 + (po[j] & ~1u);
        if (po[j] & 1u) *(u32x4*)p = u32x4{pk2(y[0], y[1]), pk2(y[2], y[3]), 0u, 0u};
        else *(u32x2*)p = u32x2{pk2(y[0], y[1]), pk2(y[2], y[3])};
      }
    }
  } else {
    float cd[4];
    unsigned po[4];
#pragma unroll
    for (int j = 0; j < 4; ++j) {
      const int ja = wn * 4 + (j ^ (2 * wm));
      cd[j] = xc[ja * 16 + (l & 15)];
      const int c = n0 - 2 * D_ + ja * 16 + (l & 15), head = c / 20, d = c - head * 20;
      po[j] = (unsigned)(head * (int)V_HEAD + (d < 16 ? (g * 16 + d) * 16 : 1024 + (g * 4 + d - 16) * 16));
    }
    char* dstb = ws + WS_V + (size_t)(row0 >> 5) * 1280;
#pragma unroll
    for (int i = 0; i < 4; ++i) {
      const int ia = wm * 4 + (i ^ (2 * wn));
      const f32x4 s01 = *(const f32x4*)(xs + ia * 16 + 4 * g), s23 = *(const f32x4*)(xs + ia * 16 + 4 * g + 2);
      const f32x4 rr = {s01[0], s01[2], s23[0], s23[2]}, nm = {s01[1], s01[3], s23[1], s23[3]};
#pragma unroll
      for (int j = 0; j < 4; ++j) {
        const f32x4 y = gm.acc[i][j] * rr + nm * cd[j];
        *(u32x2*)(dstb + (ia >> 1) * 1280 + (i & 1) * 8 + po[j]) = u32x2{pk2(y[0], y[1]), pk2(y[2], y[3])};
      }
    }
  }
  STDUMP(gm, ws, 1);
}

DEV void colsum_job(char* smem, const char* w1, const char* w3, float* cs, int blk) {
  const int tid = threadIdx.x, c = tid % 17, rg = tid / 17;
  const int gc = blk * 17 + c;
  const char* base = gc < 80 ? w1 + gc * 16 : w3 + (gc - 80) * 16;
  const int ld = gc < 80 ? D_ * 2 : F_ * 2;
  float a[8] = {0.f, 0.f, 0.f, 0.f, 0.f, 0.f, 0.f, 0.f};
  if (rg < 20) {
#pragma unroll 16
    for (int r = rg; r < D_; r += 20) {
      u32x4 v = *(const u32x4*)(base + (size_t)r * ld);
#pragma unroll
      for (int e = 0; e < 4; ++e) { a[2 * e] += bf_lo(v[e]); a[2 * e + 1] += bf_hi(v[e]); }
    }
  }
  float* sm = (float*)smem;
  if (rg < 20) {
#pragma unroll
    for (int e = 0; e < 8; ++e) sm[rg * 136 + c * 8 + e] = a[e];
  }
  __syncthreads();
  if (tid < 136) {
    float t = 0.f;
#pragma unroll
    for (int r = 0; r < 20; ++r) t += sm[r * 136 + tid];
    cs[blk * 136 + tid] = t;
  }
}

__global__ __launch_bounds__(384) void k1_qkv(const char* x, const char* wqkv, const char* wo, const char* w1, const char* w2,
                                              const char* w3, const char* w4, char* ws) {
  extern __shared__ __attribute__((aligned(16))) char smem[];
  const int b = blockIdx.x, xcd = b & 7, j = b >> 3, r = j & 3, c = j >> 2;
  {
    const u32x4 e = {EMPTY, EMPTY, EMPTY, EMPTY};
    const int t = threadIdx.x, bt = b >> 4;
    *(u32x4*)(ws + WS_O + b * 10240 + t * 16) = e;
    if (t < 256) *(u32x4*)(ws + WS_O + b * 10240 + 6144 + t * 16) = e;
    else if (t < 336) *(u32x4*)(ws + WS_G + ((bt >> 1) * 16 + (bt & 1) * 15) * 20480 + (b & 15) * 1280 + (t - 256) * 16) = e;
  }
  if ((xcd & 1) && c == 7) { colsum_job(smem, w1, w3, (float*)(ws + WS_CS), (xcd >> 1) * 4 + r); return; }
  const int rt = (xcd >> 1) * 4 + r, ct = (xcd & 1) * 8 + c;
  const int wave = __builtin_amdgcn_readfirstlane(threadIdx.x >> 6);
  const WJob job = make_job(rt * 15 + ct, wave & 1, j < 28 ? j : 299, wo, w1, w2, w3, w4, ws);
  const int k0 = (3 * r + 4 * c) % 10;
  if (ct >= 10) qkv_body<true>(smem, x, wqkv, ws, rt, ct, k0, job);
  else qkv_body<false>(smem, x, wqkv, ws, rt, ct, k0, job);
}

typedef __attribute__((ext_vector_type(2))) float f32x2;
typedef unsigned long long u64;
DEV f32x2 unpk(unsigned u) { return f32x2{bf_lo(u), bf_hi(u)}; }
DEV unsigned pk2v(f32x2 v) { return pk2(v[0], v[1]); }
DEV f32x2 bfr2(f32x2 v) { unsigned u = pk2v(v); asm("" : "+v"(u)); return unpk(u); }
DEV void ln_fast(float sum, float sq, float& mean, float& var, float& m, float& s) {
  mean = sum * (1.0f / D_);
  var = fmaxf(sq * (1.0f / D_) - mean * mean, 0.f);
  const f32x2 r = bfr2(f32x2{mean, var});
  m = r[0];
  s = bfr(__builtin_amdgcn_sqrtf(bfr(r[1] + 1e-5f)));
}
DEV f32x2 gelu2(f32x2 x) {
  const float c0 = -2.0f * 0.7978845608028654f * 1.4426950408889634f, c1 = c0 * 0.044715f;
  const f32x2 z = x * (x * x * c1 + c0);
  const f32x2 d = f32x2{__builtin_amdgcn_exp2f(z[0]), __builtin_amdgcn_exp2f(z[1])} + 1.0f;
  return x * f32x2{__builtin_amdgcn_rcpf(d[0]), __builtin_amdgcn_rcpf(d[1])};
}
DEV f32x2 lo2(f32x4 v) { return f32x2{v[0], v[1]}; }
DEV f32x2 hi2(f32x4 v) { return f32x2{v[2], v[3]}; }

typedef __amdgpu_buffer_rsrc_t rsrc_t;
constexpr unsigned OOB = 0x40000000u;
DEV unsigned full(unsigned v) { return v < EMPTY ? v : EMPTY - 1; }
DEV void st8(rsrc_t r, unsigned v, unsigned s, u32x2 d) { __builtin_amdgcn_raw_buffer_store_b64(u32x2{full(d[0]), full(d[1])}, r, v, s, 16); }
template <int AUX> DEV void st16r(rsrc_t r, unsigned v, unsigned s, u32x4 d) {
  asm("" : "+v"(d));
  __builtin_amdgcn_raw_buffer_store_b128(d, r, v, s, AUX);
  asm volatile("s_nop 1" ::"v"(d) : "memory");
}
DEV void st16(rsrc_t r, unsigned v, unsigned s, u32x4 d) { st16r<16>(r, v, s, u32x4{full(d[0]), full(d[1]), full(d[2]), full(d[3])}); }
DEV void st_empty(rsrc_t r, unsigned v, unsigned s) { st16r<16>(r, v, s, u32x4{EMPTY, EMPTY, EMPTY, EMPTY}); }
DEV u32x4 ld16(rsrc_t r, unsigned v, unsigned s) { return __builtin_amdgcn_raw_buffer_load_b128(r, v, s, 16); }
DEV u32x2 ld8(rsrc_t r, unsigned v, unsigned s) { return __builtin_amdgcn_raw_buffer_load_b64(r, v, s, 16); }
DEV u32x4 ld16p(rsrc_t r, unsigned v, unsigned s) { return __builtin_amdgcn_raw_buffer_load_b128(r, v, s, 0); }
DEV void vm0() { __builtin_amdgcn_s_waitcnt(0x0f70); cfence(); }

constexpr int LDS_TOT = 163840;
constexpr int AT_Q = 0, AT_K = AT_Q + 16 * 768, AT_V = AT_K + 32 * 768;
constexpr int SLOT = 25600, SLOT7 = 43008;
constexpr int L_W = 51200, L_W2 = 57344;
constexpr int L_SX = 159744;
constexpr int L_SY = 161792;
constexpr int C_P = L_SY, C_S = L_SY + 128, C_F = L_SY + 256, C_W2 = L_SY + 384, C_W4 = L_SY + 388, C_WC = L_SY + 392, C_OK = L_SY + 396, C_WL = L_SY + 400;
constexpr int L_LN = L_SY + 512;
constexpr int L_DUM = L_SY + 1024;
DEV void touch(const char* p, int l, unsigned lb) {
  asm volatile("s_mov_b32 m0, %0\n\ts_nop 0\n\tglobal_load_lds_dword %1, %2" ::"s"(lb + L_DUM + 512), "v"(l * 128), "s"(p) : "memory");
}
DEV void lds_inc(unsigned a) {
  asm volatile("s_mov_b64 exec, 1\n\tds_add_u32 %0, %1\n\ts_mov_b64 exec, -1" ::"v"(a), "v"(1u) : "memory");
}
DEV void lds_wait(unsigned a, unsigned target) {
  for (int it = 0; it < (1 << 22); ++it) {
    const unsigned v = __builtin_amdgcn_readfirstlane(*(volatile LAS unsigned*)(uintptr_t)a);
    if (v >= target) break;
  }
  cfence();
}
DEV f32x4 lds_ld4(unsigned a) { return *(const LAS f32x4*)(uintptr_t)a; }
DEV unsigned max3u(unsigned a, unsigned b, unsigned c) { a = a > b ? a : b; return a > c ? a : c; }

template <int KW, int MODE>
DEV void m_issue(u32x4 (&a)[6], rsrc_t wr, int l, unsigned s, unsigned v) {
#pragma unroll
  for (int k = 0; k < (MODE == 1 ? KW - 1 : KW); ++k) a[k] = ld16(wr, v + k * 1024, s);
  if constexpr (MODE == 1) {
    const unsigned t = (l >> 4) * 256 + (l & 15) * 8 + 4096;
    const u32x2 t0 = ld8(wr, t, s), t1 = ld8(wr, t + 128, s);
    a[KW - 1] = u32x4{t0[0], t0[1], t1[0], t1[1]};
  }
  cfence();
}
template <int NT, int KW, int NTILE, int MODE, int NB, int PRE, class TS, class TV, class TG, class TL, class TH>
DEV void m_phase(const bf16x8 (&W)[30], u32x4 (&a)[3][6], int l, unsigned lb, rsrc_t wr, int n0, int part, TS&& so, TV&& vo, TG&& guard,
                 TL&& slot, TH&& hook STARG) {
  constexpr int RS = NT * 64 + 16;
  const int g = l >> 4, li = l & 15;
  const unsigned pw = lb + part * (16 * RS) + li * RS + g * 16;
  const bool dg = g == (li >> 2);
  const unsigned sx = dg ? lb + L_SX + li * 32 + part * 8 : lb + L_DUM + l * 8, sm = dg ? 512u : 0u;
  static_for<PRE, NB - 1>([&](auto i) { m_issue<KW, MODE>(a[i.value], wr, l, so(i), vo(i)); });
  static_for<0, NTILE>([&](auto i) {
    constexpr int B = i.value % NB;
    const int n = n0 + i;
    auto bad = [&]() {
      unsigned m = 0;
#pragma unroll
      for (int k = 0; k < KW; ++k) m = max3u(max3u(m, a[B][k][0], a[B][k][1]), a[B][k][2], a[B][k][3]);
      return __builtin_amdgcn_ballot_w64(m == EMPTY) != 0;
    };
    if (bad()) {
      int it = 0;
#pragma unroll 1
      do { m_issue<KW, MODE>(a[B], wr, l, so(i), vo(i)); vm0(); } while (bad() && ++it < (1 << 16));
    }
    cfence();
    if (i == 0) { ST(STI); }
    const f32x4 z4 = {0.f, 0.f, 0.f, 0.f};
    f32x4 sa = z4, qa = z4;
    if constexpr (MODE == 2) {
      const u32x4 one = {0x3F803F80u, 0x3F803F80u, 0x3F803F80u, 0x3F803F80u};
#pragma unroll
      for (int k = 0; k < KW; ++k) {
        const bf16x8 f = __builtin_bit_cast(bf16x8, a[B][k]);
        qa = mfma16(f, f, qa); sa = mfma16(__builtin_bit_cast(bf16x8, one), f, sa);
      }
      __builtin_amdgcn_sched_barrier(0);
    }
    if constexpr (i.value + NB - 1 < NTILE) {
      constexpr std::integral_constant<int, i.value + NB - 1> j{};
      m_issue<KW, MODE>(a[j.value % NB], wr, l, so(j), vo(j));
    }
    const unsigned ga = guard(i);
    const unsigned gv = *(volatile LAS unsigned*)(uintptr_t)ga;
    __builtin_amdgcn_sched_barrier(0);
    f32x4 acc[NT];
#pragma unroll
    for (int j = 0; j < NT; ++j) acc[j] = mfma16(W[j], __builtin_bit_cast(bf16x8, a[B][0]), z4);
    if constexpr (MODE == 2) {
      const float q01 = (li & 1) ? qa[1] : qa[0], q23 = (li & 1) ? qa[3] : qa[2];
      *(LAS f32x2*)(uintptr_t)(sx + (i.value & 3) * sm) = f32x2{sa[0], (li & 2) ? q23 : q01};
      lds_inc(lb + C_S + n * 4);
      __builtin_amdgcn_sched_barrier(0);
    }
#pragma unroll
    for (int k = 1; k < KW; ++k)
#pragma unroll
      for (int j = 0; j < NT; ++j) acc[j] = mfma16(W[k * NT + j], __builtin_bit_cast(bf16x8, a[B][k]), acc[j]);
    __builtin_amdgcn_sched_barrier(0);
    if (__builtin_amdgcn_readfirstlane(gv) < 4u) lds_wait(ga, 4);
    const unsigned sl = slot(i);
#pragma unroll
    for (int j = 0; j < NT; ++j) *(LAS f32x4*)(uintptr_t)(pw + sl + j * 64) = acc[j];
    lds_inc(lb + C_P + n * 4);
    hook(i);
  });
  ST(STI + 1);
}

DEV unsigned lds_cnt(unsigned a) { const unsigned v = *(volatile LAS unsigned*)(uintptr_t)a; cfence(); return v; }
template <int NP, int RS>
DEV void e_sum(f32x4& lo, f32x4& hi, unsigned lb, unsigned a, int n) {
  f32x4 p[NP][2];
#pragma unroll 1
  for (int it = 0;; ++it) {
    const unsigned c = lds_cnt(lb + C_P + n * 4);
#pragma unroll
    for (int q = 0; q < NP; ++q) { p[q][0] = lds_ld4(a + q * (16 * RS)); p[q][1] = lds_ld4(a + q * (16 * RS) + 16); }
    if (__builtin_amdgcn_readfirstlane(c) >= (unsigned)NP || it) break;
    lds_wait(lb + C_P + n * 4, NP);
  }
  lds_inc(lb + C_F + n * 4);
#pragma unroll
  for (int s = 1; s < NP; s *= 2)
#pragma unroll
    for (int q = 0; q < NP; q += 2 * s) { p[q][0] += p[q + s][0]; p[q][1] += p[q + s][1]; }
  lo = p[0][0]; hi = p[0][1];
}
struct StatRd { unsigned c; f32x4 a, b; };
DEV StatRd e_stat0(unsigned lb, int n, int li) {
  StatRd r;
  r.c = lds_cnt(lb + C_S + n * 4);
  r.a = lds_ld4(lb + L_SX + (n & 3) * 512 + li * 32); r.b = lds_ld4(lb + L_SX + (n & 3) * 512 + li * 32 + 16);
  cfence();
  return r;
}
DEV void e_stat1(StatRd r, float& sum, float& sq, unsigned lb, int n, int li) {
  if (__builtin_amdgcn_readfirstlane(r.c) < 4u) { lds_wait(lb + C_S + n * 4, 4); r = e_stat0(lb, n, li); }
  const f32x4 t = r.a + r.b;
  sum = t[0] + t[2]; sq = t[1] + t[3];
}

template <int I, class TH>
DEV void attention(char* smem, int xcd, int w, u32x2& om, u32x2& ot, TH&& hook) {
  const int l = threadIdx.x & 63, g = l >> 4, li = l & 15;
  u32x4 qv = *(const u32x4*)(smem + AT_Q + (2 * w + I) * 768 + (g < 3 ? g : 2) * 256 + li * 16);
  if (g == 3) qv = u32x4{0u, 0u, 0u, 0u};
  const bf16x8 qf = __builtin_bit_cast(bf16x8, qv);
  const char* Ks = smem + AT_K + (2 * w + I) * 768 + (g < 3 ? g : 2) * 256 + li * 16;
  const int kbase = xcd * 256 + 32 * w + 16 * I - 256;
  f32x4 s0[17];
  const f32x4 z4 = {0.f, 0.f, 0.f, 0.f};
#pragma unroll
  for (int c = 0; c < 17; ++c) s0[c] = mfma16(*(const bf16x8*)(Ks + c * 768), qf, z4);
  const float NEG = -__builtin_inff();
  hook(std::integral_constant<int, 5 * I>{});
#pragma unroll
  for (int r = 0; r < 4; ++r) {
    const int kj = 4 * g + r;
    if (kj <= li) s0[0][r] = NEG;
    if (kj > li) s0[16][r] = NEG;
  }
  if (kbase < 0) {
#pragma unroll
    for (int c = 0; c < 17; ++c)
      if (kbase + 16 * c < 0) s0[c] = f32x4{NEG, NEG, NEG, NEG};
  }
  float m0 = NEG, m1 = NEG;
#pragma unroll
  for (int c = 0; c < 17; ++c) {
    m0 = fmaxf(fmaxf(m0, s0[c][0]), s0[c][1]);
    m1 = fmaxf(fmaxf(m1, s0[c][2]), s0[c][3]);
  }
  m0 = max_g(fmaxf(m0, m1));
  const float C2 = 0.22360679774997896f * 1.4426950408889634f;
  const float b0 = -m0 * C2;
  u32x2 p0[18];
  static_for<0, 17>([&](auto ci) {
    constexpr int c = ci.value;
    if constexpr (c % 5 == 0) hook(std::integral_constant<int, 5 * I + 1 + c / 5>{});
    const f32x4 e = s0[c] * C2 + b0;
    p0[c + I] = u32x2{pk2(__builtin_amdgcn_exp2f(e[0]), __builtin_amdgcn_exp2f(e[1])), pk2(__builtin_amdgcn_exp2f(e[2]), __builtin_amdgcn_exp2f(e[3]))};
  });
  p0[I ? 0 : 17] = u32x2{0u, 0u};
  const char* Vs0 = smem + AT_V + w * 1280 + (g * 16 + li) * 16;
  const char* Vs1 = smem + AT_V + w * 1280 + 1024 + (g * 4 + (li & 3)) * 16;
  f32x4 o0 = z4, o1 = z4;
#pragma unroll
  for (int s = 0; s < 9; ++s) {
    bf16x8 v0 = *(const bf16x8*)(Vs0 + s * 1280);
    u32x4 v1u = *(const u32x4*)(Vs1 + s * 1280);
    if (li == 4) v1u = u32x4{0x3F803F80u, 0x3F803F80u, 0x3F803F80u, 0x3F803F80u};
    const u32x4 pa = {p0[2 * s][0], p0[2 * s][1], p0[2 * s + 1][0], p0[2 * s + 1][1]};
    const bf16x8 pf = __builtin_bit_cast(bf16x8, pa);
    o0 = mfma16(v0, pf, o0); o1 = mfma16(__builtin_bit_cast(bf16x8, v1u), pf, o1);
  }
  const float r0 = 1.0f / __shfl(o1[0], 16 + li);
  om = u32x2{pk2(o0[0] * r0, o0[1] * r0), pk2(o0[2] * r0, o0[3] * r0)};
  ot = u32x2{pk2(o1[0] * r0, o1[1] * r0), pk2(o1[2] * r0, o1[3] * r0)};
}

#define CTX \
  int tid_ = threadIdx.x, b_ = blockIdx.x; \
  asm volatile("" : "+v"(tid_)); asm volatile("" : "+s"(b_)); \
  const int xcd = b_ & 7, jj = b_ >> 3, l = tid_ & 63, g = l >> 4, li = l & 15; \
  const int w = __builtin_amdgcn_readfirstlane(tid_ >> 6), q = w & 3, grp = w >> 2; \
  const int ct = jj & 7, ct16 = jj & 15; \
  const unsigned vl = l * 16; \
  const int G4 = (xcd * 4 + (jj >> 3)) * 4, dir = (jj >> 3) & 1; \
  const int B8 = (xcd * 2 + (jj >> 4)) * 8; \
  auto g4 = [&](int i) { return G4 + (dir ? 3 - i : i); }; \
  auto g8 = [&](int i) { return B8 + ((i & 1) ? 7 - (i >> 1) : (i >> 1)); }; \
  auto lane = [&](int) { return vl; }; \
  auto ring = [&](int n) { return lb + (n >= 2 ? C_F + (n - 2) * 4 : C_OK); }; \
  auto slot2 = [&](int i) { return (unsigned)((i & 1) * SLOT); }; \
  const int wid = jj * 4 + q; \
  auto nohook = [](int) {}; \
  const int nu = q < 2 ? 3 : 2, un = (q < 2 ? 3 * q : 2 * q + 2) + (g < nu ? g : nu - 1); \
  const bool act = g < nu; \
  const int ko = ct * 10 + un; \
  const unsigned vft = (ko >> 2) * 1024 + (ko & 3) * 256 + li * 16, vst = act ? vft : OOB; \
  const unsigned pe = lb + li * 336 + un * 32; \
  const unsigned xo = li * (D_ * 2) + ko * 16; \
  const int k6 = ct16 * 12 + 3 * q + (g < 3 ? g : 2); \
  const unsigned v6 = g < 3 ? (unsigned)((k6 >> 2) * 1024 + (k6 & 3) * 256 + li * 16) : OOB; \
  const unsigned w4b = (unsigned)WS_W4 + ct * (5 * 48 * 1024) + w * 6144; \
  (void)0

__global__ __launch_bounds__(512) void kp_main(const char* x, char* ws, char* out) {
  extern __shared__ __attribute__((aligned(16))) char smem[];
  STDECL;
  STR(0); ST(1);
  const unsigned lb = lds_addr(smem);
  const rsrc_t wr = __builtin_amdgcn_make_buffer_rsrc(ws, 0, (int)WS_END, 0x00020000);
  bf16x8 W[30];
  u32x4 a[3][6];
  int ga, gb;
  {
    CTX;
    const unsigned qo = (unsigned)WS_Q + jj * (unsigned)QK_HEAD + xcd * 16 * 768;
    const unsigned ko0 = (unsigned)WS_K + jj * (unsigned)QK_HEAD, vo0 = (unsigned)WS_V + jj * (unsigned)V_HEAD;
    const int koff0 = (xcd * 16 - 16) * 768, voff0 = (xcd * 8 - 8) * 1280;
    const unsigned wb = (unsigned)WS_WO + grp * (D_ * D_ * 2) + ct * (5 * 20 * 1024) + q * 5120;
    static_for<0, 7>([&](auto ii) {
      constexpr int i = ii.value;
      const int p = w + 8 * i, ok = koff0 + (p - 12) * 1024, ov = voff0 + (p - 36) * 1024;
      const unsigned src = p < 12 ? qo + p * 1024 : p < 36 ? ko0 + (ok < 0 ? 0 : ok) : vo0 + (ov < 0 ? 0 : ov);
      glds16(ws + src, vl, lb + AT_Q + p * 1024);
      if constexpr (i >= 2) {
#pragma unroll
        for (int j = 0; j < 5; ++j) W[(i - 2) * 5 + j] = __builtin_bit_cast(bf16x8, ld16p(wr, vl + (i - 2) * 1024, wb + j * (20 * 1024)));
        cfence();
      }
    });
#pragma unroll
    for (int i = 0; i < 13; ++i) {
      int u = w + 8 * i;
      u = u < 100 ? u : 99;
      const int t = u / 5, j = u - t * 5;
      glds16(ws + WS_W2 + (unsigned)(ct * (5 * 20 * 1024) + (j * 20 + t) * 1024), vl, lb + L_W2 + u * 1024);
    }
#pragma unroll
    for (int i = 0; i < 4; ++i) {
      st_empty(wr, vst, (unsigned)(grp ? WS_H : WS_G) + (G4 + i) * 20480);
      st_empty(wr, grp ? vst : OOB, (unsigned)WS_X1 + (G4 + i) * 20480);
    }
    *(LAS unsigned*)(uintptr_t)(lb + L_SY + (tid_ & 127) * 4) = (tid_ & 127) == (C_OK - L_SY) / 4 ? 4u : 0u;
    ST(14);
    wait_vm<26>();
    __builtin_amdgcn_s_barrier();
    cfence();
    ST(2);
    const int tu = jj * 8 + w;
    auto feed = [&](auto hi) {
      constexpr int h = hi.value;
      __builtin_amdgcn_sched_barrier(0);
      if constexpr (h == 0) touch(tu < 40 ? x + (unsigned)(xcd * 327680 + tu * 8192) : ws + WS_W3 + (unsigned)((tu - 40) * 8192), l, lb);
      if constexpr (h == 4) {
        const int u = tu < 144 ? tu - 24 : 119;
        touch(tu < 24 ? ws + WS_W3 + (unsigned)((216 + tu) * 8192) : ws + WS_W4 + (unsigned)((u / 3) * 49152 + 24576 + (u % 3) * 8192), l, lb);
        ST(15);
      }
      __builtin_amdgcn_sched_barrier(0);
    };
    u32x2 om0, ot0, om1, ot1;
    attention<0>(smem, xcd, w, om0, ot0, feed);
    __builtin_amdgcn_sched_barrier(0);
    wait_vm<2>();
    __builtin_amdgcn_s_barrier();
    ST(16);
    attention<1>(smem, xcd, w, om1, ot1, feed);
    {
      const auto s0 = __builtin_amdgcn_permlane16_swap(om0[0], om1[0], false, false);
      const auto s1 = __builtin_amdgcn_permlane16_swap(om0[1], om1[1], false, false);
      const auto t0 = __builtin_amdgcn_permlane16_swap(ot0[0], ot1[0], false, false);
      const auto t1 = __builtin_amdgcn_permlane16_swap(ot0[1], ot1[1], false, false);
      const unsigned Of = (unsigned)WS_O + (xcd * 16 + 2 * w) * 20480 + (jj >> 3) * 5120;
      st16(wr, (g & 1) * 20480 + (g >> 1) * 256 + li * 16, Of + (jj & 7) * 512, u32x4{s0[0], s1[0], s0[1], s1[1]});
      st8(wr, g < 2 ? g * 20480 + li * 8 : OOB, Of + 4096 + (jj & 7) * 128, u32x2{t0[0], t1[0]});
    }
    ST(3);
    __builtin_amdgcn_s_barrier();
    cfence();
    ga = grp; gb = grp;
    asm volatile("" : "+s"(ga), "+s"(gb));
  }
  if (ga == 0) {
    CTX;
#pragma unroll
    for (int i = 0; i < 8; ++i) st_empty(wr, v6, (unsigned)WS_U + (B8 + i) * 49152);
    f32x4 cs[2];
#pragma unroll
    for (int i = 0; i < 2; ++i) cs[i] = __builtin_bit_cast(f32x4, ld16p(wr, ko * 32 + i * 16, (unsigned)WS_CS));
    m_phase<5, 5, 4, 1, 3, 0>(W, a, l, lb, wr, 0, q, [&](int i) { return (unsigned)WS_O + g4(i) * 20480 + q * 5120; }, lane, ring, slot2,
                              nohook STPASS(4));
#pragma unroll
    for (int f = 0; f < 25; ++f) W[f] = *(const LAS bf16x8*)(uintptr_t)(lb + L_W2 + (q * 25 + f) * 1024 + vl);
    lds_inc(lb + C_W2);
    lds_wait(lb + C_W2, 4);
    auto stage4 = [&](int f) { glds16(ws + (w4b + 4 * 6144 + (f % 5) * (48 * 1024) + (f / 5) * 1024), vl, lb + L_W + (q * 26 + f) * 1024); };
#pragma unroll
    for (int f = 0; f < 8; ++f) stage4(f);
    const int sr = li - ((q & 2) ? ((q & 1) ? -2 : 2) : ((q & 1) ? -1 : 1));
    const unsigned vg = (unsigned)(((sr >> 4) + 1) * 20480 + g * 256 + (sr & 15) * 16);
    auto sog = [&](int i) { return (unsigned)WS_G + (g4(i) - 1) * 20480 + q * 5120; };
    auto vog = [&](int i) { return (unsigned)(g4(i) * 16 + sr) < (unsigned)S_ ? vg : OOB; };
    {
      StatRd sd = e_stat0(lb, 4, li);
      static_for<0, 4>([&](auto i) {
        constexpr int n = 4 + i.value;
        float sum, sq, mean, var, m1, s1;
        e_stat1(sd, sum, sq, lb, n, li);
        ln_fast(sum, sq, mean, var, m1, s1);
        const float r1 = __builtin_amdgcn_rcpf(s1);
        const f32x2 mv = bfr2(f32x2{(mean - m1) * r1, var * r1 * r1});
        const float s2 = bfr(__builtin_amdgcn_sqrtf(bfr(mv[1] + 1e-5f)));
        float rr = r1 * __builtin_amdgcn_rcpf(s2);
        const float nm = -(m1 + mv[0] * s1) * rr;
        if (w == 0) *(LAS f32x2*)(uintptr_t)(lb + L_LN + (i * 16 + li) * 8) = f32x2{m1, r1};
        f32x4 c0 = cs[0] * nm, c1 = cs[1] * nm;
        asm volatile("" : "+v"(rr), "+v"(c0), "+v"(c1));
        f32x4 lo, hi;
        e_sum<4, 336>(lo, hi, lb, pe + (n & 1) * SLOT, n);
        if constexpr (i.value < 3) sd = e_stat0(lb, n + 1, li);
        if (i == 0) { ST(6); }
        lo = lo * rr + c0; hi = hi * rr + c1;
        st16(wr, vst, (unsigned)WS_G + g4(i) * 20480,
             u32x4{pk2v(gelu2(bfr2(lo2(lo)))), pk2v(gelu2(bfr2(hi2(lo)))), pk2v(gelu2(bfr2(lo2(hi)))), pk2v(gelu2(bfr2(hi2(hi))))});
        if constexpr (i.value < 3) {
#pragma unroll
          for (int f = 0; f < 6; ++f) stage4(8 + 6 * i + f);
        }
        if constexpr (i.value == 2) m_issue<5, 0>(a[0], wr, l, sog(0), vog(0));
      });
    }
    ST(7);
#pragma unroll
    for (int i = 0; i < 2; ++i) cs[i] = __builtin_bit_cast(f32x4, ld16p(wr, k6 * 32 + i * 16, (unsigned)WS_CS + D_ * 4));
    m_phase<5, 5, 4, 0, 3, 1>(W, a, l, lb, wr, 8, q, sog, vog, [&](int i) { return ring(8 + i); }, slot2,
                              [&](int i) { if (i == 0) lds_inc(lb + C_W4); } STPASS(8));
    {
      const unsigned p6 = lb + li * 400 + (3 * q + (g < 3 ? g : 2)) * 32;
      StatRd sd = e_stat0(lb, 12, li);
      static_for<0, 8>([&](auto i) {
        constexpr int n = 12 + i.value;
        if constexpr (i.value < 6) {
#pragma unroll
          for (int j = 0; j < 5; ++j) W[i.value * 5 + j] = __builtin_bit_cast(bf16x8, ld16p(wr, vl + i.value * 1024, w4b + j * (48 * 1024)));
          cfence();
        }
        if constexpr (i.value == 7) m_issue<6, 0>(a[0], wr, l, (unsigned)WS_U + g4(0) * 49152 + w * 6144, vl);
        float sum, sq, mean, var, m, s;
        e_stat1(sd, sum, sq, lb, n, li);
        ln_fast(sum, sq, mean, var, m, s);
        float rinv = __builtin_amdgcn_rcpf(s);
        const float nm = -m * rinv;
        f32x4 c0 = cs[0] * nm, c1 = cs[1] * nm;
        asm volatile("" : "+v"(rinv), "+v"(c0), "+v"(c1));
        f32x4 lo, hi;
        e_sum<4, 400>(lo, hi, lb, p6 + (n & 1) * SLOT, n);
        if constexpr (i.value < 7) sd = e_stat0(lb, n + 1, li);
        if (i == 0) { ST(10); }
        lo = lo * rinv + c0; hi = hi * rinv + c1;
        st16(wr, v6, (unsigned)WS_U + g8(i) * 49152,
             u32x4{pk2v(gelu2(bfr2(lo2(lo)))), pk2v(gelu2(bfr2(hi2(lo)))), pk2v(gelu2(bfr2(lo2(hi)))), pk2v(gelu2(bfr2(hi2(hi))))});
      });
      ST(11);
    }
  }
  if (gb != 0) {
    CTX;
    const rsrc_t xr = __builtin_amdgcn_make_buffer_rsrc((void*)x, 0, S_ * D_ * 2, 0x00020000);
    auto soh = [&](int i) { return (unsigned)WS_H + g4(i) * 20480 + q * 5120; };
    auto sox = [&](int i) { return (unsigned)WS_X1 + g8(i) * 20480 + q * 5120; };
    {
      u32x4 xv[4];
#pragma unroll
      for (int i = 0; i < 4; ++i) xv[i] = ld16p(xr, xo, g4(i) * (16 * D_ * 2));
      cfence();
#pragma unroll
      for (int i = 0; i < 4; ++i) {
        f32x4 lo, hi;
        e_sum<4, 336>(lo, hi, lb, pe + (i & 1) * SLOT, i);
        if (i == 0) { ST(4); }
        st16(wr, vst, (unsigned)WS_H + g4(i) * 20480,
             u32x4{pk2v(unpk(xv[i][0]) + bfr2(lo2(lo))), pk2v(unpk(xv[i][1]) + bfr2(hi2(lo))),
                   pk2v(unpk(xv[i][2]) + bfr2(lo2(hi))), pk2v(unpk(xv[i][3]) + bfr2(hi2(hi)))});
        if (i == 2) m_issue<5, 2>(a[0], wr, l, soh(0), vl);
      }
      ST(5);
    }
    m_phase<5, 5, 4, 2, 3, 1>(W, a, l, lb, wr, 4, q, soh, lane, [&](int i) { return ring(4 + i); }, slot2, nohook STPASS(6));
    {
      u32x4 hv[4];
#pragma unroll
      for (int i = 0; i < 4; ++i) hv[i] = ld16(wr, vft, (unsigned)WS_H + g4(i) * 20480);
      cfence();
      { const int u = wid < 120 ? wid : 119; touch(ws + WS_W4 + (unsigned)((u / 3) * 49152 + (u % 3) * 8192), l, lb); }
      const unsigned w3b = (unsigned)WS_W3 + ct16 * (6 * 20 * 1024) + q * 5120;
      auto load3 = [&](int f) { W[f] = __builtin_bit_cast(bf16x8, ld16p(wr, vl + (f / 6) * 1024, w3b + (f % 6) * (20 * 1024))); };
#pragma unroll
      for (int f = 0; f < 12; ++f) load3(f);
      cfence();
#pragma unroll
      for (int i = 0; i < 4; ++i) {
        const int n = 8 + i;
        f32x4 lo, hi;
        e_sum<4, 336>(lo, hi, lb, pe + (n & 1) * SLOT, n);
        if (i == 0) { ST(8); }
        const f32x2 mr = *(const LAS f32x2*)(uintptr_t)(lb + L_LN + (i * 16 + li) * 8);
        const float m1 = mr[0], r1 = mr[1];
        st16(wr, vst, (unsigned)WS_X1 + g4(i) * 20480,
             u32x4{pk2v(bfr2(bfr2(unpk(hv[i][0]) - m1) * r1) + bfr2(lo2(lo))), pk2v(bfr2(bfr2(unpk(hv[i][1]) - m1) * r1) + bfr2(hi2(lo))),
                   pk2v(bfr2(bfr2(unpk(hv[i][2]) - m1) * r1) + bfr2(lo2(hi))), pk2v(bfr2(bfr2(unpk(hv[i][3]) - m1) * r1) + bfr2(hi2(hi)))});
        if (i < 3) {
#pragma unroll
          for (int f = 0; f < 6; ++f) load3(12 + 6 * i + f);
          cfence();
        }
        if (i == 2) { m_issue<5, 2>(a[0], wr, l, sox(0), vl); m_issue<5, 2>(a[1], wr, l, sox(1), vl); }
      }
      ST(9);
    }
    m_phase<6, 5, 8, 2, 3, 2>(W, a, l, lb, wr, 12, q, sox, lane, [&](int i) { return ring(12 + i); }, slot2, nohook STPASS(10));
    m_issue<6, 0>(a[0], wr, l, (unsigned)WS_U + g4(0) * 49152 + w * 6144, vl);
#pragma unroll
    for (int j = 1; j < 5; ++j) W[25 + j] = __builtin_bit_cast(bf16x8, ld16p(wr, vl + 5 * 1024, w4b + j * (48 * 1024)));
    lds_wait(lb + C_W4, 4);
#pragma unroll
    for (int f = 0; f < 26; ++f) W[f] = *(const LAS bf16x8*)(uintptr_t)(lb + L_W + (q * 26 + f) * 1024 + vl);
    lds_inc(lb + C_WC);
  }
  {
    CTX;
    const rsrc_t orr = __builtin_amdgcn_make_buffer_rsrc(out, 0, S_ * D_ * 2, 0x00020000);
    u32x4 hv[2], xv[2];
#pragma unroll
    for (int i = 0; i < 2; ++i) {
      const unsigned to = g4(2 * i + grp) * 20480;
      hv[i] = ld16(wr, vft, (unsigned)WS_H + to); xv[i] = ld16(wr, vft, (unsigned)WS_X1 + to);
    }
    auto fin = [&](int p, auto si) {
      constexpr int s = si.value;
      f32x4 lo, hi;
      e_sum<8, 336>(lo, hi, lb, pe + (p % 3) * SLOT7, 20 + p);
      const u32x4 y = {pk2v(unpk(hv[s][0]) + bfr2(unpk(xv[s][0]) + bfr2(lo2(lo)))), pk2v(unpk(hv[s][1]) + bfr2(unpk(xv[s][1]) + bfr2(hi2(lo)))),
                       pk2v(unpk(hv[s][2]) + bfr2(unpk(xv[s][2]) + bfr2(lo2(hi)))), pk2v(unpk(hv[s][3]) + bfr2(unpk(xv[s][3]) + bfr2(hi2(hi))))};
      st16r<0>(orr, act ? xo : OOB, g4(p) * (16 * D_ * 2), y);
    };
    m_phase<5, 6, 4, 0, 2, 1>(W, a, l, lb, wr, 20, w, [&](int i) { return (unsigned)WS_U + g4(i) * 49152 + w * 6144; }, lane,
        [&](auto i) { return lb + (i.value == 0 ? C_F + 19 * 4 : i.value == 1 ? C_WC : i.value == 2 ? C_OK : C_F + 20 * 4); },
        [&](auto i) { return (unsigned)((i.value % 3) * SLOT7); },
        [&](auto i) {
          if constexpr (i.value == 1) { if (grp == 0) fin(0, std::integral_constant<int, 0>{}); }
          if constexpr (i.value == 2) { if (grp == 1) fin(1, std::integral_constant<int, 0>{}); }
          if constexpr (i.value == 3) fin(2 + grp, std::integral_constant<int, 1>{});
        } STPASS(12));
    STDUMP2(ws, 2 + grp);
  }
}

extern "C" void kernel_launch(void* const* d_in, const int* in_sizes, int n_in, void* d_out, int out_size,
                              void* d_ws, size_t ws_size, hipStream_t stream) {
  const char* x = (const char*)d_in[0];
  const char* wqkv = (const char*)d_in[1];
  const char* wo = (const char*)d_in[2];
  const char* w1 = (const char*)d_in[4];
  const char* w2 = (const char*)d_in[5];
  const char* w3 = (const char*)d_in[6];
  const char* w4 = (const char*)d_in[7];
  char* ws = (char*)d_ws;
  if (ws_size < WS_END) return;
  constexpr int LQ = 4 * 32768 + 2048 + 16384;
  (void)hipFuncSetAttribute((const void*)k1_qkv, hipFuncAttributeMaxDynamicSharedMemorySize, LQ);
  (void)hipFuncSetAttribute((const void*)kp_main, hipFuncAttributeMaxDynamicSharedMemorySize, LDS_TOT);
  k1_qkv<<<256, 384, LQ, stream>>>(x, wqkv, wo, w1, w2, w3, w4, ws);
  kp_main<<<256, 512, LDS_TOT, stream>>>(x, ws, (char*)d_out);
}
```

```cpp
#include <hip/hip_runtime.h>
#include <hip/hip_bf16.h>
#include <stdint.h>
#include <type_traits>

typedef __attribute__((ext_vector_type(8))) __bf16 bf16x8;
typedef __attribute__((ext_vector_type(4))) __bf16 bf16x4;
typedef __attribute__((ext_vector_type(2))) __bf16 bf16x2;
typedef __attribute__((ext_vector_type(4))) short s16x4;
typedef __attribute__((ext_vector_type(4))) float f32x4;
typedef __attribute__((ext_vector_type(2))) unsigned u32x2;
typedef __attribute__((ext_vector_type(4))) unsigned u32x4;
#define LAS __attribute__((address_space(3)))
#define DEV __device__ __forceinline__

constexpr int S_ = 2048, D_ = 640, F_ = 1536, H_ = 32;

constexpr size_t QK_HEAD = 128 * 768;
constexpr size_t V_HEAD  = 64 * 1280;
constexpr size_t WS_Q  = 0;
constexpr size_t WS_K  = WS_Q + H_ * QK_HEAD;
constexpr size_t WS_V  = WS_K + H_ * QK_HEAD;
constexpr size_t WS_O  = WS_V + H_ * V_HEAD;
constexpr size_t WS_H  = WS_O + (size_t)S_ * D_ * 2;
constexpr size_t WS_G  = WS_H + (size_t)S_ * D_ * 2;
constexpr size_t WS_X1 = WS_G + (size_t)S_ * D_ * 2;
constexpr size_t WS_U  = WS_X1 + (size_t)S_ * D_ * 2;
constexpr size_t WS_CS = WS_U + (size_t)S_ * F_ * 2;
constexpr size_t WS_WO = WS_CS + 16384;
constexpr size_t WS_W1 = WS_WO + (size_t)D_ * D_ * 2;
constexpr size_t WS_W2 = WS_W1 + (size_t)D_ * D_ * 2;
constexpr size_t WS_W3 = WS_W2 + (size_t)D_ * D_ * 2;
constexpr size_t WS_W4 = WS_W3 + (size_t)D_ * F_ * 2;
constexpr size_t WS_END = WS_W4 + (size_t)D_ * F_ * 2;
static_assert(WS_CS - WS_O == 256 * 65536, "fill");
#define FILL_AUX 16
constexpr unsigned EMPTY = 0xffffffffu;

template <int I, int N, class Fn> DEV void static_for(Fn&& f) {
  if constexpr (I < N) { f(std::integral_constant<int, I>{}); static_for<I + 1, N>(f); }
}
template <int N> DEV void wait_vm() { asm volatile("s_waitcnt vmcnt(%0) lgkmcnt(0)" ::"n"(N) : "memory"); }
DEV void glds16(const char* sbase, unsigned voff, unsigned lds) {
  asm volatile("s_mov_b32 m0, %0\n\ts_nop 0\n\tglobal_load_lds_dwordx4 %1, %2" ::"s"(lds), "v"(voff), "s"(sbase) : "memory");
}
DEV unsigned lds_addr(const void* p) { return (unsigned)(uintptr_t)(LAS const void*)p; }
#define ST(i)
#define DT(ph, i)
#define STR(i)
#define STDECL
#define STDUMP(o, ws, kid)
#define STARG
#define STPASS(i)
#define STDUMP2(ws, kid)
DEV void wait_lgkm0() { asm volatile("s_waitcnt lgkmcnt(0)" ::: "memory"); }
DEV void cfence() { asm volatile("" ::: "memory"); }
DEV float bfr(float x) { return (float)(__bf16)x; }
DEV unsigned pk2(float a, float b) { bf16x2 v = {(__bf16)a, (__bf16)b}; return __builtin_bit_cast(unsigned, v); }
DEV float bf_lo(unsigned u) { return __builtin_bit_cast(float, u << 16); }
DEV float bf_hi(unsigned u) { return __builtin_bit_cast(float, u & 0xffff0000u); }
DEV f32x4 mfma16(bf16x8 a, bf16x8 b, f32x4 c) { return __builtin_amdgcn_mfma_f32_16x16x32_bf16(a, b, c, 0, 0, 0); }
DEV unsigned ft_off(int row, int k, int KS) {
  return (unsigned)(((row >> 4) * KS + (k >> 5)) * 1024 + ((row & 15) + 16 * ((k >> 3) & 3)) * 16 + (k & 7) * 2);
}

DEV void ln_stats(float sum, float sq, float& mean, float& var, float& m, float& s) {
  mean = sum * (1.0f / D_);
  var = fmaxf(sq * (1.0f / D_) - mean * mean, 0.f);
  m = bfr(mean);
  s = bfr(sqrtf(bfr(bfr(var) + 1e-5f)));
}

DEV void dot_sum(float& s, bf16x8 f) {
  const bf16x2 one2 = {(__bf16)1.0f, (__bf16)1.0f};
#pragma unroll
  for (int e = 0; e < 4; ++e) { bf16x2 v = {f[2 * e], f[2 * e + 1]}; s = __builtin_amdgcn_fdot2_f32_bf16(v, one2, s, false); }
}
DEV void dot_sq(float& s, bf16x8 f) {
#pragma unroll
  for (int e = 0; e < 4; ++e) { bf16x2 v = {f[2 * e], f[2 * e + 1]}; s = __builtin_amdgcn_fdot2_f32_bf16(v, v, s, false); }
}
DEV unsigned f2u(float v) { return __builtin_bit_cast(unsigned, v); }
DEV float u2f(unsigned v) { return __builtin_bit_cast(float, v); }
DEV float red_g(float v) {
  const auto r = __builtin_amdgcn_permlane16_swap(f2u(v), f2u(v), false, false);
  v = u2f(r[0]) + u2f(r[1]);
  const auto s = __builtin_amdgcn_permlane32_swap(f2u(v), f2u(v), false, false);
  return u2f(s[0]) + u2f(s[1]);
}
DEV float max_g(float v) {
  const auto r = __builtin_amdgcn_permlane16_swap(f2u(v), f2u(v), false, false);
  v = fmaxf(u2f(r[0]), u2f(r[1]));
  const auto s = __builtin_amdgcn_permlane32_swap(f2u(v), f2u(v), false, false);
  return fmaxf(u2f(s[0]), u2f(s[1]));
}

DEV unsigned wrm_voff(int l, int q, int ldw) {
  return (unsigned)((l >> 4) * ldw + ((((l & 15) >> 1) ^ ((l >> 4) | (q << 2))) * 32) + (l & 1) * 16);
}
DEV bf16x8 wrm_frag(const char* p) {
  s16x4 lo = __builtin_amdgcn_ds_read_tr16_b64_v4i16((LAS s16x4*)(p));
  s16x4 hi = __builtin_amdgcn_ds_read_tr16_b64_v4i16((LAS s16x4*)(p + 1024));
  typedef __attribute__((ext_vector_type(8))) short s16x8;
  s16x8 v = __builtin_shufflevector(lo, hi, 0, 1, 2, 3, 4, 5, 6, 7);
  return __builtin_bit_cast(bf16x8, v);
}
DEV unsigned wrm_lane(int l, int jhi, int jj) {
  const int g = l >> 4, i = l & 15;
  return (unsigned)((8 * g + (i >> 2)) * 256 + ((((jhi ^ (g & 1)) << 2) | (jj ^ (i >> 2))) * 32) + (i & 3) * 8);
}

struct WJob { const char* src; char* dst; const char* code; char* fill; int ldw, wks, r4, perm; unsigned coff; };
DEV WJob make_job(int id, const char* wo, const char* w1, const char* w2, const char* w3, const char* w4, char* ws) {
  const bool live = id < 390;
  const int b = live ? id : 389;
  const char* wsrc; char* wdst; int ldw = D_ * 2, wks = 20, kt, nt;
  if (b < 150) {
    const int mi = b / 50, u = b - mi * 50;
    wsrc = mi == 0 ? wo : (mi == 1 ? w1 : w2); wdst = ws + WS_WO + (size_t)mi * (D_ * D_ * 2);
    kt = u / 5; nt = u - kt * 5;
  } else if (b < 270) {
    const int u = b - 150; wsrc = w3; wdst = ws + WS_W3; ldw = F_ * 2; kt = u / 12; nt = u - kt * 12;
  } else {
    const int u = b - 270; wsrc = w4; wdst = ws + WS_W4; wks = 48; kt = u / 5; nt = u - kt * 5;
  }
  WJob j;
  j.src = wsrc + nt * 256;
  j.r4 = kt * 16; j.perm = b < 50;
  j.dst = live ? wdst + (size_t)(nt * 8 * wks + kt * 2) * 1024 : nullptr;
  j.ldw = ldw; j.wks = wks;
  return j;
}

template <bool SWAP>
struct GemmQ {
  f32x4 acc[4][4];
  STDECL;
  static constexpr int NG = 4, D = 3, NGRP = 10, SB = 32768, XCH = NG * SB;
  DEV bool run(char* smem, const char* Ab, const char* Wb, int k0, const WJob& job) {
    STR(0); ST(1);
    constexpr int LDA = D_ * 2, LDW = 3 * D_ * 2;
    const int tid = threadIdx.x, l = tid & 63;
    const int wave = __builtin_amdgcn_readfirstlane(tid >> 6);
    if (wave >= 4) {
      const bool isw = wave == 5;
      unsigned vo[2];
#pragma unroll
      for (int q = 0; q < 2; ++q)
        vo[q] = isw ? wrm_voff(l, q, LDW) : (unsigned)((l >> 3) * LDA + (((l & 7) ^ (q * 4 + (l >> 4))) * 16));
      const unsigned vx[4] = {vo[0], isw ? vo[0] : vo[1], isw ? vo[1] : vo[0], vo[1]};
      const unsigned dst0 = lds_addr(smem) + (isw ? 16384 : 0);
      const char* src0 = isw ? Wb : Ab;
      const int gstr = isw ? 64 * LDW : 128, istr = isw ? 4 * LDW : 8 * LDA;
      auto issue = [&](int grp) {
        int kg = grp + k0;
        kg = kg >= NGRP ? kg - NGRP : kg;
        const char* sp = src0 + (size_t)kg * gstr;
#pragma unroll
        for (int i = 0; i < 16; ++i) glds16(sp + (size_t)i * istr, vx[i & 3], dst0 + (grp % NG) * SB + i * 1024);
      };
      static_for<0, D>([&](auto gi) { issue(gi.value); });
      static_for<0, NGRP>([&](auto gi) {
        constexpr int grp = gi.value;
        constexpr int issued = grp + D < NGRP ? grp + D : NGRP;
        wait_vm<16 * (issued - grp - 1) + (grp > NGRP - D ? 16 : 0) + (grp >= NGRP - D ? 1 : 0)>();
        __builtin_amdgcn_s_barrier();
        cfence();
        if constexpr (grp + D < NGRP) issue(grp + D);
        if constexpr (grp == NGRP - D - 1) {
          asm volatile("s_mov_b32 m0, %0\n\ts_nop 0\n\tglobal_load_lds_dword %1, %2" ::"s"(lds_addr(smem) + XCH + 1536), "v"(job.coff), "s"(job.code) : "memory");
        }
        if constexpr (grp == NGRP - D) {
#pragma unroll
          for (int i = 0; i < 16; ++i) {
            int r4 = job.r4 + i;
            const int kq = r4 / 40, rr = r4 - kq * 40;
            if (job.perm) r4 = rr < 32 ? (8 * kq + (rr >> 2)) * 5 + (rr & 3) : (8 * kq + rr - 32) * 5 + 4;
            glds16(job.src + (size_t)r4 * 4 * job.ldw, wrm_voff(l, (i >> 1) & 1, job.ldw), dst0 + ((grp - 1) % NG) * SB + i * 1024);
          }
        }
      });
      __builtin_amdgcn_s_barrier();
      wait_vm<0>();
      if (job.dst) {
        const char* tb = smem + ((NGRP - D - 1) % NG) * SB + (isw ? 16384 : 0);
#pragma unroll
        for (int kk = 0; kk < 2; ++kk)
#pragma unroll
          for (int j = 0; j < 8; ++j) {
            const bf16x8 f = wrm_frag(tb + kk * 8192 + wrm_lane(l, j >> 2, j & 3));
            *(bf16x8*)(job.dst + (size_t)(j * job.wks + kk) * 1024 + l * 16) = f;
          }
      }
      return false;
    }
    const int wm = wave >> 1, wn = wave & 1, g = l >> 4;
    float rs[2] = {0.f, 0.f}, rq[2] = {0.f, 0.f}, cd[2] = {0.f, 0.f};
#pragma unroll
    for (int i = 0; i < 4; ++i)
#pragma unroll
      for (int j = 0; j < 4; ++j) acc[i][j] = f32x4{0.f, 0.f, 0.f, 0.f};
    const unsigned a0 = (unsigned)((wm * 64 + (l & 15)) * 128 + ((g ^ ((l & 15) >> 1)) * 16));
    const char* al[2][2] = {{smem + a0 + wn * 4096, smem + a0 + 4096 - wn * 4096},
                            {smem + (a0 ^ 64u) + wn * 4096, smem + (a0 ^ 64u) + 4096 - wn * 4096}};
    const char* wl[4];
#pragma unroll
    for (int j = 0; j < 4; ++j) wl[j] = smem + 16384 + wrm_lane(l, wn, j ^ (2 * wm));
    bf16x8 af[2][4], wf[2][4];
    const __amdgpu_buffer_rsrc_t fr = __builtin_amdgcn_make_buffer_rsrc(job.fill, 0, 65536, 0x00020000);
    auto ldfrag = [&](auto ti) {
      constexpr int t = ti.value, kk = t & 1, so = ((t >> 1) % NG) * SB;
#pragma unroll
      for (int i = 0; i < 4; ++i) af[kk][i] = *(const bf16x8*)(al[kk][i >> 1] + so + (i & 1) * 2048);
#pragma unroll
      for (int j = 0; j < 4; ++j) wf[kk][j] = wrm_frag(wl[j] + so + kk * 8192);
    };
    __builtin_amdgcn_s_barrier();
    cfence();
    ST(3);
    ldfrag(std::integral_constant<int, 0>{});
    static_for<0, 2 * NGRP>([&](auto ti) {
      constexpr int t = ti.value, kk = t & 1;
      if constexpr (t + 1 < 2 * NGRP) {
        if constexpr (kk == 1) {
          wait_lgkm0();
          __builtin_amdgcn_s_barrier();
          cfence();
          ST(3 + (t + 1) / 2);
        }
        ldfrag(std::integral_constant<int, t + 1>{});
      }
      if constexpr (t >= 2 && t < 18) __builtin_amdgcn_raw_buffer_store_b128(u32x4{EMPTY, EMPTY, EMPTY, EMPTY}, fr, tid * 16, (t - 2) * 4096, FILL_AUX);
      __builtin_amdgcn_sched_barrier(0);
#pragma unroll
      for (int i = 0; i < 4; ++i)
#pragma unroll
        for (int j = 0; j < 4; ++j)
          acc[i][j] = SWAP ? mfma16(wf[kk][j], af[kk][i], acc[i][j]) : mfma16(af[kk][i], wf[kk][j], acc[i][j]);
#pragma unroll
      for (int i = 0; i < 2; ++i) { dot_sum(rs[i], af[kk][i]); dot_sq(rq[i], af[kk][i]); dot_sum(cd[i], wf[kk][i]); }
#pragma unroll
      for (int q = 0; q < 8; ++q) {
        __builtin_amdgcn_sched_group_barrier(0x8, 2, 0);
        __builtin_amdgcn_sched_group_barrier(0x2, 3, 0);
      }
      asm volatile("" : "+v"(rs[0]), "+v"(rs[1]), "+v"(rq[0]), "+v"(rq[1]), "+v"(cd[0]), "+v"(cd[1]));
      __builtin_amdgcn_sched_barrier(0);
    });
    ST(21);
    float2* xs = (float2*)(smem + XCH);
    float* xc = (float*)(smem + XCH + 1024);
#pragma unroll
    for (int i = 0; i < 2; ++i) {
      const float a = red_g(rs[i]), b = red_g(rq[i]), c = red_g(cd[i]);
      float mean, var, m, sd;
      ln_stats(a, b, mean, var, m, sd);
      const float rinv = 1.0f / sd;
      if (l < 16) { xs[(wm * 4 + wn * 2 + i) * 16 + l] = float2{rinv, -m * rinv}; xc[(wn * 4 + wm * 2 + i) * 16 + l] = c; }
    }
    wait_lgkm0();
    __builtin_amdgcn_s_barrier();
    cfence();
    return true;
  }
};

template <bool ISV>
DEV void qkv_body(char* smem, const char* x, const char* wqkv, char* ws, int rt, int ct, int k0, const WJob& job) {
  using GQ = GemmQ<!ISV>;
  GQ gm;
  const int row0 = rt * 128, n0 = ct * 128;
  if (!gm.run(smem, x + (size_t)row0 * (D_ * 2), wqkv + n0 * 2, k0, job)) return;
  const int tid = threadIdx.x, l = tid & 63, wave = tid >> 6, wm = wave >> 1, wn = wave & 1, g = l >> 4;
  const float2* xs = (const float2*)(smem + GQ::XCH);
  const float* xc = (const float*)(smem + GQ::XCH + 1024);
  if constexpr (!ISV) {
    f32x4 cs[4];
    unsigned po[4];
#pragma unroll
    for (int j = 0; j < 4; ++j) {
      const int ja = wn * 4 + (j ^ (2 * wm));
      cs[j] = *(const f32x4*)(xc + ja * 16 + 4 * g);
      const int c = (n0 + ja * 16 + 4 * g) % D_, head = c / 20, d0 = c - head * 20;
      po[j] = (unsigned)(head * (int)QK_HEAD + (d0 >> 3) * 256 + (d0 & 7) * 2) | (d0 == 16 ? 1u : 0u);
    }
    char* dstb = ws + (ct < 5 ? WS_Q : WS_K) + (size_t)(row0 >> 4) * 768 + (l & 15) * 16;
#pragma unroll
    for (int i = 0; i < 4; ++i) {
      const int ia = wm * 4 + (i ^ (2 * wn));
      const float2 st = xs[ia * 16 + (l & 15)];
#pragma unroll
      for (int j = 0; j < 4; ++j) {
        const f32x4 y = gm.acc[i][j] * st.x + cs[j] * st.y;
        char* p = dstb + ia * 768 + (po[j] & ~1u);
        if (po[j] & 1u) *(u32x4*)p = u32x4{pk2(y[0], y[1]), pk2(y[2], y[3]), 0u, 0u};
        else *(u32x2*)p = u32x2{pk2(y[0], y[1]), pk2(y[2], y[3])};
      }
    }
  } else {
    float cd[4];
    unsigned po[4];
#pragma unroll
    for (int j = 0; j < 4; ++j) {
      const int ja = wn * 4 + (j ^ (2 * wm));
      cd[j] = xc[ja * 16 + (l & 15)];
      const int c = n0 - 2 * D_ + ja * 16 + (l & 15), head = c / 20, d = c - head * 20;
      po[j] = (unsigned)(head * (int)V_HEAD + (d < 16 ? (g * 16 + d) * 16 : 1024 + (g * 4 + d - 16) * 16));
    }
    char* dstb = ws + WS_V + (size_t)(row0 >> 5) * 1280;
#pragma unroll
    for (int i = 0; i < 4; ++i) {
      const int ia = wm * 4 + (i ^ (2 * wn));
      const f32x4 s01 = *(const f32x4*)(xs + ia * 16 + 4 * g), s23 = *(const f32x4*)(xs + ia * 16 + 4 * g + 2);
      const f32x4 rr = {s01[0], s01[2], s23[0], s23[2]}, nm = {s01[1], s01[3], s23[1], s23[3]};
#pragma unroll
      for (int j = 0; j < 4; ++j) {
        const f32x4 y = gm.acc[i][j] * rr + nm * cd[j];
        *(u32x2*)(dstb + (ia >> 1) * 1280 + (i & 1) * 8 + po[j]) = u32x2{pk2(y[0], y[1]), pk2(y[2], y[3])};
      }
    }
  }
  STDUMP(gm, ws, 1);
}

DEV void colsum_job(char* smem, const char* w1, const char* w3, float* cs, int blk) {
  const int tid = threadIdx.x, c = tid % 17, rg = tid / 17;
  const int gc = blk * 17 + c;
  const char* base = gc < 80 ? w1 + gc * 16 : w3 + (gc - 80) * 16;
  const int ld = gc < 80 ? D_ * 2 : F_ * 2;
  float a[8] = {0.f, 0.f, 0.f, 0.f, 0.f, 0.f, 0.f, 0.f};
  if (rg < 20) {
#pragma unroll 16
    for (int r = rg; r < D_; r += 20) {
      u32x4 v = *(const u32x4*)(base + (size_t)r * ld);
#pragma unroll
      for (int e = 0; e < 4; ++e) { a[2 * e] += bf_lo(v[e]); a[2 * e + 1] += bf_hi(v[e]); }
    }
  }
  float* sm = (float*)smem;
  if (rg < 20) {
#pragma unroll
    for (int e = 0; e < 8; ++e) sm[rg * 136 + c * 8 + e] = a[e];
  }
  __syncthreads();
  if (tid < 136) {
    float t = 0.f;
#pragma unroll
    for (int r = 0; r < 20; ++r) t += sm[r * 136 + tid];
    cs[blk * 136 + tid] = t;
  }
}

__global__ __launch_bounds__(384) void k1_qkv(const char* x, const char* wqkv, const char* wo, const char* w1, const char* w2,
                                              const char* w3, const char* w4, char* ws) {
  extern __shared__ __attribute__((aligned(16))) char smem[];
  const int b = blockIdx.x, xcd = b & 7, j = b >> 3, r = j & 3, c = j >> 2;
  const char* pc;
  unsigned c0, c1;
  asm volatile("s_getpc_b64 %0\nmx_k1_pc:\n\ts_mov_b32 %1, mx_kp_begin-mx_k1_pc\n\ts_mov_b32 %2, mx_kp_end-mx_k1_pc" : "=s"(pc), "=s"(c0), "=s"(c1));
  if (threadIdx.x < 64) {
    const unsigned o = j * 8192 + threadIdx.x * 128;
    if (j * 8192 < c0)
      asm volatile("s_mov_b32 m0, %0\n\ts_nop 0\n\tglobal_load_lds_dword %1, %2" ::"s"(lds_addr(smem) + 4 * 32768 + 1536), "v"(o < c0 ? o : c0), "s"(pc) : "memory");
  }
  if ((xcd & 1) && c == 7) {
    colsum_job(smem, w1, w3, (float*)(ws + WS_CS), (xcd >> 1) * 4 + r);
    if (threadIdx.x < 256) {
#pragma unroll
      for (int i = 0; i < 16; ++i) *(u32x4*)(ws + WS_O + b * 65536 + i * 4096 + threadIdx.x * 16) = u32x4{EMPTY, EMPTY, EMPTY, EMPTY};
    }
    return;
  }
  const int rt = (xcd >> 1) * 4 + r, ct = (xcd & 1) * 8 + c;
  const int wave = __builtin_amdgcn_readfirstlane(threadIdx.x >> 6);
  WJob job = make_job((rt * 15 + ct) * 2 + (wave & 1), wo, w1, w2, w3, w4, ws);
  {
    const unsigned o = c0 + ((j & 15) * 2 + (wave & 1)) * 2048 + (threadIdx.x & 15) * 128;
    job.code = pc; job.coff = o < c1 ? o : c1;
    job.fill = ws + WS_O + b * 65536;
  }
  const int k0 = (3 * r + 4 * c) % 10;
  if (ct >= 10) qkv_body<true>(smem, x, wqkv, ws, rt, ct, k0, job);
  else qkv_body<false>(smem, x, wqkv, ws, rt, ct, k0, job);
}

typedef __attribute__((ext_vector_type(2))) float f32x2;
typedef unsigned long long u64;
DEV f32x2 unpk(unsigned u) { return f32x2{bf_lo(u), bf_hi(u)}; }
DEV unsigned pk2v(f32x2 v) { return pk2(v[0], v[1]); }
DEV f32x2 bfr2(f32x2 v) { unsigned u = pk2v(v); asm("" : "+v"(u)); return unpk(u); }
DEV void ln_fast(float sum, float sq, float& mean, float& var, float& m, float& s) {
  mean = sum * (1.0f / D_);
  var = fmaxf(sq * (1.0f / D_) - mean * mean, 0.f);
  const f32x2 r = bfr2(f32x2{mean, var});
  m = r[0];
  s = bfr(__builtin_amdgcn_sqrtf(bfr(r[1] + 1e-5f)));
}
DEV f32x2 gelu2(f32x2 x) {
  const float c0 = -2.0f * 0.7978845608028654f * 1.4426950408889634f, c1 = c0 * 0.044715f;
  const f32x2 z = x * (x * x * c1 + c0);
  const f32x2 d = f32x2{__builtin_amdgcn_exp2f(z[0]), __builtin_amdgcn_exp2f(z[1])} + 1.0f;
  return x * f32x2{__builtin_amdgcn_rcpf(d[0]), __builtin_amdgcn_rcpf(d[1])};
}
DEV f32x2 lo2(f32x4 v) { return f32x2{v[0], v[1]}; }
DEV f32x2 hi2(f32x4 v) { return f32x2{v[2], v[3]}; }

typedef __amdgpu_buffer_rsrc_t rsrc_t;
constexpr unsigned OOB = 0x40000000u;
DEV unsigned full(unsigned v) { return v < EMPTY ? v : EMPTY - 1; }
DEV void st8(rsrc_t r, unsigned v, unsigned s, u32x2 d) { __builtin_amdgcn_raw_buffer_store_b64(u32x2{full(d[0]), full(d[1])}, r, v, s, 16); }
template <int AUX> DEV void st16r(rsrc_t r, unsigned v, unsigned s, u32x4 d) {
  asm("" : "+v"(d));
  __builtin_amdgcn_raw_buffer_store_b128(d, r, v, s, AUX);
  asm volatile("s_nop 1" ::"v"(d) : "memory");
}
DEV void st16(rsrc_t r, unsigned v, unsigned s, u32x4 d) { st16r<16>(r, v, s, u32x4{full(d[0]), full(d[1]), full(d[2]), full(d[3])}); }
DEV void st_empty(rsrc_t r, unsigned v, unsigned s) { st16r<16>(r, v, s, u32x4{EMPTY, EMPTY, EMPTY, EMPTY}); }
DEV u32x4 ld16(rsrc_t r, unsigned v, unsigned s) { return __builtin_amdgcn_raw_buffer_load_b128(r, v, s, 16); }
DEV u32x2 ld8(rsrc_t r, unsigned v, unsigned s) { return __builtin_amdgcn_raw_buffer_load_b64(r, v, s, 16); }
DEV u32x4 ld16p(rsrc_t r, unsigned v, unsigned s) { return __builtin_amdgcn_raw_buffer_load_b128(r, v, s, 0); }
DEV void vm0() { __builtin_amdgcn_s_waitcnt(0x0f70); cfence(); }

constexpr int LDS_TOT = 163840;
constexpr int AT_Q = 0, AT_K = AT_Q + 16 * 768, AT_V = AT_K + 32 * 768;
constexpr int SLOT = 25600, SLOT7 = 43008;
constexpr int L_W = 51200, L_W2 = 57344;
constexpr int L_SX = 159744;
constexpr int L_SY = 161792;
constexpr int C_P = L_SY, C_S = L_SY + 128, C_F = L_SY + 256, C_W2 = L_SY + 384, C_W4 = L_SY + 388, C_WC = L_SY + 392, C_OK = L_SY + 396, C_WL = L_SY + 400;
constexpr int L_LN = L_SY + 512;
constexpr int L_DUM = L_SY + 1024;
DEV void touch(const char* p, int l, unsigned lb) {
  asm volatile("s_mov_b32 m0, %0\n\ts_nop 0\n\tglobal_load_lds_dword %1, %2" ::"s"(lb + L_DUM + 512), "v"(l * 128), "s"(p) : "memory");
}
DEV void lds_inc(unsigned a) {
  asm volatile("s_mov_b64 exec, 1\n\tds_add_u32 %0, %1\n\ts_mov_b64 exec, -1" ::"v"(a), "v"(1u) : "memory");
}
DEV void lds_wait(unsigned a, unsigned target) {
  for (int it = 0; it < (1 << 22); ++it) {
    const unsigned v = __builtin_amdgcn_readfirstlane(*(volatile LAS unsigned*)(uintptr_t)a);
    if (v >= target) break;
  }
  cfence();
}
DEV f32x4 lds_ld4(unsigned a) { return *(const LAS f32x4*)(uintptr_t)a; }
DEV unsigned max3u(unsigned a, unsigned b, unsigned c) { a = a > b ? a : b; return a > c ? a : c; }

template <int KW, int MODE>
DEV void m_issue(u32x4 (&a)[6], rsrc_t wr, int l, unsigned s, unsigned v) {
#pragma unroll
  for (int k = 0; k < (MODE == 1 ? KW - 1 : KW); ++k) a[k] = ld16(wr, v + k * 1024, s);
  if constexpr (MODE == 1) {
    const unsigned t = (l >> 4) * 256 + (l & 15) * 8 + 4096;
    const u32x2 t0 = ld8(wr, t, s), t1 = ld8(wr, t + 128, s);
    a[KW - 1] = u32x4{t0[0], t0[1], t1[0], t1[1]};
  }
  cfence();
}
template <int NT, int KW, int NTILE, int MODE, int NB, int PRE, class TS, class TV, class TG, class TL, class TH>
DEV void m_phase(const bf16x8 (&W)[30], u32x4 (&a)[3][6], int l, unsigned lb, rsrc_t wr, int n0, int part, TS&& so, TV&& vo, TG&& guard,
                 TL&& slot, TH&& hook STARG) {
  constexpr int RS = NT * 64 + 16;
  const int g = l >> 4, li = l & 15;
  const unsigned pw = lb + part * (16 * RS) + li * RS + g * 16;
  const bool dg = g == (li >> 2);
  const unsigned sx = dg ? lb + L_SX + li * 32 + part * 8 : lb + L_DUM + l * 8, sm = dg ? 512u : 0u;
  static_for<PRE, NB - 1>([&](auto i) { m_issue<KW, MODE>(a[i.value], wr, l, so(i), vo(i)); });
  static_for<0, NTILE>([&](auto i) {
    constexpr int B = i.value % NB;
    const int n = n0 + i;
    auto bad = [&]() {
      unsigned m = 0;
#pragma unroll
      for (int k = 0; k < KW; ++k) m = max3u(max3u(m, a[B][k][0], a[B][k][1]), a[B][k][2], a[B][k][3]);
      return __builtin_amdgcn_ballot_w64(m == EMPTY) != 0;
    };
    if (bad()) {
      int it = 0;
#pragma unroll 1
      do { m_issue<KW, MODE>(a[B], wr, l, so(i), vo(i)); vm0(); } while (bad() && ++it < (1 << 16));
    }
    cfence();
    if (i == 0) { ST(STI); }
    DT(STI, 2 + 4 * i.value);
    const f32x4 z4 = {0.f, 0.f, 0.f, 0.f};
    f32x4 sa = z4, qa = z4;
    if constexpr (MODE == 2) {
      const u32x4 one = {0x3F803F80u, 0x3F803F80u, 0x3F803F80u, 0x3F803F80u};
#pragma unroll
      for (int k = 0; k < KW; ++k) {
        const bf16x8 f = __builtin_bit_cast(bf16x8, a[B][k]);
        qa = mfma16(f, f, qa); sa = mfma16(__builtin_bit_cast(bf16x8, one), f, sa);
      }
      __builtin_amdgcn_sched_barrier(0);
    }
    if constexpr (i.value + NB - 1 < NTILE) {
      constexpr std::integral_constant<int, i.value + NB - 1> j{};
      m_issue<KW, MODE>(a[j.value % NB], wr, l, so(j), vo(j));
    }
    const unsigned ga = guard(i);
    const unsigned gv = *(volatile LAS unsigned*)(uintptr_t)ga;
    __builtin_amdgcn_sched_barrier(0);
    f32x4 acc[NT];
#pragma unroll
    for (int j = 0; j < NT; ++j) acc[j] = mfma16(W[j], __builtin_bit_cast(bf16x8, a[B][0]), z4);
    if constexpr (MODE == 2) {
      const float q01 = (li & 1) ? qa[1] : qa[0], q23 = (li & 1) ? qa[3] : qa[2];
      *(LAS f32x2*)(uintptr_t)(sx + (i.value & 3) * sm) = f32x2{sa[0], (li & 2) ? q23 : q01};
      lds_inc(lb + C_S + n * 4);
      __builtin_amdgcn_sched_barrier(0);
    }
    DT(STI, 3 + 4 * i.value);
#pragma unroll
    for (int k = 1; k < KW; ++k)
#pragma unroll
      for (int j = 0; j < NT; ++j) acc[j] = mfma16(W[k * NT + j], __builtin_bit_cast(bf16x8, a[B][k]), acc[j]);
    __builtin_amdgcn_sched_barrier(0);
    if (__builtin_amdgcn_readfirstlane(gv) < 4u) lds_wait(ga, 4);
    DT(STI, 4 + 4 * i.value);
    const unsigned sl = slot(i);
#pragma unroll
    for (int j = 0; j < NT; ++j) *(LAS f32x4*)(uintptr_t)(pw + sl + j * 64) = acc[j];
    lds_inc(lb + C_P + n * 4);
    DT(STI, 5 + 4 * i.value);
    hook(i);
  });
  ST(STI + 1);
}

DEV unsigned lds_cnt(unsigned a) { const unsigned v = *(volatile LAS unsigned*)(uintptr_t)a; cfence(); return v; }
template <int NP, int RS>
DEV void e_sum(f32x4& lo, f32x4& hi, unsigned lb, unsigned a, int n) {
  f32x4 p[NP][2];
#pragma unroll 1
  for (int it = 0;; ++it) {
    const unsigned c = lds_cnt(lb + C_P + n * 4);
#pragma unroll
    for (int q = 0; q < NP; ++q) { p[q][0] = lds_ld4(a + q * (16 * RS)); p[q][1] = lds_ld4(a + q * (16 * RS) + 16); }
    if (__builtin_amdgcn_readfirstlane(c) >= (unsigned)NP || it) break;
    lds_wait(lb + C_P + n * 4, NP);
  }
  lds_inc(lb + C_F + n * 4);
#pragma unroll
  for (int s = 1; s < NP; s *= 2)
#pragma unroll
    for (int q = 0; q < NP; q += 2 * s) { p[q][0] += p[q + s][0]; p[q][1] += p[q + s][1]; }
  lo = p[0][0]; hi = p[0][1];
}
struct StatRd { unsigned c; f32x4 a, b; };
DEV StatRd e_stat0(unsigned lb, int n, int li) {
  StatRd r;
  r.c = lds_cnt(lb + C_S + n * 4);
  r.a = lds_ld4(lb + L_SX + (n & 3) * 512 + li * 32); r.b = lds_ld4(lb + L_SX + (n & 3) * 512 + li * 32 + 16);
  cfence();
  return r;
}
DEV void e_stat1(StatRd r, float& sum, float& sq, unsigned lb, int n, int li) {
  if (__builtin_amdgcn_readfirstlane(r.c) < 4u) { lds_wait(lb + C_S + n * 4, 4); r = e_stat0(lb, n, li); }
  const f32x4 t = r.a + r.b;
  sum = t[0] + t[2]; sq = t[1] + t[3];
}

template <class TH>
DEV void attention(char* smem, int xcd, int w, u32x2& om0, u32x2& ot0, u32x2& om1, u32x2& ot1, TH&& hook) {
  const int l = threadIdx.x & 63, g = l >> 4, li = l & 15;
  bf16x8 qf[2];
#pragma unroll
  for (int i = 0; i < 2; ++i) {
    u32x4 v = *(const u32x4*)(smem + AT_Q + (2 * w + i) * 768 + (g < 3 ? g : 2) * 256 + li * 16);
    if (g == 3) v = u32x4{0u, 0u, 0u, 0u};
    qf[i] = __builtin_bit_cast(bf16x8, v);
  }
  const char* Ks = smem + AT_K + 2 * w * 768 + (g < 3 ? g : 2) * 256 + li * 16;
  const int kbase = xcd * 256 + 32 * w - 256;
  f32x4 s0[17], s1[17];
  const f32x4 z4 = {0.f, 0.f, 0.f, 0.f};
#pragma unroll
  for (int c = 0; c < 18; ++c) {
    bf16x8 kf = *(const bf16x8*)(Ks + c * 768);
    if (c < 17) s0[c] = mfma16(kf, qf[0], z4);
    if (c > 0) s1[c - 1] = mfma16(kf, qf[1], z4);
  }
  hook(std::integral_constant<int, 0>{});
  const float NEG = -__builtin_inff();
#pragma unroll
  for (int r = 0; r < 4; ++r) {
    const int kj = 4 * g + r;
    if (kj <= li) { s0[0][r] = NEG; s1[0][r] = NEG; }
    if (kj > li) { s0[16][r] = NEG; s1[16][r] = NEG; }
  }
  if (__builtin_expect(kbase < 0, 0)) {
#pragma unroll
    for (int c = 0; c < 17; ++c) {
      if (kbase + 16 * c < 0) s0[c] = f32x4{NEG, NEG, NEG, NEG};
      if (kbase + 16 * (c + 1) < 0) s1[c] = f32x4{NEG, NEG, NEG, NEG};
    }
  }
  float m0 = NEG, m1 = NEG;
#pragma unroll
  for (int c = 0; c < 17; ++c)
#pragma unroll
    for (int r = 0; r < 4; r += 2) {
      m0 = fmaxf(fmaxf(m0, s0[c][r]), s0[c][r + 1]);
      m1 = fmaxf(fmaxf(m1, s1[c][r]), s1[c][r + 1]);
    }
  m0 = max_g(m0); m1 = max_g(m1);
  const float C2 = 0.22360679774997896f * 1.4426950408889634f;
  float b0 = -m0 * C2, b1 = -m1 * C2;
  u32x2 p0[18], p1[18];
  static_for<0, 17>([&](auto ci) {
    constexpr int c = ci.value;
    if constexpr (c % 3 == 0 && c < 15) {
      if constexpr (c > 0) asm volatile("" : "+v"(p0[c - 3]), "+v"(p0[c - 2]), "+v"(p0[c - 1]), "+v"(p1[c - 2]), "+v"(p1[c - 1]), "+v"(p1[c]));
      hook(std::integral_constant<int, 1 + c / 3>{});
      asm volatile("" : "+v"(b0), "+v"(b1));
    }
    const f32x4 e = s0[c] * C2 + b0, f = s1[c] * C2 + b1;
    p0[c] = u32x2{pk2(__builtin_amdgcn_exp2f(e[0]), __builtin_amdgcn_exp2f(e[1])), pk2(__builtin_amdgcn_exp2f(e[2]), __builtin_amdgcn_exp2f(e[3]))};
    p1[c + 1] = u32x2{pk2(__builtin_amdgcn_exp2f(f[0]), __builtin_amdgcn_exp2f(f[1])), pk2(__builtin_amdgcn_exp2f(f[2]), __builtin_amdgcn_exp2f(f[3]))};
  });
  p0[17] = u32x2{0u, 0u}; p1[0] = u32x2{0u, 0u};
  hook(std::integral_constant<int, 6>{});
  const char* Vs0 = smem + AT_V + w * 1280 + (g * 16 + li) * 16;
  const char* Vs1 = smem + AT_V + w * 1280 + 1024 + (g * 4 + (li & 3)) * 16;
  f32x4 o00 = z4, o01 = z4, o10 = z4, o11 = z4;
#pragma unroll
  for (int s = 0; s < 9; ++s) {
    bf16x8 v0 = *(const bf16x8*)(Vs0 + s * 1280);
    u32x4 v1u = *(const u32x4*)(Vs1 + s * 1280);
    if (li == 4) v1u = u32x4{0x3F803F80u, 0x3F803F80u, 0x3F803F80u, 0x3F803F80u};
    bf16x8 v1 = __builtin_bit_cast(bf16x8, v1u);
    u32x4 pa = {p0[2 * s][0], p0[2 * s][1], p0[2 * s + 1][0], p0[2 * s + 1][1]};
    u32x4 pb = {p1[2 * s][0], p1[2 * s][1], p1[2 * s + 1][0], p1[2 * s + 1][1]};
    bf16x8 pfa = __builtin_bit_cast(bf16x8, pa), pfb = __builtin_bit_cast(bf16x8, pb);
    o00 = mfma16(v0, pfa, o00); o01 = mfma16(v1, pfa, o01);
    o10 = mfma16(v0, pfb, o10); o11 = mfma16(v1, pfb, o11);
  }
  hook(std::integral_constant<int, 7>{});
  const float r0 = 1.0f / __shfl(o01[0], 16 + li), r1 = 1.0f / __shfl(o11[0], 16 + li);
  om0 = u32x2{pk2(o00[0] * r0, o00[1] * r0), pk2(o00[2] * r0, o00[3] * r0)};
  ot0 = u32x2{pk2(o01[0] * r0, o01[1] * r0), pk2(o01[2] * r0, o01[3] * r0)};
  om1 = u32x2{pk2(o10[0] * r1, o10[1] * r1), pk2(o10[2] * r1, o10[3] * r1)};
  ot1 = u32x2{pk2(o11[0] * r1, o11[1] * r1), pk2(o11[2] * r1, o11[3] * r1)};
}

#define CTX \
  int tid_ = threadIdx.x, b_ = blockIdx.x; \
  asm volatile("" : "+v"(tid_)); asm volatile("" : "+s"(b_)); \
  const int xcd = b_ & 7, jj = b_ >> 3, l = tid_ & 63, g = l >> 4, li = l & 15; \
  const int w = __builtin_amdgcn_readfirstlane(tid_ >> 6) & 7, q = w & 3, grp = w >> 2; \
  const int ct = jj & 7, ct16 = jj & 15; \
  const unsigned vl = l * 16; \
  const int G4 = (xcd * 4 + (jj >> 3)) * 4, dir = (jj >> 3) & 1; \
  const int B8 = (xcd * 2 + (jj >> 4)) * 8; \
  auto g4 = [&](int i) { return G4 + (dir ? 3 - i : i); }; \
  auto g8 = [&](int i) { return B8 + ((i & 1) ? 7 - (i >> 1) : (i >> 1)); }; \
  auto lane = [&](int) { return vl; }; \
  auto ring = [&](int n) { return lb + (n >= 2 ? C_F + (n - 2) * 4 : C_OK); }; \
  auto slot2 = [&](int i) { return (unsigned)((i & 1) * SLOT); }; \
  const int wid = jj * 4 + q; \
  auto nohook = [](int) {}; \
  const int nu = q < 2 ? 3 : 2, un = (q < 2 ? 3 * q : 2 * q + 2) + (g < nu ? g : nu - 1); \
  const bool act = g < nu; \
  const int ko = ct * 10 + un; \
  const unsigned vft = (ko >> 2) * 1024 + (ko & 3) * 256 + li * 16, vst = act ? vft : OOB; \
  const unsigned pe = lb + li * 336 + un * 32; \
  const unsigned xo = li * (D_ * 2) + ko * 16; \
  const int k6 = ct16 * 12 + 3 * q + (g < 3 ? g : 2); \
  const unsigned v6 = g < 3 ? (unsigned)((k6 >> 2) * 1024 + (k6 & 3) * 256 + li * 16) : OOB; \
  const unsigned w4b = (unsigned)WS_W4 + ct * (5 * 48 * 1024) + w * 6144; \
  (void)0

__global__ __launch_bounds__(512) void kp_main(const char* x, char* ws, char* out) {
  extern __shared__ __attribute__((aligned(16))) char smem[];
  STDECL;
  asm volatile("mx_kp_begin:");
  STR(0); ST(1);
  const unsigned lb = lds_addr(smem);
  const rsrc_t wr = __builtin_amdgcn_make_buffer_rsrc(ws, 0, (int)WS_END, 0x00020000);
  bf16x8 W[30];
  u32x4 a[3][6];
  int ga, gb;
  {
    CTX;
    const int qo = (int)WS_Q + jj * (int)QK_HEAD + xcd * 16 * 768 + w * 1024;
    const int ko0 = (int)WS_K + jj * (int)QK_HEAD, vo0 = (int)WS_V + jj * (int)V_HEAD;
    const int ko1 = ko0 + (xcd * 16 - 16) * 768 + (w - 12) * 1024, vo1 = vo0 + (xcd * 8 - 8) * 1280 + (w - 36) * 1024;
    static_for<0, 7>([&](auto ii) {
      constexpr int i = ii.value;
      const int k = ko1 + i * 8192, v = vo1 + i * 8192, sk = k < ko0 ? ko0 : k, sv = v < vo0 ? vo0 : v;
      int src = i < 2 ? qo + i * 8192 : i < 5 ? sk : sv;
      if constexpr (i == 1) src = grp ? sk : src;
      if constexpr (i == 4) src = grp ? sv : src;
      asm volatile("" : "+s"(src));
      glds16(ws + (unsigned)src, vl, lb + AT_Q + w * 1024 + i * 8192);
      if constexpr (i == 0) { ST(18); }
      if constexpr (i == 3) { ST(19); }
    });
    ST(20);
    *(LAS unsigned*)(uintptr_t)(lb + L_SY + (tid_ & 127) * 4) = (tid_ & 127) == (C_OK - L_SY) / 4 ? 4u : 0u;
    __builtin_amdgcn_s_barrier();
    {
      const char* wb = ws + WS_WO + (unsigned)(ct * (5 * 20 * 1024));
#pragma unroll
      for (int i = 0; i < 13; ++i) {
        const int t = i < 10 ? w + 8 * (i / 5) : 16 + (w >> 1), j = i < 10 ? i % 5 : (w & 1) * 2 + i - 10;
        glds16(wb + (unsigned)((j * 20 + t) * 1024), vl, lb + L_W2 + (t * 5 + j) * 1024);
      }
    }
    ST(14);
    wait_vm<13>();
    __builtin_amdgcn_s_barrier();
    cfence();
    ST(2);
    DT(2, 2);
    auto feed = [&](auto hi) {
      constexpr int h = hi.value;
      __builtin_amdgcn_sched_barrier(0);
      DT(2, 3 + 2 * h);
      if constexpr (h >= 1 && h < 6) {
        if (w == h - 1) {
          const int tu = (h - 1) * 32 + jj;
          touch(tu < 40 ? x + (unsigned)(xcd * 327680 + tu * 8192) : ws + WS_W1 + (unsigned)((tu - 40) * 8192), l, lb);
        }
      }
      if constexpr (h == 4) { ST(15); }
      if constexpr (h == 6) { ST(16); }
      __builtin_amdgcn_sched_barrier(0);
    };
    u32x2 om0, ot0, om1, ot1;
    attention(smem, xcd, w, om0, ot0, om1, ot1, feed);
    {
      const auto s0 = __builtin_amdgcn_permlane16_swap(om0[0], om1[0], false, false);
      const auto s1 = __builtin_amdgcn_permlane16_swap(om0[1], om1[1], false, false);
      const auto t0 = __builtin_amdgcn_permlane16_swap(ot0[0], ot1[0], false, false);
      const auto t1 = __builtin_amdgcn_permlane16_swap(ot0[1], ot1[1], false, false);
      const unsigned Of = (unsigned)WS_O + (xcd * 16 + 2 * w) * 20480 + (jj >> 3) * 5120;
      st16(wr, (g & 1) * 20480 + (g >> 1) * 256 + li * 16, Of + (jj & 7) * 512, u32x4{s0[0], s1[0], s0[1], s1[1]});
      st8(wr, g < 2 ? g * 20480 + li * 8 : OOB, Of + 4096 + (jj & 7) * 128, u32x2{t0[0], t1[0]});
    }
    ST(3);
    DT(2, 19);
    if (w < 5) wait_vm<3>(); else wait_vm<2>();
    __builtin_amdgcn_s_barrier();
    cfence();
    ST(17);
    DT(2, 20);
    ga = grp; gb = grp;
    asm volatile("" : "+s"(ga), "+s"(gb));
  }
  if (ga == 0) {
    CTX;
    auto soo = [&](int i) { return (unsigned)WS_O + g4(i) * 20480 + q * 5120; };
    m_issue<5, 1>(a[0], wr, l, soo(0), vl); m_issue<5, 1>(a[1], wr, l, soo(1), vl);
#pragma unroll
    for (int f = 0; f < 25; ++f) W[f] = *(const LAS bf16x8*)(uintptr_t)(lb + L_W2 + (q * 25 + f) * 1024 + vl);
    lds_inc(lb + C_W2);
    m_phase<5, 5, 4, 1, 3, 2>(W, a, l, lb, wr, 0, q, soo, lane, ring, slot2, nohook STPASS(4));
    f32x4 cs[2];
#pragma unroll
    for (int i = 0; i < 2; ++i) cs[i] = __builtin_bit_cast(f32x4, ld16p(wr, ko * 32 + i * 16, (unsigned)WS_CS));
    {
      const unsigned wb = (unsigned)WS_W2 + ct * (5 * 20 * 1024) + q * 5120;
#pragma unroll
      for (int k = 0; k < 5; ++k) {
#pragma unroll
        for (int j = 0; j < 5; ++j) W[k * 5 + j] = __builtin_bit_cast(bf16x8, ld16p(wr, vl + k * 1024, wb + j * (20 * 1024)));
      }
      cfence();
    }
    const int sr = li - ((q & 2) ? ((q & 1) ? -2 : 2) : ((q & 1) ? -1 : 1));
    const unsigned vg = (unsigned)(((sr >> 4) + 1) * 20480 + g * 256 + (sr & 15) * 16);
    auto sog = [&](int i) { return (unsigned)WS_G + (g4(i) - 1) * 20480 + q * 5120; };
    auto vog = [&](int i) { return (unsigned)(g4(i) * 16 + sr) < (unsigned)S_ ? vg : OOB; };
    {
      StatRd sd = e_stat0(lb, 4, li);
      static_for<0, 4>([&](auto i) {
        constexpr int n = 4 + i.value;
        float sum, sq, mean, var, m1, s1;
        e_stat1(sd, sum, sq, lb, n, li);
        DT(6, 2 + 5 * i.value);
        ln_fast(sum, sq, mean, var, m1, s1);
        const float r1 = __builtin_amdgcn_rcpf(s1);
        const f32x2 mv = bfr2(f32x2{(mean - m1) * r1, var * r1 * r1});
        const float s2 = bfr(__builtin_amdgcn_sqrtf(bfr(mv[1] + 1e-5f)));
        float rr = r1 * __builtin_amdgcn_rcpf(s2);
        const float nm = -(m1 + mv[0] * s1) * rr;
        if (w == 0) *(LAS f32x2*)(uintptr_t)(lb + L_LN + (i * 16 + li) * 8) = f32x2{m1, r1};
        f32x4 c0 = cs[0] * nm, c1 = cs[1] * nm;
        asm volatile("" : "+v"(rr), "+v"(c0), "+v"(c1));
        DT(6, 3 + 5 * i.value);
        f32x4 lo, hi;
        e_sum<4, 336>(lo, hi, lb, pe + (n & 1) * SLOT, n);
        if constexpr (i.value < 3) sd = e_stat0(lb, n + 1, li);
        if (i == 0) { ST(6); }
        DT(6, 4 + 5 * i.value);
        lo = lo * rr + c0; hi = hi * rr + c1;
        st16(wr, vst, (unsigned)WS_G + g4(i) * 20480,
             u32x4{pk2v(gelu2(bfr2(lo2(lo)))), pk2v(gelu2(bfr2(hi2(lo)))), pk2v(gelu2(bfr2(lo2(hi)))), pk2v(gelu2(bfr2(hi2(hi))))});
        DT(6, 5 + 5 * i.value);
        if constexpr (i.value < 2) { const int u = i.value * 128 + wid; touch(ws + WS_W4 + (unsigned)((u < 240 ? u : 239) * 8192), l, lb); }
        if constexpr (i.value == 2) m_issue<5, 0>(a[0], wr, l, sog(0), vog(0));
        DT(6, 6 + 5 * i.value);
      });
    }
    ST(7);
#pragma unroll
    for (int i = 0; i < 2; ++i) cs[i] = __builtin_bit_cast(f32x4, ld16p(wr, k6 * 32 + i * 16, (unsigned)WS_CS + D_ * 4));
    m_phase<5, 5, 4, 0, 3, 1>(W, a, l, lb, wr, 8, q, sog, vog, [&](int i) { return ring(8 + i); }, slot2, nohook STPASS(8));
    {
      const unsigned p6 = lb + li * 400 + (3 * q + (g < 3 ? g : 2)) * 32;
      StatRd sd = e_stat0(lb, 12, li);
      static_for<0, 8>([&](auto i) {
        constexpr int n = 12 + i.value;
        if constexpr (i.value < 6) {
#pragma unroll
          for (int j = 0; j < 5; ++j) W[i.value * 5 + j] = __builtin_bit_cast(bf16x8, ld16p(wr, vl + i.value * 1024, w4b + j * (48 * 1024)));
          cfence();
        }
        if constexpr (i.value == 7) m_issue<6, 0>(a[0], wr, l, (unsigned)WS_U + g4(0) * 49152 + w * 6144, vl);
        float sum, sq, mean, var, m, s;
        e_stat1(sd, sum, sq, lb, n, li);
        DT(10, 2 + 5 * i.value);
        ln_fast(sum, sq, mean, var, m, s);
        float rinv = __builtin_amdgcn_rcpf(s);
        const float nm = -m * rinv;
        f32x4 c0 = cs[0] * nm, c1 = cs[1] * nm;
        asm volatile("" : "+v"(rinv), "+v"(c0), "+v"(c1));
        DT(10, 3 + 5 * i.value);
        f32x4 lo, hi;
        e_sum<4, 400>(lo, hi, lb, p6 + (n & 1) * SLOT, n);
        if constexpr (i.value < 7) sd = e_stat0(lb, n + 1, li);
        if (i == 0) { ST(10); }
        DT(10, 4 + 5 * i.value);
        lo = lo * rinv + c0; hi = hi * rinv + c1;
        st16(wr, v6, (unsigned)WS_U + g8(i) * 49152,
             u32x4{pk2v(gelu2(bfr2(lo2(lo)))), pk2v(gelu2(bfr2(hi2(lo)))), pk2v(gelu2(bfr2(lo2(hi)))), pk2v(gelu2(bfr2(hi2(hi))))});
        DT(10, 5 + 5 * i.value);
      });
      ST(11);
    }
  }
  if (gb != 0) {
    CTX;
    const rsrc_t xr = __builtin_amdgcn_make_buffer_rsrc((void*)x, 0, S_ * D_ * 2, 0x00020000);
    auto soh = [&](int i) { return (unsigned)WS_H + g4(i) * 20480 + q * 5120; };
    auto sox = [&](int i) { return (unsigned)WS_X1 + g8(i) * 20480 + q * 5120; };
    {
      u32x4 xv[4];
#pragma unroll
      for (int i = 0; i < 4; ++i) xv[i] = ld16p(xr, xo, g4(i) * (16 * D_ * 2));
      cfence();
      const unsigned wb = (unsigned)WS_W1 + ct * (5 * 20 * 1024) + q * 5120;
#pragma unroll
      for (int k = 0; k < 5; ++k) {
#pragma unroll
        for (int j = 0; j < 5; ++j) W[k * 5 + j] = __builtin_bit_cast(bf16x8, ld16p(wr, vl + k * 1024, wb + j * (20 * 1024)));
      }
      if (wid < 80) touch(ws + WS_W2 + (unsigned)((20 + wid) * 8192), l, lb);
      cfence();
#pragma unroll
      for (int i = 0; i < 4; ++i) {
        f32x4 lo, hi;
        e_sum<4, 336>(lo, hi, lb, pe + (i & 1) * SLOT, i);
        if (i == 0) { ST(4); }
        st16(wr, vst, (unsigned)WS_H + g4(i) * 20480,
             u32x4{pk2v(unpk(xv[i][0]) + bfr2(lo2(lo))), pk2v(unpk(xv[i][1]) + bfr2(hi2(lo))),
                   pk2v(unpk(xv[i][2]) + bfr2(lo2(hi))), pk2v(unpk(xv[i][3]) + bfr2(hi2(hi)))});
        if (i < 2) { const int u = i * 128 + wid; touch(ws + WS_W3 + (unsigned)((u < 240 ? u : 239) * 8192), l, lb); }
        if (i == 2) m_issue<5, 2>(a[0], wr, l, soh(0), vl);
      }
      ST(5);
    }
    m_phase<5, 5, 4, 2, 3, 1>(W, a, l, lb, wr, 4, q, soh, lane, [&](int i) { return ring(4 + i); }, slot2, nohook STPASS(6));
    {
      u32x4 hv[4];
#pragma unroll
      for (int i = 0; i < 4; ++i) hv[i] = ld16(wr, vft, (unsigned)WS_H + g4(i) * 20480);
      cfence();
      const unsigned w3b = (unsigned)WS_W3 + ct16 * (6 * 20 * 1024) + q * 5120;
      auto load3 = [&](int f) { W[f] = __builtin_bit_cast(bf16x8, ld16p(wr, vl + (f / 6) * 1024, w3b + (f % 6) * (20 * 1024))); };
#pragma unroll
      for (int f = 0; f < 30; ++f) {
        load3(f);
      }
      lds_wait(lb + C_W2, 4);
      auto stage4 = [&](int f) { glds16(ws + (w4b + (f % 5) * (48 * 1024) + (f / 5) * 1024), vl, lb + L_W + (q * 26 + f) * 1024); };
#pragma unroll
      for (int f = 0; f < 8; ++f) stage4(f);
      cfence();
#pragma unroll
      for (int i = 0; i < 4; ++i) {
        const int n = 8 + i;
        f32x4 lo, hi;
        e_sum<4, 336>(lo, hi, lb, pe + (n & 1) * SLOT, n);
        if (i == 0) { ST(8); }
        const f32x2 mr = *(const LAS f32x2*)(uintptr_t)(lb + L_LN + (i * 16 + li) * 8);
        const float m1 = mr[0], r1 = mr[1];
        st16(wr, vst, (unsigned)WS_X1 + g4(i) * 20480,
             u32x4{pk2v(bfr2(bfr2(unpk(hv[i][0]) - m1) * r1) + bfr2(lo2(lo))), pk2v(bfr2(bfr2(unpk(hv[i][1]) - m1) * r1) + bfr2(hi2(lo))),
                   pk2v(bfr2(bfr2(unpk(hv[i][2]) - m1) * r1) + bfr2(lo2(hi))), pk2v(bfr2(bfr2(unpk(hv[i][3]) - m1) * r1) + bfr2(hi2(hi)))});
        if (i < 3) {
#pragma unroll
          for (int f = 0; f < 6; ++f) stage4(8 + 6 * i + f);
          cfence();
        }
        if (i == 2) { m_issue<5, 2>(a[0], wr, l, sox(0), vl); m_issue<5, 2>(a[1], wr, l, sox(1), vl); }
      }
      ST(9);
    }
    m_phase<6, 5, 8, 2, 3, 2>(W, a, l, lb, wr, 12, q, sox, lane, [&](int i) { return ring(12 + i); }, slot2, nohook STPASS(10));
    m_issue<6, 0>(a[0], wr, l, (unsigned)WS_U + g4(0) * 49152 + w * 6144, vl);
#pragma unroll
    for (int j = 1; j < 5; ++j) W[25 + j] = __builtin_bit_cast(bf16x8, ld16p(wr, vl + 5 * 1024, w4b + j * (48 * 1024)));
#pragma unroll
    for (int f = 0; f < 26; ++f) W[f] = *(const LAS bf16x8*)(uintptr_t)(lb + L_W + (q * 26 + f) * 1024 + vl);
    lds_inc(lb + C_WC);
  }
  {
    CTX;
    const rsrc_t orr = __builtin_amdgcn_make_buffer_rsrc(out, 0, S_ * D_ * 2, 0x00020000);
    u32x4 hv[2], xv[2];
#pragma unroll
    for (int i = 0; i < 2; ++i) {
      const unsigned to = g4(2 * i + grp) * 20480;
      hv[i] = ld16(wr, vft, (unsigned)WS_H + to); xv[i] = ld16(wr, vft, (unsigned)WS_X1 + to);
    }
    auto fin = [&](int p, auto si) {
      constexpr int s = si.value;
      f32x4 lo, hi;
      e_sum<8, 336>(lo, hi, lb, pe + (p % 3) * SLOT7, 20 + p);
      const u32x4 y = {pk2v(unpk(hv[s][0]) + bfr2(unpk(xv[s][0]) + bfr2(lo2(lo)))), pk2v(unpk(hv[s][1]) + bfr2(unpk(xv[s][1]) + bfr2(hi2(lo)))),
                       pk2v(unpk(hv[s][2]) + bfr2(unpk(xv[s][2]) + bfr2(lo2(hi)))), pk2v(unpk(hv[s][3]) + bfr2(unpk(xv[s][3]) + bfr2(hi2(hi))))};
      st16r<0>(orr, act ? xo : OOB, g4(p) * (16 * D_ * 2), y);
    };
    m_phase<5, 6, 4, 0, 2, 1>(W, a, l, lb, wr, 20, w, [&](int i) { return (unsigned)WS_U + g4(i) * 49152 + w * 6144; }, lane,
        [&](auto i) { return lb + (i.value == 0 ? C_F + 19 * 4 : i.value == 1 ? C_WC : i.value == 2 ? C_OK : C_F + 20 * 4); },
        [&](auto i) { return (unsigned)((i.value % 3) * SLOT7); },
        [&](auto i) {
          if constexpr (i.value == 1) { if (grp == 0) fin(0, std::integral_constant<int, 0>{}); }
          if constexpr (i.value == 2) { if (grp == 1) fin(1, std::integral_constant<int, 0>{}); }
          if constexpr (i.value == 3) fin(2 + grp, std::integral_constant<int, 1>{});
        } STPASS(12));
    STDUMP2(ws, 2 + grp);
  }
  asm volatile("mx_kp_end:");
}

extern "C" void kernel_launch(void* const* d_in, const int* in_sizes, int n_in, void* d_out, int out_size,
                              void* d_ws, size_t ws_size, hipStream_t stream) {
  const char* x = (const char*)d_in[0];
  const char* wqkv = (const char*)d_in[1];
  const char* wo = (const char*)d_in[2];
  const char* w1 = (const char*)d_in[4];
  const char* w2 = (const char*)d_in[5];
  const char* w3 = (const char*)d_in[6];
  const char* w4 = (const char*)d_in[7];
  char* ws = (char*)d_ws;
  if (ws_size < WS_END) return;
  constexpr int LQ = 4 * 32768 + 2048;
  (void)hipFuncSetAttribute((const void*)k1_qkv, hipFuncAttributeMaxDynamicSharedMemorySize, LQ);
  (void)hipFuncSetAttribute((const void*)kp_main, hipFuncAttributeMaxDynamicSharedMemorySize, LDS_TOT);
  k1_qkv<<<256, 384, LQ, stream>>>(x, wqkv, wo, w1, w2, w3, w4, ws);
  kp_main<<<256, 512, LDS_TOT, stream>>>(x, ws, (char*)d_out);
}
```

```cpp
#include <hip/hip_runtime.h>
#include <hip/hip_bf16.h>
#include <stdint.h>
#include <type_traits>

typedef __attribute__((ext_vector_type(8))) __bf16 bf16x8;
typedef __attribute__((ext_vector_type(4))) __bf16 bf16x4;
typedef __attribute__((ext_vector_type(2))) __bf16 bf16x2;
typedef __attribute__((ext_vector_type(4))) short s16x4;
typedef __attribute__((ext_vector_type(4))) float f32x4;
typedef __attribute__((ext_vector_type(2))) unsigned u32x2;
typedef __attribute__((ext_vector_type(4))) unsigned u32x4;
#define LAS __attribute__((address_space(3)))
#define DEV __device__ __forceinline__

constexpr int S_ = 2048, D_ = 640, F_ = 1536, H_ = 32;

constexpr size_t QK_HEAD = 128 * 640;
constexpr size_t V_HEAD  = 64 * 1280;
constexpr size_t WS_Q  = 0;
constexpr size_t WS_K  = WS_Q + H_ * QK_HEAD;
constexpr size_t WS_V  = WS_K + H_ * QK_HEAD;
constexpr size_t WS_O  = WS_V + H_ * V_HEAD;
constexpr size_t WS_H  = WS_O + (size_t)S_ * D_ * 2;
constexpr size_t WS_G  = WS_H + (size_t)S_ * D_ * 2;
constexpr size_t WS_X1 = WS_G + (size_t)S_ * D_ * 2;
constexpr size_t WS_U  = WS_X1 + (size_t)S_ * D_ * 2;
constexpr size_t WS_WO = WS_U + (size_t)S_ * F_ * 2;
constexpr size_t WS_W1 = WS_WO + (size_t)D_ * D_ * 2;
constexpr size_t WS_W2 = WS_W1 + (size_t)D_ * D_ * 2;
constexpr size_t WS_W3 = WS_W2 + (size_t)D_ * D_ * 2;
constexpr size_t WS_W4 = WS_W3 + (size_t)D_ * F_ * 2;
constexpr size_t WS_END = WS_W4 + (size_t)D_ * F_ * 2;
static_assert(WS_WO - WS_O == 4096 * 4096, "fill");
constexpr unsigned EMPTY = 0xffffffffu;

template <int I, int N, class Fn> DEV void static_for(Fn&& f) {
  if constexpr (I < N) { f(std::integral_constant<int, I>{}); static_for<I + 1, N>(f); }
}
template <int N> DEV void wait_vm() { asm volatile("s_waitcnt vmcnt(%0) lgkmcnt(0)" ::"n"(N) : "memory"); }
DEV void glds16(const char* sbase, unsigned voff, unsigned lds) {
  asm volatile("s_mov_b32 m0, %0\n\ts_nop 0\n\tglobal_load_lds_dwordx4 %1, %2" ::"s"(lds), "v"(voff), "s"(sbase) : "memory");
}
DEV unsigned lds_addr(const void* p) { return (unsigned)(uintptr_t)(LAS const void*)p; }
#define ST(i)
#define DT(ph, i)
#define STR(i)
#define STDECL
#define STDUMP(o, ws, kid)
#define STARG
#define STSIDE(ws, kid, t0)
#define STPASS(i)
#define STDUMP2(ws, kid)
DEV void wait_lgkm0() { asm volatile("s_waitcnt lgkmcnt(0)" ::: "memory"); }
DEV void cfence() { asm volatile("" ::: "memory"); }
DEV float bfr(float x) { return (float)(__bf16)x; }
DEV unsigned pk2(float a, float b) { bf16x2 v = {(__bf16)a, (__bf16)b}; return __builtin_bit_cast(unsigned, v); }
DEV float bf_lo(unsigned u) { return __builtin_bit_cast(float, u << 16); }
DEV float bf_hi(unsigned u) { return __builtin_bit_cast(float, u & 0xffff0000u); }
DEV f32x4 mfma16(bf16x8 a, bf16x8 b, f32x4 c) { return __builtin_amdgcn_mfma_f32_16x16x32_bf16(a, b, c, 0, 0, 0); }
typedef __amdgpu_buffer_rsrc_t rsrc_t;
constexpr unsigned OOB = 0x40000000u;
template <int AUX> DEV void st16r(rsrc_t r, unsigned v, unsigned s, u32x4 d) {
  asm("" : "+v"(d));
  __builtin_amdgcn_raw_buffer_store_b128(d, r, v, s, AUX);
  asm volatile("s_nop 1" ::"v"(d) : "memory");
}
DEV bf16x8 ones8() { return __builtin_bit_cast(bf16x8, u32x4{0x3F803F80u, 0x3F803F80u, 0x3F803F80u, 0x3F803F80u}); }

DEV void ln_stats(float sum, float sq, float& mean, float& var, float& m, float& s) {
  mean = sum * (1.0f / D_);
  var = fmaxf(sq * (1.0f / D_) - mean * mean, 0.f);
  m = bfr(mean);
  s = bfr(sqrtf(bfr(bfr(var) + 1e-5f)));
}

DEV unsigned f2u(float v) { return __builtin_bit_cast(unsigned, v); }
DEV float u2f(unsigned v) { return __builtin_bit_cast(float, v); }
DEV float max_g(float v) {
  const auto r = __builtin_amdgcn_permlane16_swap(f2u(v), f2u(v), false, false);
  v = fmaxf(u2f(r[0]), u2f(r[1]));
  const auto s = __builtin_amdgcn_permlane32_swap(f2u(v), f2u(v), false, false);
  return fmaxf(u2f(s[0]), u2f(s[1]));
}

DEV unsigned wrm_voff(int l, int q, int ldw) {
  return (unsigned)((l >> 4) * ldw + ((((l & 15) >> 1) ^ ((l >> 4) | (q << 2))) * 32) + (l & 1) * 16);
}
DEV bf16x8 wrm_frag(const char* p) {
  s16x4 lo = __builtin_amdgcn_ds_read_tr16_b64_v4i16((LAS s16x4*)(p));
  s16x4 hi = __builtin_amdgcn_ds_read_tr16_b64_v4i16((LAS s16x4*)(p + 1024));
  typedef __attribute__((ext_vector_type(8))) short s16x8;
  s16x8 v = __builtin_shufflevector(lo, hi, 0, 1, 2, 3, 4, 5, 6, 7);
  return __builtin_bit_cast(bf16x8, v);
}
DEV unsigned wrm_lane(int l, int jhi, int jj) {
  const int g = l >> 4, i = l & 15;
  return (unsigned)((8 * g + (i >> 2)) * 256 + ((((jhi ^ (g & 1)) << 2) | (jj ^ (i >> 2))) * 32) + (i & 3) * 8);
}

struct WJob { const char* src; const char* code; char* ws; unsigned dst, coff, c1off, fill; int ldw, wks, r4, perm; };
DEV WJob make_job(int id, const char* wo, const char* w1, const char* w2, const char* w3, const char* w4) {
  const bool live = id < 390;
  const int b = live ? id : 389;
  const char* wsrc; unsigned wdst; int ldw = D_ * 2, wks = 20, kt, nt;
  if (b < 150) {
    const int mi = b / 50, u = b - mi * 50;
    wsrc = mi == 0 ? wo : (mi == 1 ? w1 : w2); wdst = (unsigned)WS_WO + mi * (D_ * D_ * 2);
    kt = u / 5; nt = u - kt * 5;
  } else if (b < 270) {
    const int u = b - 150; wsrc = w3; wdst = (unsigned)WS_W3; ldw = F_ * 2; kt = u / 12; nt = u - kt * 12;
  } else {
    const int u = b - 270; wsrc = w4; wdst = (unsigned)WS_W4; wks = 48; kt = u / 5; nt = u - kt * 5;
  }
  WJob j;
  j.src = wsrc + nt * 256;
  j.r4 = kt * 16; j.perm = b < 50;
  j.dst = live ? wdst + (unsigned)(nt * 8 * wks + kt * 2) * 1024 : OOB;
  j.ldw = ldw; j.wks = wks;
  return j;
}
DEV void touch4(const char* p, unsigned voff, unsigned lds) {
  asm volatile("s_mov_b32 m0, %0\n\ts_nop 0\n\tglobal_load_lds_dword %1, %2" ::"s"(lds), "v"(voff), "s"(p) : "memory");
}

template <bool SWAP>
struct GemmQ {
  f32x4 acc[4][4];
  STDECL;
  static constexpr int NG = 4, D = 3, NGRP = 10, SB = 32768, JOB = NG * SB, XCH = JOB;
  DEV bool run(char* smem, const char* Ab, const char* Wb, int k0, const WJob& job, rsrc_t wr) {
    STR(0); ST(1);
    constexpr int LDA = D_ * 2, LDW = 3 * D_ * 2;
    const int tid = threadIdx.x, l = tid & 63;
    const int wave = __builtin_amdgcn_readfirstlane(tid >> 6);
    if (wave >= 4) {
      const bool isw = wave == 5;
      unsigned vo[2];
#pragma unroll
      for (int q = 0; q < 2; ++q)
        vo[q] = isw ? wrm_voff(l, q, LDW) : (unsigned)((l >> 3) * LDA + (((l & 7) ^ (q * 4 + (l >> 4))) * 16));
      const unsigned vx[4] = {vo[0], isw ? vo[0] : vo[1], isw ? vo[1] : vo[0], vo[1]};
      const unsigned dst0 = lds_addr(smem) + (isw ? 16384 : 0);
      const char* src0 = isw ? Wb : Ab;
      const int gstr = isw ? 64 * LDW : 128, istr = isw ? 4 * LDW : 8 * LDA;
      auto issue = [&](int grp) {
        int kg = grp + k0;
        kg = kg >= NGRP ? kg - NGRP : kg;
        const char* sp = src0 + (size_t)kg * gstr;
#pragma unroll
        for (int i = 0; i < 16; ++i) glds16(sp + (size_t)i * istr, vx[i & 3], dst0 + (grp % NG) * SB + i * 1024);
      };
      static_for<0, D>([&](auto gi) { issue(gi.value); });
      touch4(job.code, job.c1off, dst0 + JOB + 2048);
      static_for<0, NGRP>([&](auto gi) {
        constexpr int grp = gi.value;
        constexpr int later[NGRP] = {33, 33, 49, 48, 48, 32, 48, 49, 33, 1};
        wait_vm<later[grp]>();
        __builtin_amdgcn_s_barrier();
        cfence();
        if constexpr (grp + D < NGRP) issue(grp + D);
        if constexpr (grp == 1) {
#pragma unroll
          for (int i = 0; i < 16; ++i) {
            int r4 = job.r4 + i;
            const int kq = r4 / 40, rr = r4 - kq * 40;
            if (job.perm) r4 = rr < 32 ? (8 * kq + (rr >> 2)) * 5 + (rr & 3) : (8 * kq + rr - 32) * 5 + 4;
            glds16(job.src + (size_t)r4 * 4 * job.ldw, wrm_voff(l, (i >> 1) & 1, job.ldw), dst0 + JOB + i * 1024);
          }
        }
        if constexpr (grp == 5) {
          const char* tb = smem + JOB + (isw ? 16384 : 0);
#pragma unroll
          for (int kk = 0; kk < 2; ++kk)
#pragma unroll
            for (int j = 0; j < 8; ++j) {
              const bf16x8 f = wrm_frag(tb + kk * 8192 + wrm_lane(l, j >> 2, j & 3));
              st16r<16>(wr, job.dst + l * 16, (unsigned)(j * job.wks + kk) * 1024, __builtin_bit_cast(u32x4, f));
            }
        }
        if constexpr (grp == 6) touch4(job.code, job.coff, dst0 + JOB + 2048);
      });
      __builtin_amdgcn_s_barrier();
      STSIDE(job.ws, 4, st_[0]);
      return false;
    }
    const int wm = wave >> 1, wn = wave & 1, g = l >> 4;
#pragma unroll
    for (int i = 0; i < 4; ++i)
#pragma unroll
      for (int j = 0; j < 4; ++j) acc[i][j] = f32x4{0.f, 0.f, 0.f, 0.f};
    const unsigned a0 = (unsigned)((wm * 64 + (l & 15)) * 128 + ((g ^ ((l & 15) >> 1)) * 16));
    const char* al[2][2] = {{smem + a0 + wn * 4096, smem + a0 + 4096 - wn * 4096},
                            {smem + (a0 ^ 64u) + wn * 4096, smem + (a0 ^ 64u) + 4096 - wn * 4096}};
    const char* wl[4];
#pragma unroll
    for (int j = 0; j < 4; ++j) wl[j] = smem + 16384 + wrm_lane(l, wn, j ^ (2 * wm));
    bf16x8 af[2][4], wf[2][4];
    f32x4 ms[2] = {}, mq[2] = {}, mc[2] = {};
    auto ldfrag = [&](auto ti) {
      constexpr int t = ti.value, kk = t & 1, so = ((t >> 1) % NG) * SB;
#pragma unroll
      for (int i = 0; i < 4; ++i) af[kk][i] = *(const bf16x8*)(al[kk][i >> 1] + so + (i & 1) * 2048);
#pragma unroll
      for (int j = 0; j < 4; ++j) wf[kk][j] = wrm_frag(wl[j] + so + kk * 8192);
    };
    __builtin_amdgcn_s_barrier();
    cfence();
    ST(3);
    ldfrag(std::integral_constant<int, 0>{});
    static_for<0, 2 * NGRP>([&](auto ti) {
      constexpr int t = ti.value, kk = t & 1;
      if constexpr (t + 1 < 2 * NGRP) {
        if constexpr (kk == 1) {
          wait_lgkm0();
          __builtin_amdgcn_s_barrier();
          cfence();
          ST(3 + (t + 1) / 2);
        }
        ldfrag(std::integral_constant<int, t + 1>{});
      }
      if constexpr (t >= 2 && t < 18 && kk == 0) __builtin_amdgcn_raw_buffer_store_b128(u32x4{EMPTY, EMPTY, EMPTY, EMPTY}, wr, tid * 16, job.fill + (t / 2 - 1) * 4096, 16);
      __builtin_amdgcn_sched_barrier(0);
#pragma unroll
      for (int i = 0; i < 4; ++i)
#pragma unroll
        for (int j = 0; j < 4; ++j)
          acc[i][j] = SWAP ? mfma16(wf[kk][j], af[kk][i], acc[i][j]) : mfma16(af[kk][i], wf[kk][j], acc[i][j]);
#pragma unroll
      for (int i = 0; i < 2; ++i) {
        ms[i] = mfma16(ones8(), af[kk][i], ms[i]); mq[i] = mfma16(af[kk][i], af[kk][i], mq[i]); mc[i] = mfma16(ones8(), wf[kk][i], mc[i]);
      }
      __builtin_amdgcn_sched_barrier(0);
    });
    ST(21);
    float2* xs = (float2*)(smem + XCH);
    float* xc = (float*)(smem + XCH + 1024);
    const int li = l & 15;
#pragma unroll
    for (int i = 0; i < 2; ++i) {
      const float q01 = (li & 1) ? mq[i][1] : mq[i][0], q23 = (li & 1) ? mq[i][3] : mq[i][2];
      float mean, var, m, sd;
      ln_stats(ms[i][0], (li & 2) ? q23 : q01, mean, var, m, sd);
      const float rinv = 1.0f / sd;
      if (g == (li >> 2)) { xs[(wm * 4 + wn * 2 + i) * 16 + li] = float2{rinv, -m * rinv}; xc[(wn * 4 + wm * 2 + i) * 16 + li] = mc[i][0]; }
    }
    wait_lgkm0();
    __builtin_amdgcn_s_barrier();
    cfence();
    return true;
  }
};

template <bool ISV>
DEV void qkv_body(char* smem, const char* x, const char* wqkv, char* ws, rsrc_t wr, int rt, int ct, int k0, const WJob& job) {
  using GQ = GemmQ<!ISV>;
  GQ gm;
  const int row0 = rt * 128, n0 = ct * 128;
  if (!gm.run(smem, x + (size_t)row0 * (D_ * 2), wqkv + n0 * 2, k0, job, wr)) return;
  const int tid = threadIdx.x, l = tid & 63, wave = __builtin_amdgcn_readfirstlane(tid >> 6), wm = wave >> 1, wn = wave & 1, g = l >> 4;
  const float2* xs = (const float2*)(smem + GQ::XCH);
  const float* xc = (const float*)(smem + GQ::XCH + 1024);
  if constexpr (!ISV) {
    f32x4 cs[4];
    unsigned po[4];
#pragma unroll
    for (int j = 0; j < 4; ++j) {
      const int ja = wn * 4 + (j ^ (2 * wm));
      cs[j] = *(const f32x4*)(xc + ja * 16 + 4 * g);
      const int qd = ct * 32 + ja * 4 + g, head = qd / 5;
      po[j] = (unsigned)(head * (int)QK_HEAD + (qd - head * 5) * 128 + (l & 15) * 8);
    }
#pragma unroll
    for (int i = 0; i < 4; ++i) {
      const int ia = wm * 4 + (i ^ (2 * wn));
      const float2 st = xs[ia * 16 + (l & 15)];
#pragma unroll
      for (int j = 0; j < 4; ++j) {
        const f32x4 y = gm.acc[i][j] * st.x + cs[j] * st.y;
        __builtin_amdgcn_raw_buffer_store_b64(u32x2{pk2(y[0], y[1]), pk2(y[2], y[3])}, wr, po[j], (unsigned)WS_Q + (rt * 8 + ia) * 640, 16);
      }
    }
  } else {
    float cd[4];
    unsigned po[4];
#pragma unroll
    for (int j = 0; j < 4; ++j) {
      const int ja = wn * 4 + (j ^ (2 * wm));
      cd[j] = xc[ja * 16 + (l & 15)];
      const int c = n0 - 2 * D_ + ja * 16 + (l & 15), head = c / 20, d = c - head * 20;
      po[j] = (unsigned)(head * (int)V_HEAD + (d < 16 ? (g * 16 + d) * 16 : 1024 + (g * 4 + d - 16) * 16));
    }
#pragma unroll
    for (int i2 = 0; i2 < 2; ++i2) {
      const int ia = wm * 4 + ((2 * i2) ^ (2 * wn));
      f32x4 rr[2], nm[2];
#pragma unroll
      for (int h = 0; h < 2; ++h) {
        const f32x4 s01 = *(const f32x4*)(xs + (ia + h) * 16 + 4 * g), s23 = *(const f32x4*)(xs + (ia + h) * 16 + 4 * g + 2);
        rr[h] = f32x4{s01[0], s01[2], s23[0], s23[2]}; nm[h] = f32x4{s01[1], s01[3], s23[1], s23[3]};
      }
#pragma unroll
      for (int j = 0; j < 4; ++j) {
        const f32x4 y = gm.acc[2 * i2][j] * rr[0] + nm[0] * cd[j], z = gm.acc[2 * i2 + 1][j] * rr[1] + nm[1] * cd[j];
        st16r<16>(wr, po[j], (unsigned)WS_V + (rt * 4 + (ia >> 1)) * 1280, u32x4{pk2(y[0], y[1]), pk2(y[2], y[3]), pk2(z[0], z[1]), pk2(z[2], z[3])});
      }
    }
  }
  STDUMP(gm, ws, 1);
}

__global__ __launch_bounds__(384) void k1_qkv(const char* x, const char* wqkv, const char* wo, const char* w1, const char* w2,
                                              const char* w3, const char* w4, char* ws) {
  extern __shared__ __attribute__((aligned(16))) char smem[];
  const int b = blockIdx.x, xcd = b & 7, j = b >> 3, r = j & 3, c = j >> 2;
  const char* pc;
  unsigned c0, c1;
  asm volatile("s_getpc_b64 %0\nmx_k1_pc:\n\ts_mov_b32 %1, mx_kp_begin-mx_k1_pc\n\ts_mov_b32 %2, mx_kp_end-mx_k1_pc" : "=s"(pc), "=s"(c0), "=s"(c1));
  const rsrc_t wr = __builtin_amdgcn_make_buffer_rsrc(ws, 0, (int)WS_END, 0x00020000);
  if ((xcd & 1) && c == 7) {
    if (threadIdx.x < 256) {
      const unsigned f0 = (unsigned)WS_O + (1920 + ((xcd >> 1) * 4 + r) * 136) * 4096;
#pragma unroll 8
      for (int i = 0; i < 136; ++i) __builtin_amdgcn_raw_buffer_store_b128(u32x4{EMPTY, EMPTY, EMPTY, EMPTY}, wr, threadIdx.x * 16, f0 + i * 4096, 16);
      STSIDE(ws, 5, t0_);
    }
    return;
  }
  const int rt = (xcd >> 1) * 4 + r, ct = (xcd & 1) * 8 + c;
  const int wave = __builtin_amdgcn_readfirstlane(threadIdx.x >> 6);
  WJob job = make_job((rt * 15 + ct) * 2 + (wave & 1), wo, w1, w2, w3, w4);
  {
    const unsigned u = (j & 15) * 2 + (wave & 1);
    const unsigned o = c0 + u * 2560 + ((threadIdx.x & 63) % 20) * 128;
    const unsigned o1 = u * 2048 + (threadIdx.x & 15) * 128;
    job.code = pc; job.coff = o < c1 ? o : c1; job.c1off = o1 < c0 ? o1 : c0;
    job.fill = (unsigned)WS_O + (rt * 15 + ct) * 32768; job.ws = ws;
  }
  const int k0 = (3 * r + 4 * c) % 10;
  if (ct >= 10) qkv_body<true>(smem, x, wqkv, ws, wr, rt, ct, k0, job);
  else qkv_body<false>(smem, x, wqkv, ws, wr, rt, ct, k0, job);
}

typedef __attribute__((ext_vector_type(2))) float f32x2;
typedef unsigned long long u64;
#define SB0 __builtin_amdgcn_sched_barrier(0)
DEV float opq(float v) { asm("" : "+v"(v)); return v; }
DEV unsigned pks(float a, float b) { unsigned u = pk2(opq(a), opq(b)); asm("" : "+v"(u)); return u; }
DEV void bfr2(float& a, float& b) { const unsigned u = pks(a, b); a = bf_lo(u); b = bf_hi(u); }
DEV float bfr1(float a) { return bf_lo(pks(a, a)); }
DEV void ln_fast(float sum, float sq, float& mean, float& var, float& m, float& s) {
  mean = sum * (1.0f / D_);
  var = fmaxf(sq * (1.0f / D_) - mean * mean, 0.f);
  float v = var;
  m = mean;
  bfr2(m, v);
  s = bfr1(__builtin_amdgcn_sqrtf(bfr1(v + 1e-5f)));
}
DEV float gelu(float x) {
  const float c0 = -2.0f * 0.7978845608028654f * 1.4426950408889634f, c1 = c0 * 0.044715f;
  const float z = x * (x * x * c1 + c0);
  return x * __builtin_amdgcn_rcpf(__builtin_amdgcn_exp2f(z) + 1.0f);
}

DEV unsigned full(unsigned v) { return v < EMPTY ? v : EMPTY - 1; }
DEV void st4(rsrc_t r, unsigned v, unsigned s, unsigned d) { __builtin_amdgcn_raw_buffer_store_b32(full(d), r, v, s, 16); }
DEV void st8(rsrc_t r, unsigned v, unsigned s, u32x2 d) { __builtin_amdgcn_raw_buffer_store_b64(u32x2{full(d[0]), full(d[1])}, r, v, s, 16); }
DEV void st16(rsrc_t r, unsigned v, unsigned s, u32x4 d) { st16r<16>(r, v, s, u32x4{full(d[0]), full(d[1]), full(d[2]), full(d[3])}); }
DEV u32x4 ld16(rsrc_t r, unsigned v, unsigned s) { return __builtin_amdgcn_raw_buffer_load_b128(r, v, s, 16); }
DEV u32x2 ld8(rsrc_t r, unsigned v, unsigned s) { return __builtin_amdgcn_raw_buffer_load_b64(r, v, s, 16); }
DEV unsigned ld4(rsrc_t r, unsigned v, unsigned s) { return __builtin_amdgcn_raw_buffer_load_b32(r, v, s, 16); }
DEV u32x4 ld16p(rsrc_t r, unsigned v, unsigned s) { return __builtin_amdgcn_raw_buffer_load_b128(r, v, s, 0); }
DEV u32x2 ld8p(rsrc_t r, unsigned v, unsigned s) { return __builtin_amdgcn_raw_buffer_load_b64(r, v, s, 0); }
DEV unsigned ld4p(rsrc_t r, unsigned v, unsigned s) { return __builtin_amdgcn_raw_buffer_load_b32(r, v, s, 0); }
DEV void vm0() { __builtin_amdgcn_s_waitcnt(0x0f70); cfence(); }

constexpr int LDS_TOT = 163840;
constexpr int AT_Q = 0, AT_K = AT_Q + 16 * 640, AT_V = AT_K + 32 * 640;
constexpr int SLOT = 24576, SLOT7 = 40960;
constexpr int L_W = 51200;
constexpr int L_CS = 157696;
constexpr int L_SX = 159744;
constexpr int L_SY = 161792;
constexpr int C_P = L_SY, C_S = L_SY + 128, C_F = L_SY + 256, C_W2 = L_SY + 384, C_WF = L_SY + 388, C_WC = L_SY + 392, C_OK = L_SY + 396, C_V = L_SY + 400;
constexpr int L_LN = L_SY + 512;
constexpr int L_DUM = L_SY + 1024;
DEV void touch(const char* p, int l, unsigned lb) { touch4(p, l * 128, lb + L_DUM + 512); }
DEV void lds_inc(unsigned a) {
  asm volatile("s_mov_b64 exec, 1\n\tds_add_u32 %0, %1\n\ts_mov_b64 exec, -1" ::"v"(a), "v"(1u) : "memory");
}
DEV void lds_wait(unsigned a, unsigned target) {
  for (int it = 0; it < (1 << 22); ++it) {
    const unsigned v = __builtin_amdgcn_readfirstlane(*(volatile LAS unsigned*)(uintptr_t)a);
    if (v >= target) break;
  }
  cfence();
}
template <class T> DEV T lds_ld(unsigned a) { return *(const volatile LAS T*)(uintptr_t)a; }
DEV unsigned max3u(unsigned a, unsigned b, unsigned c) { a = a > b ? a : b; return a > c ? a : c; }

template <int KW, int MODE>
DEV void m_issue(u32x4 (&a)[6], rsrc_t wr, int l, unsigned s, unsigned v) {
#pragma unroll
  for (int k = 0; k < (MODE == 1 ? KW - 1 : KW); ++k) a[k] = ld16(wr, v + k * 1024, s);
  if constexpr (MODE == 1) {
    const unsigned t = (l >> 4) * 256 + (l & 15) * 8 + 4096;
    const u32x2 t0 = ld8(wr, t, s), t1 = ld8(wr, t + 128, s);
    a[KW - 1] = u32x4{t0[0], t0[1], t1[0], t1[1]};
  }
  cfence();
}
template <int NT, int KW, int NTILE, int MODE, int NB, int PRE, class TS, class TV, class TG, class TL, class TH>
DEV void m_phase(const bf16x8 (&W)[30], u32x4 (&a)[3][6], int l, unsigned lb, rsrc_t wr, int n0, int part, TS&& so, TV&& vo, TG&& guard,
                 TL&& slot, TH&& hook STARG) {
  const int g = l >> 4, li = l & 15;
  const unsigned pw = lb + part * (NT * 1024) + l * 16;
  const bool dg = g == (li >> 2);
  const unsigned sx = dg ? lb + L_SX + li * 32 + part * 8 : lb + L_DUM + l * 8, sm = dg ? 512u : 0u;
  const f32x4 z4 = {0.f, 0.f, 0.f, 0.f};
  static_for<PRE, NB - 1>([&](auto i) { m_issue<KW, MODE>(a[i.value], wr, l, so(i), vo(i)); });
  if constexpr (MODE == 2) {
    f32x4 c[NT];
#pragma unroll
    for (int k = 0; k < KW; ++k)
#pragma unroll
      for (int j = 0; j < NT; ++j) c[j] = mfma16(W[k * NT + j], ones8(), k ? c[j] : z4);
    if (li == 0) {
#pragma unroll
      for (int j = 0; j < NT; ++j) *(LAS f32x4*)(uintptr_t)(lb + L_CS + part * 384 + j * 64 + g * 16) = c[j];
    }
    cfence();
    SB0;
  }
  static_for<0, NTILE>([&](auto i) {
    constexpr int B = i.value % NB;
    const int n = n0 + i;
    auto bad = [&]() {
      unsigned m = 0;
#pragma unroll
      for (int k = 0; k < KW; ++k) m = max3u(max3u(m, a[B][k][0], a[B][k][1]), a[B][k][2], a[B][k][3]);
      return __builtin_amdgcn_ballot_w64(m == EMPTY) != 0;
    };
    if (bad()) {
      int it = 0;
#pragma unroll 1
      do { m_issue<KW, MODE>(a[B], wr, l, so(i), vo(i)); vm0(); } while (bad() && ++it < (1 << 16));
    }
    cfence();
    if (i == 0) { ST(STI); }
    DT(STI, 2 + 4 * i.value);
    f32x4 sa = z4, qa = z4;
    if constexpr (MODE == 2) {
#pragma unroll
      for (int k = 0; k < KW; ++k) {
        const bf16x8 f = __builtin_bit_cast(bf16x8, a[B][k]);
        qa = mfma16(f, f, qa); sa = mfma16(ones8(), f, sa);
      }
      SB0;
    }
    if constexpr (i.value + NB - 1 < NTILE) {
      constexpr std::integral_constant<int, i.value + NB - 1> j{};
      m_issue<KW, MODE>(a[j.value % NB], wr, l, so(j), vo(j));
    }
    const unsigned ga = guard(i);
    const unsigned gv = *(volatile LAS unsigned*)(uintptr_t)ga;
    SB0;
    f32x4 acc[NT];
#pragma unroll
    for (int j = 0; j < NT; ++j) acc[j] = mfma16(W[j], __builtin_bit_cast(bf16x8, a[B][0]), z4);
    if constexpr (MODE == 2) {
      const float q01 = (li & 1) ? qa[1] : qa[0], q23 = (li & 1) ? qa[3] : qa[2];
      *(LAS f32x2*)(uintptr_t)(sx + (i.value & 3) * sm) = f32x2{sa[0], (li & 2) ? q23 : q01};
      lds_inc(lb + C_S + n * 4);
      SB0;
    }
    DT(STI, 3 + 4 * i.value);
#pragma unroll
    for (int k = 1; k < KW; ++k)
#pragma unroll
      for (int j = 0; j < NT; ++j) acc[j] = mfma16(W[k * NT + j], __builtin_bit_cast(bf16x8, a[B][k]), acc[j]);
    SB0;
    if (__builtin_expect(__builtin_amdgcn_readfirstlane(gv) < 4u, 0)) lds_wait(ga, 4);
    DT(STI, 4 + 4 * i.value);
    const unsigned sl = slot(i);
#pragma unroll
    for (int j = 0; j < NT; ++j) *(LAS f32x4*)(uintptr_t)(pw + sl + j * 1024) = acc[j];
    lds_inc(lb + C_P + n * 4);
    DT(STI, 5 + 4 * i.value);
    hook(i);
  });
  ST(STI + 1);
}

DEV unsigned lds_cnt(unsigned a) { return lds_ld<unsigned>(a); }
template <int NP> DEV float tree(float (&t)[NP]) {
#pragma unroll
  for (int s = 1; s < NP; s *= 2)
#pragma unroll
    for (int q = 0; q < NP; q += 2 * s) t[q] = opq(t[q] + t[q + s]);
  return t[0];
}
template <int NP, int NT>
DEV void e_sum(float (&y)[4], f32x2 (&x)[NP], unsigned lb, unsigned a, unsigned ax, int n) {
  f32x4 p[NP];
#pragma unroll 1
  for (int it = 0;; ++it) {
    const unsigned c = lds_cnt(lb + C_P + n * 4);
#pragma unroll
    for (int q = 0; q < NP; ++q) p[q] = lds_ld<f32x4>(a + q * (NT * 1024));
#pragma unroll
    for (int q = 0; q < NP; ++q) x[q] = lds_ld<f32x2>(ax + q * (NT * 1024));
    if (__builtin_amdgcn_readfirstlane(c) >= (unsigned)NP || it) break;
    lds_wait(lb + C_P + n * 4, NP);
  }
  lds_inc(lb + C_F + n * 4);
#pragma unroll
  for (int r = 0; r < 4; ++r) {
    float t[NP];
#pragma unroll
    for (int q = 0; q < NP; ++q) t[q] = p[q][r];
    y[r] = tree(t);
  }
}
template <int NP> DEV void e_sum2(float (&y)[2], const f32x2 (&x)[NP]) {
#pragma unroll
  for (int r = 0; r < 2; ++r) {
    float t[NP];
#pragma unroll
    for (int q = 0; q < NP; ++q) t[q] = x[q][r];
    y[r] = tree(t);
  }
}
DEV void e_cs(float (&c)[6], unsigned a, unsigned ax) {
  f32x4 p[4];
  f32x2 x[4];
#pragma unroll
  for (int q = 0; q < 4; ++q) { p[q] = lds_ld<f32x4>(a + q * 384); x[q] = lds_ld<f32x2>(ax + q * 384); }
#pragma unroll
  for (int r = 0; r < 6; ++r) {
    float t[4];
#pragma unroll
    for (int q = 0; q < 4; ++q) t[q] = r < 4 ? p[q][r & 3] : x[q][r & 1];
    c[r] = tree(t);
  }
}
struct StatRd { unsigned c; f32x4 a, b; };
DEV StatRd e_stat0(unsigned lb, int n, int li) {
  StatRd r;
  r.c = lds_cnt(lb + C_S + n * 4);
  r.a = lds_ld<f32x4>(lb + L_SX + (n & 3) * 512 + li * 32); r.b = lds_ld<f32x4>(lb + L_SX + (n & 3) * 512 + li * 32 + 16);
  return r;
}
DEV void e_stat1(StatRd r, float& sum, float& sq, unsigned lb, int n, int li) {
  if (__builtin_amdgcn_readfirstlane(r.c) < 4u) { lds_wait(lb + C_S + n * 4, 4); r = e_stat0(lb, n, li); }
  sum = opq(opq(r.a[0] + r.b[0]) + opq(r.a[2] + r.b[2])); sq = opq(opq(r.a[1] + r.b[1]) + opq(r.a[3] + r.b[3]));
}

template <class TH>
DEV void attention(unsigned lb, int xcd, int w, int l, u32x2& om0, u32x2& ot0, u32x2& om1, u32x2& ot1, TH&& hook) {
  const int g = l >> 4, li = l & 15;
  const unsigned fo = (g < 3 ? g : 2) * 128 + li * 8;
  auto frag = [&](unsigned a) {
    const u32x2 lo = *(const LAS u32x2*)(uintptr_t)a, hi = *(const LAS u32x2*)(uintptr_t)(a + 384);
    return u32x4{lo[0], lo[1], hi[0], hi[1]};
  };
  bf16x8 qf[2];
#pragma unroll
  for (int i = 0; i < 2; ++i) {
    u32x4 v = frag(lb + AT_Q + (2 * w + i) * 640 + fo);
    if (g >= 2) { v[2] = 0u; v[3] = 0u; }
    if (g == 3) { v[0] = 0u; v[1] = 0u; }
    qf[i] = __builtin_bit_cast(bf16x8, v);
  }
  const unsigned Ks = lb + AT_K + 2 * w * 640 + fo;
  const int nneg = xcd ? -1 : 16 - 2 * w;
  const float NEG = -__builtin_inff();
  const f32x4 n4 = {NEG, NEG, NEG, NEG}, z4 = {0.f, 0.f, 0.f, 0.f};
  f32x4 cf, cl, ci;
#pragma unroll
  for (int r = 0; r < 4; ++r) {
    cf[r] = (4 * g + r <= li || nneg > 0) ? NEG : 0.f;
    cl[r] = 4 * g + r > li ? NEG : 0.f;
    ci[r] = nneg > 0 ? NEG : 0.f;
  }
  auto edge = [&](int c) { if (__builtin_expect(c == nneg, 0)) { ci = z4; asm volatile("" : "+v"(ci)); } };
  f32x4 s0[17], s1[17];
  u32x4 kf[4];
  float m0 = NEG, m1 = NEG;
  auto mx = [&](float& m, const f32x4& s) { m = fmaxf(fmaxf(m, s[0]), s[1]); m = opq(fmaxf(fmaxf(m, s[2]), s[3])); };
#pragma unroll
  for (int c = 0; c < 3; ++c) kf[c] = frag(Ks + c * 640);
  SB0;
  static_for<0, 17>([&](auto cc) {
    constexpr int c = cc.value;
    constexpr int nx = c + 3 < 17 ? c + 3 : c + 3 - 16;
    if constexpr (c >= 2 && c < 16 && c % 2 == 0) edge(c);
    s0[c] = mfma16(__builtin_bit_cast(bf16x8, kf[c % 4]), qf[0], c == 0 ? cf : c == 16 ? cl : ci);
    kf[(c + 3) % 4] = frag(Ks + nx * 640);
    if constexpr (c >= 2) mx(m0, s0[c - 2]);
    SB0;
  });
  ci = nneg > 0 ? n4 : z4;
  hook(std::integral_constant<int, 0>{});
  auto qk1 = [&](auto cc) {
    constexpr int c = cc.value;
    if constexpr (c < 17) {
      if constexpr (c < 16 && c % 2 == 1) edge(c + 1);
      s1[c] = mfma16(__builtin_bit_cast(bf16x8, kf[(c + 17) % 4]), qf[1], c == 0 ? cf : c == 16 ? cl : ci);
      if constexpr (c + 3 < 17) kf[(c + 20) % 4] = frag(Ks + (c + 4) * 640);
    }
  };
  qk1(std::integral_constant<int, 0>{});
  mx(m0, s0[15]); mx(m0, s0[16]);
  m0 = max_g(m0);
  const float C2 = 0.22360679774997896f * 1.4426950408889634f;
  const float b0 = -m0 * C2;
  SB0;
  u32x2 p0[18], p1[18];
  auto fm4 = [&](f32x4& s, float b) {
#pragma unroll
    for (int r = 0; r < 4; ++r) s[r] = opq(s[r] * C2 + b);
  };
  auto ex4 = [&](f32x4& s) {
#pragma unroll
    for (int r = 0; r < 4; ++r) s[r] = __builtin_amdgcn_exp2f(s[r]);
  };
  auto cv = [&](const f32x4& s) { return u32x2{pks(s[0], s[1]), pks(s[2], s[3])}; };
  fm4(s0[0], b0);
  SB0;
  static_for<0, 17>([&](auto cc) {
    constexpr int c = cc.value;
    qk1(std::integral_constant<int, c + 1>{});
    ex4(s0[c]);
    SB0;
    if constexpr (c < 16) fm4(s0[c + 1], b0);
    if constexpr (c >= 1) mx(m1, s1[c - 1]);
    p0[c] = cv(s0[c]);
    SB0;
    if constexpr (c % 2 == 1) hook(std::integral_constant<int, 1 + c / 2>{});
  });
  p0[17] = u32x2{0u, 0u}; p1[0] = u32x2{0u, 0u};
  mx(m1, s1[16]);
  m1 = max_g(m1);
  const float b1 = -m1 * C2;
  hook(std::integral_constant<int, 9>{});
  const unsigned Vs0 = lb + AT_V + w * 1280 + l * 16, Vs1 = lb + AT_V + w * 1280 + 1024 + (g * 4 + (li & 3)) * 16;
  u32x4 v0[2], v1[2];
  auto ldv = [&](int s) { v0[s & 1] = *(const LAS u32x4*)(uintptr_t)(Vs0 + s * 1280); v1[s & 1] = *(const LAS u32x4*)(uintptr_t)(Vs1 + s * 1280); };
  f32x4 o00 = z4, o01 = z4, o10 = z4, o11 = z4;
  auto pv = [&](int s, int t) {
    const u32x2* p = t ? p1 : p0;
    const bf16x8 pf = __builtin_bit_cast(bf16x8, u32x4{p[2 * s][0], p[2 * s][1], p[2 * s + 1][0], p[2 * s + 1][1]});
    if (t == 0) { if (li == 4) v1[s & 1] = __builtin_bit_cast(u32x4, ones8()); }
    f32x4 &oa = t ? o10 : o00, &ob = t ? o11 : o01;
    oa = mfma16(__builtin_bit_cast(bf16x8, v0[s & 1]), pf, oa);
    ob = mfma16(__builtin_bit_cast(bf16x8, v1[s & 1]), pf, ob);
  };
  ldv(0);
  fm4(s1[0], b1);
  SB0;
  static_for<0, 17>([&](auto cc) {
    constexpr int c = cc.value;
    if constexpr (c % 2 == 0) pv(c / 2, 0); else { pv(c / 2, 1); ldv(c / 2 + 1); }
    ex4(s1[c]);
    SB0;
    if constexpr (c < 16) fm4(s1[c + 1], b1);
    p1[c + 1] = cv(s1[c]);
    SB0;
    if constexpr (c % 2 == 1) hook(std::integral_constant<int, 10 + c / 2>{});
  });
  pv(8, 1);
  const float r0 = 1.0f / __shfl(o01[0], 16 + li), r1 = 1.0f / __shfl(o11[0], 16 + li);
  om0 = u32x2{pk2(o00[0] * r0, o00[1] * r0), pk2(o00[2] * r0, o00[3] * r0)};
  ot0 = u32x2{pk2(o01[0] * r0, o01[1] * r0), pk2(o01[2] * r0, o01[3] * r0)};
  om1 = u32x2{pk2(o10[0] * r1, o10[1] * r1), pk2(o10[2] * r1, o10[3] * r1)};
  ot1 = u32x2{pk2(o11[0] * r1, o11[1] * r1), pk2(o11[2] * r1, o11[3] * r1)};
}

#define CTX \
  int tid_ = threadIdx.x, b_ = blockIdx.x; \
  asm volatile("" : "+v"(tid_)); asm volatile("" : "+s"(b_)); \
  const int xcd = b_ & 7, jj = b_ >> 3, l = tid_ & 63, g = l >> 4, li = l & 15; \
  const int w = __builtin_amdgcn_readfirstlane(tid_ >> 6) & 7, q = w & 3, grp = w >> 2; \
  const int ct = jj & 7, ct16 = jj & 15; \
  const unsigned vl = l * 16; \
  const int G4 = (xcd * 4 + (jj >> 3)) * 4, dir = (jj >> 3) & 1; \
  const int B8 = (xcd * 2 + (jj >> 4)) * 8; \
  auto g4 = [&](int i) { return G4 + (dir ? 3 - i : i); }; \
  auto g8 = [&](int i) { return B8 + ((i & 1) ? 7 - (i >> 1) : (i >> 1)); }; \
  auto lane = [&](int) { return vl; }; \
  auto ring = [&](int n) { return lb + (n >= 2 ? C_F + (n - 2) * 4 : C_OK); }; \
  auto slot2 = [&](int i) { return (unsigned)((i & 1) * SLOT); }; \
  const int wid = jj * 4 + q; \
  auto nohook = [](int) {}; \
  auto turn = [&](int i) { return (q >> 1) == (i & 1); }; \
  const unsigned v8 = (g >> 1) * 256 + li * 16 + (g & 1) * 8, v4 = li * 16 + g * 4; \
  const unsigned u5 = (ct * 10 + 2 * q) * 256, u5x = (ct * 10 + 8 + (q & 1)) * 256; \
  const unsigned pe = lb + q * 1024 + vl, px = lb + 4096 + (q & 1) * 512 + v8; \
  const unsigned ce = lb + L_CS + q * 64 + g * 16; \
  const unsigned xo = li * (D_ * 2) + g * 8, xox = li * (D_ * 2) + g * 4; \
  const unsigned xc = ct * 160 + q * 32, xcx = ct * 160 + 128 + (q & 1) * 16; \
  const unsigned w4b = (unsigned)WS_W4 + ct * (5 * 48 * 1024) + w * 6144; \
  (void)0

#define HW {3, 3, 3, 3, 3, 3, 3, 3, 1, 0, 0, 0, 0, 0, 0, 0, 0, 0}
#define H2 {0, 0, 0, 0, 0, 0, 0, 0, 0, 2, 1, 2, 1, 2, 1, 1, 1, 1}
#define HV 7
constexpr int hw_[18] = HW, h2_[18] = H2;
constexpr int upto(const int (&t)[18], int h) { int s = 0; for (int i = 0; i < h; ++i) s += t[i]; return s; }
static_assert(upto(hw_, 18) == 25 && upto(h2_, 18) == 12, "feed");

__global__ __launch_bounds__(512) void kp_main(const char* x, char* ws, char* out) {
  extern __shared__ __attribute__((aligned(16))) char smem[];
  STDECL;
  asm volatile("mx_kp_begin:");
  STR(0); ST(1);
  const unsigned lb = lds_addr(smem);
  const rsrc_t wr = __builtin_amdgcn_make_buffer_rsrc(ws, 0, (int)WS_END, 0x00020000);
  bf16x8 W[30];
  u32x4 a[3][6];
  u32x2 xv[4];
  unsigned xx[4];
  int ga, gb;
  {
    CTX;
    const int ko0 = (int)WS_K + jj * (int)QK_HEAD, vo0 = (int)WS_V + jj * (int)V_HEAD;
    const int qo = (int)WS_Q + jj * (int)QK_HEAD + xcd * 16 * 640 + w * 1024;
    const int ko1 = ko0 + (xcd * 16 - 16) * 640 + (w - 10) * 1024, vo1 = vo0 + (xcd * 8 - 8) * 1280 + (w - 30) * 1024;
    const int w2o = (int)WS_W2 + ct * 102400 + (w - 50) * 1024;
    auto piece = [&](auto ii) {
      constexpr int i = ii.value;
      const int k = ko1 + i * 8192, v = vo1 + i * 8192, sk = k < ko0 ? ko0 : k, sv = v < vo0 ? vo0 : v;
      int src = i < 2 ? qo + i * 8192 : i < 4 ? sk : i < 7 ? sv : w2o + i * 8192, dst = w * 1024;
      if constexpr (i == 1) src = w < 2 ? src : sk;
      if constexpr (i == 3) src = w < 6 ? src : sv;
      if constexpr (i == 6) src = w < 2 ? src : w2o + i * 8192;
      if constexpr (i == 18) { src = w < 6 ? src : src - 2048; dst = w < 6 ? dst : dst - 2048; }
      asm volatile("" : "+s"(src));
      glds16(ws + (unsigned)src, vl, lb + dst + i * 8192);
    };
    static_for<0, 7>(piece);
    ST(20);
    *(LAS unsigned*)(uintptr_t)(lb + L_SY + (tid_ & 127) * 4) = (tid_ & 127) == (C_OK - L_SY) / 4 ? 4u : 0u;
    wait_vm<3>();
    __builtin_amdgcn_s_barrier();
    cfence();
    ST(2);
    const unsigned wb = (grp ? (unsigned)WS_W1 : (unsigned)WS_WO) + ct * 102400 + q * 5120;
    const rsrc_t xr = __builtin_amdgcn_make_buffer_rsrc((void*)x, 0, S_ * D_ * 2, 0x00020000);
    auto feed = [&](auto hi) {
      constexpr int h = hi.value;
      SB0;
      if constexpr (h == 9) { ST(15); lds_wait(lb + C_V, 8); ST(16); }
      static_for<upto(hw_, h), upto(hw_, h + 1)>([&](auto fi) {
        constexpr int f = fi.value;
        W[f] = __builtin_bit_cast(bf16x8, ld16p(wr, vl + (f / 5) * 1024, wb + (f % 5) * 20480));
      });
      static_for<7 + upto(h2_, h), 7 + upto(h2_, h + 1)>(piece);
      if constexpr (h == HV) { asm volatile("s_waitcnt vmcnt(%0)" ::"n"(upto(hw_, h + 1) + upto(h2_, h + 1)) : "memory"); lds_inc(lb + C_V); }
      if constexpr (h == 17) {
#pragma unroll
        for (int i = 0; i < 4; ++i) { xv[i] = ld8p(xr, xo, xc + g4(i) * (16 * D_ * 2)); xx[i] = ld4p(xr, xox, xcx + g4(i) * (16 * D_ * 2)); }
      }
      if constexpr (h == 0) { ST(14); }
      cfence();
      SB0;
    };
    u32x2 om0, ot0, om1, ot1;
    attention(lb, xcd, w, l, om0, ot0, om1, ot1, feed);
    {
      const auto s0 = __builtin_amdgcn_permlane16_swap(om0[0], om1[0], false, false);
      const auto s1 = __builtin_amdgcn_permlane16_swap(om0[1], om1[1], false, false);
      const auto t0 = __builtin_amdgcn_permlane16_swap(ot0[0], ot1[0], false, false);
      const auto t1 = __builtin_amdgcn_permlane16_swap(ot0[1], ot1[1], false, false);
      const unsigned Of = (unsigned)WS_O + (xcd * 16 + 2 * w) * 20480 + (jj >> 3) * 5120;
      st16(wr, (g & 1) * 20480 + (g >> 1) * 256 + li * 16, Of + (jj & 7) * 512, u32x4{s0[0], s1[0], s0[1], s1[1]});
      st8(wr, g < 2 ? g * 20480 + li * 8 : OOB, Of + 4096 + (jj & 7) * 128, u32x2{t0[0], t1[0]});
    }
    ST(3);
    ga = grp; gb = grp;
    asm volatile("" : "+s"(ga), "+s"(gb));
  }
  if (ga == 0) {
    CTX;
    auto soo = [&](int i) { return (unsigned)WS_O + g4(i) * 20480 + q * 5120; };
    m_issue<5, 1>(a[0], wr, l, soo(0), vl); m_issue<5, 1>(a[1], wr, l, soo(1), vl);
    __builtin_amdgcn_s_barrier();
    cfence();
    ST(17);
    m_phase<5, 5, 4, 1, 3, 2>(W, a, l, lb, wr, 0, q, soo, lane, ring, slot2,
                              [&](int i) { if (i == 0) lds_inc(lb + C_W2); } STPASS(4));
    lds_wait(lb + C_W2, 8);
#pragma unroll
    for (int f = 0; f < 25; ++f) W[f] = *(const LAS bf16x8*)(uintptr_t)(lb + L_W + ((f % 5) * 20 + 5 * q + f / 5) * 1024 + vl);
    cfence();
    lds_inc(lb + C_WF);
    const int sr = li - ((q & 2) ? ((q & 1) ? -2 : 2) : ((q & 1) ? -1 : 1));
    const unsigned vg = (unsigned)(((sr >> 4) + 1) * 20480 + g * 256 + (sr & 15) * 16);
    auto sog = [&](int i) { return (unsigned)WS_G + (g4(i) - 1) * 20480 + q * 5120; };
    auto vog = [&](int i) { return (unsigned)(g4(i) * 16 + sr) < (unsigned)S_ ? vg : OOB; };
    float cs[6];
    {
      StatRd sd = e_stat0(lb, 4, li);
      static_for<0, 4>([&](auto i) {
        constexpr int n = 4 + i.value;
        float sum, sq, mean, var, m1, s1;
        e_stat1(sd, sum, sq, lb, n, li);
        if constexpr (i.value == 0) e_cs(cs, ce, lb + L_CS + 256 + (q & 1) * 32 + g * 8);
        DT(6, 2 + 5 * i.value);
        ln_fast(sum, sq, mean, var, m1, s1);
        const float r1 = __builtin_amdgcn_rcpf(s1);
        float m2 = (mean - m1) * r1, v2 = var * r1 * r1;
        bfr2(m2, v2);
        const float s2 = bfr1(__builtin_amdgcn_sqrtf(bfr1(v2 + 1e-5f)));
        float rr = r1 * __builtin_amdgcn_rcpf(s2);
        const float nm = -(m1 + m2 * s1) * rr;
        if (w == 0) *(LAS f32x2*)(uintptr_t)(lb + L_LN + (i * 16 + li) * 8) = f32x2{m1, r1};
        float c[6];
#pragma unroll
        for (int r = 0; r < 6; ++r) c[r] = cs[r] * nm;
        asm volatile("" : "+v"(rr), "+v"(c[0]), "+v"(c[1]), "+v"(c[2]), "+v"(c[3]), "+v"(c[4]), "+v"(c[5]));
        DT(6, 3 + 5 * i.value);
        float y[4], z[2];
        f32x2 e[4];
        e_sum<4, 5>(y, e, lb, pe + (n & 1) * SLOT, px + (n & 1) * SLOT, n);
        if constexpr (i.value < 3) sd = e_stat0(lb, n + 1, li);
        if (i == 0) { ST(6); }
        DT(6, 4 + 5 * i.value);
#pragma unroll
        for (int r = 0; r < 4; ++r) y[r] = y[r] * rr + c[r];
        bfr2(y[0], y[1]); bfr2(y[2], y[3]);
        const unsigned go = (unsigned)WS_G + g4(i) * 20480;
        st8(wr, v8, go + u5, u32x2{pks(gelu(y[0]), gelu(y[1])), pks(gelu(y[2]), gelu(y[3]))});
        if (turn(i)) {
          e_sum2(z, e);
          z[0] = z[0] * rr + c[4]; z[1] = z[1] * rr + c[5];
          bfr2(z[0], z[1]);
          st4(wr, v4, go + u5x, pks(gelu(z[0]), gelu(z[1])));
        }
        DT(6, 5 + 5 * i.value);
        if constexpr (i.value < 2) { const int u = i.value * 128 + wid; touch(ws + WS_W4 + (unsigned)((u < 240 ? u : 239) * 8192), l, lb); }
        if constexpr (i.value == 2) m_issue<5, 0>(a[0], wr, l, sog(0), vog(0));
        DT(6, 6 + 5 * i.value);
      });
    }
    ST(7);
    m_phase<5, 5, 4, 0, 3, 1>(W, a, l, lb, wr, 8, q, sog, vog, [&](int i) { return ring(8 + i); }, slot2, nohook STPASS(8));
    {
      const unsigned px6 = px + (q >> 1) * 1024, u6 = (ct16 * 12 + 2 * q) * 256, u6x = (ct16 * 12 + 8 + q) * 256;
      StatRd sd = e_stat0(lb, 12, li);
      static_for<0, 8>([&](auto i) {
        constexpr int n = 12 + i.value;
        if constexpr (i.value < 6) {
#pragma unroll
          for (int j = 0; j < 5; ++j) W[i.value * 5 + j] = __builtin_bit_cast(bf16x8, ld16p(wr, vl + i.value * 1024, w4b + j * (48 * 1024)));
          cfence();
        }
        if constexpr (i.value == 7) m_issue<6, 0>(a[0], wr, l, (unsigned)WS_U + g4(0) * 49152 + w * 6144, vl);
        float sum, sq, mean, var, m, s;
        e_stat1(sd, sum, sq, lb, n, li);
        if constexpr (i.value == 0) e_cs(cs, ce, lb + L_CS + 256 + q * 32 + g * 8);
        DT(10, 2 + 5 * i.value);
        ln_fast(sum, sq, mean, var, m, s);
        float rr = __builtin_amdgcn_rcpf(s);
        const float nm = -m * rr;
        float c[6];
#pragma unroll
        for (int r = 0; r < 6; ++r) c[r] = cs[r] * nm;
        asm volatile("" : "+v"(rr), "+v"(c[0]), "+v"(c[1]), "+v"(c[2]), "+v"(c[3]), "+v"(c[4]), "+v"(c[5]));
        DT(10, 3 + 5 * i.value);
        float y[4], z[2];
        f32x2 e[4];
        e_sum<4, 6>(y, e, lb, pe + (n & 1) * SLOT, px6 + (n & 1) * SLOT, n);
        if constexpr (i.value < 7) sd = e_stat0(lb, n + 1, li);
        if (i == 0) { ST(10); }
        DT(10, 4 + 5 * i.value);
        e_sum2(z, e);
#pragma unroll
        for (int r = 0; r < 4; ++r) y[r] = y[r] * rr + c[r];
        z[0] = z[0] * rr + c[4]; z[1] = z[1] * rr + c[5];
        bfr2(y[0], y[1]); bfr2(y[2], y[3]); bfr2(z[0], z[1]);
        const unsigned uo = (unsigned)WS_U + g8(i) * 49152;
        st8(wr, v8, uo + u6, u32x2{pks(gelu(y[0]), gelu(y[1])), pks(gelu(y[2]), gelu(y[3]))});
        st4(wr, v4, uo + u6x, pks(gelu(z[0]), gelu(z[1])));
        DT(10, 5 + 5 * i.value);
      });
      ST(11);
    }
  }
  if (gb != 0) {
    CTX;
    auto soh = [&](int i) { return (unsigned)WS_H + g4(i) * 20480 + q * 5120; };
    auto sox = [&](int i) { return (unsigned)WS_X1 + g8(i) * 20480 + q * 5120; };
    __builtin_amdgcn_s_barrier();
    wait_vm<2>();
    lds_inc(lb + C_W2);
    ST(17);
    auto add2 = [&](unsigned u, float y0, float y1) { bfr2(y0, y1); return pks(bf_lo(u) + y0, bf_hi(u) + y1); };
#pragma unroll
    for (int i = 0; i < 4; ++i) {
      float y[4], z[2];
      f32x2 e[4];
      e_sum<4, 5>(y, e, lb, pe + (i & 1) * SLOT, px + (i & 1) * SLOT, i);
      if (i == 0) { ST(4); }
      const unsigned ho = (unsigned)WS_H + g4(i) * 20480;
      xv[i] = u32x2{add2(xv[i][0], y[0], y[1]), add2(xv[i][1], y[2], y[3])};
      st8(wr, v8, ho + u5, xv[i]);
      if (turn(i)) {
        e_sum2(z, e);
        xx[i] = add2(xx[i], z[0], z[1]);
        st4(wr, v4, ho + u5x, xx[i]);
      }
      if (i < 2) { const int u = i * 128 + wid; touch(ws + WS_W3 + (unsigned)((u < 240 ? u : 239) * 8192), l, lb); }
      if (i == 2) m_issue<5, 2>(a[0], wr, l, soh(0), vl);
    }
    ST(5);
    m_phase<5, 5, 4, 2, 3, 1>(W, a, l, lb, wr, 4, q, soh, lane, [&](int i) { return ring(4 + i); }, slot2, nohook STPASS(6));
    {
      const unsigned w3b = (unsigned)WS_W3 + ct16 * (6 * 20 * 1024) + q * 5120;
#pragma unroll
      for (int f = 0; f < 30; ++f) W[f] = __builtin_bit_cast(bf16x8, ld16p(wr, vl + (f / 6) * 1024, w3b + (f % 6) * (20 * 1024)));
      lds_wait(lb + C_WF, 4);
      auto stage4 = [&](int f) { glds16(ws + (w4b + (f % 5) * (48 * 1024) + (f / 5) * 1024), vl, lb + L_W + (q * 26 + f) * 1024); };
#pragma unroll
      for (int f = 0; f < 8; ++f) stage4(f);
      cfence();
#pragma unroll
      for (int i = 0; i < 4; ++i) {
        const int n = 8 + i;
        float y[4], z[2];
        f32x2 e[4];
        e_sum<4, 5>(y, e, lb, pe + (n & 1) * SLOT, px + (n & 1) * SLOT, n);
        if (i == 0) { ST(8); }
        const f32x2 mr = lds_ld<f32x2>(lb + L_LN + (i * 16 + li) * 8);
        auto hb = [&](unsigned u) { float p = bf_lo(u) - mr[0], r = bf_hi(u) - mr[0]; bfr2(p, r); return pks(p * mr[1], r * mr[1]); };
        const unsigned xo1 = (unsigned)WS_X1 + g4(i) * 20480;
        st8(wr, v8, xo1 + u5, u32x2{add2(hb(xv[i][0]), y[0], y[1]), add2(hb(xv[i][1]), y[2], y[3])});
        if (turn(i)) {
          e_sum2(z, e);
          st4(wr, v4, xo1 + u5x, add2(hb(xx[i]), z[0], z[1]));
        }
        if (i < 3) {
#pragma unroll
          for (int f = 0; f < 6; ++f) stage4(8 + 6 * i + f);
          cfence();
        }
        if (i == 2) { m_issue<5, 2>(a[0], wr, l, sox(0), vl); m_issue<5, 2>(a[1], wr, l, sox(1), vl); }
      }
      ST(9);
    }
    m_phase<6, 5, 8, 2, 3, 2>(W, a, l, lb, wr, 12, q, sox, lane, [&](int i) { return ring(12 + i); }, slot2, nohook STPASS(10));
    m_issue<6, 0>(a[0], wr, l, (unsigned)WS_U + g4(0) * 49152 + w * 6144, vl);
#pragma unroll
    for (int j = 1; j < 5; ++j) W[25 + j] = __builtin_bit_cast(bf16x8, ld16p(wr, vl + 5 * 1024, w4b + j * (48 * 1024)));
#pragma unroll
    for (int f = 0; f < 26; ++f) W[f] = *(const LAS bf16x8*)(uintptr_t)(lb + L_W + (q * 26 + f) * 1024 + vl);
    cfence();
    lds_inc(lb + C_WC);
  }
  {
    CTX;
    const rsrc_t orr = __builtin_amdgcn_make_buffer_rsrc(out, 0, S_ * D_ * 2, 0x00020000);
    u32x2 hv[2], yv[2];
    unsigned hx, yx;
#pragma unroll
    for (int i = 0; i < 2; ++i) {
      const unsigned to = g4(2 * i + grp) * 20480 + u5;
      hv[i] = ld8(wr, v8, (unsigned)WS_H + to); yv[i] = ld8(wr, v8, (unsigned)WS_X1 + to);
    }
    {
      const unsigned to = g4(2 * (q >> 1) + grp) * 20480 + u5x;
      hx = ld4(wr, v4, (unsigned)WS_H + to); yx = ld4(wr, v4, (unsigned)WS_X1 + to);
    }
    auto add3 = [&](unsigned h, unsigned u, float y0, float y1) {
      bfr2(y0, y1);
      y0 += bf_lo(u); y1 += bf_hi(u);
      bfr2(y0, y1);
      return pks(bf_lo(h) + y0, bf_hi(h) + y1);
    };
    auto fin = [&](int p, auto si) {
      constexpr int s = si.value;
      float y[4], z[2];
      f32x2 e[8];
      e_sum<8, 5>(y, e, lb, pe + (p % 3) * SLOT7, px + (p % 3) * SLOT7, 20 + p);
      const unsigned oo = g4(p) * (16 * D_ * 2);
      __builtin_amdgcn_raw_buffer_store_b64(u32x2{add3(hv[s][0], yv[s][0], y[0], y[1]), add3(hv[s][1], yv[s][1], y[2], y[3])}, orr, xo, oo + xc, 0);
      if (turn(s)) {
        e_sum2(z, e);
        __builtin_amdgcn_raw_buffer_store_b32(add3(hx, yx, z[0], z[1]), orr, xox, oo + xcx, 0);
      }
    };
    m_phase<5, 6, 4, 0, 2, 1>(W, a, l, lb, wr, 20, w, [&](int i) { return (unsigned)WS_U + g4(i) * 49152 + w * 6144; }, lane,
        [&](auto i) { return lb + (i.value == 0 ? C_F + 19 * 4 : i.value == 1 ? C_WC : i.value == 2 ? C_OK : C_F + 20 * 4); },
        [&](auto i) { return (unsigned)((i.value % 3) * SLOT7); },
        [&](auto i) {
          if constexpr (i.value == 1) { if (grp == 0) fin(0, std::integral_constant<int, 0>{}); }
          if constexpr (i.value == 2) { if (grp == 1) fin(1, std::integral_constant<int, 0>{}); }
          if constexpr (i.value == 3) fin(2 + grp, std::integral_constant<int, 1>{});
        } STPASS(12));
    STDUMP2(ws, 2 + grp);
  }
  asm volatile("mx_kp_end:");
}

extern "C" void kernel_launch(void* const* d_in, const int* in_sizes, int n_in, void* d_out, int out_size,
                              void* d_ws, size_t ws_size, hipStream_t stream) {
  const char* x = (const char*)d_in[0];
  const char* wqkv = (const char*)d_in[1];
  const char* wo = (const char*)d_in[2];
  const char* w1 = (const char*)d_in[4];
  const char* w2 = (const char*)d_in[5];
  const char* w3 = (const char*)d_in[6];
  const char* w4 = (const char*)d_in[7];
  char* ws = (char*)d_ws;
  if (ws_size < WS_END) return;
  (void)hipFuncSetAttribute((const void*)k1_qkv, hipFuncAttributeMaxDynamicSharedMemorySize, LDS_TOT);
  (void)hipFuncSetAttribute((const void*)kp_main, hipFuncAttributeMaxDynamicSharedMemorySize, LDS_TOT);
  k1_qkv<<<256, 384, LDS_TOT, stream>>>(x, wqkv, wo, w1, w2, w3, w4, ws);
  kp_main<<<256, 512, LDS_TOT, stream>>>(x, ws, (char*)d_out);
}
```

```cpp
#include <hip/hip_runtime.h>
#include <hip/hip_bf16.h>
#include <stdint.h>
#include <type_traits>

typedef __attribute__((ext_vector_type(8))) __bf16 bf16x8;
typedef __attribute__((ext_vector_type(4))) __bf16 bf16x4;
typedef __attribute__((ext_vector_type(2))) __bf16 bf16x2;
typedef __attribute__((ext_vector_type(4))) short s16x4;
typedef __attribute__((ext_vector_type(4))) float f32x4;
typedef __attribute__((ext_vector_type(2))) unsigned u32x2;
typedef __attribute__((ext_vector_type(4))) unsigned u32x4;
#define LAS __attribute__((address_space(3)))
#define DEV __device__ __forceinline__

constexpr int S_ = 2048, D_ = 640, F_ = 1536, H_ = 32;

constexpr size_t QK_HEAD = 128 * 640;
constexpr size_t V_HEAD  = 64 * 1280;
constexpr size_t WS_Q  = 0;
constexpr size_t WS_K  = WS_Q + H_ * QK_HEAD;
constexpr size_t WS_V  = WS_K + H_ * QK_HEAD;
constexpr size_t WS_O  = WS_V + H_ * V_HEAD;
constexpr size_t WS_G  = WS_O + (size_t)S_ * D_ * 2;
constexpr size_t WS_H  = WS_G + (size_t)S_ * D_ * 2;
constexpr size_t WS_X1 = WS_H + (size_t)S_ * D_ * 2;
constexpr size_t WS_U  = WS_X1 + (size_t)S_ * D_ * 2;
constexpr size_t WS_WO = WS_U + (size_t)S_ * F_ * 2;
constexpr size_t WS_W1 = WS_WO + (size_t)D_ * D_ * 2;
constexpr size_t WS_W2 = WS_W1 + (size_t)D_ * D_ * 2;
constexpr size_t WS_W3 = WS_W2 + (size_t)D_ * D_ * 2;
constexpr size_t WS_W4 = WS_W3 + (size_t)D_ * F_ * 2;
constexpr size_t WS_END = WS_W4 + (size_t)D_ * F_ * 2;
static_assert(WS_H - WS_O == 1280 * 4096, "fill");
constexpr unsigned EMPTY = 0xffffffffu;

template <int I, int N, class Fn> DEV void static_for(Fn&& f) {
  if constexpr (I < N) { f(std::integral_constant<int, I>{}); static_for<I + 1, N>(f); }
}
template <int N> DEV void wait_vm() { asm volatile("s_waitcnt vmcnt(%0) lgkmcnt(0)" ::"n"(N) : "memory"); }
DEV void glds16(const char* sbase, unsigned voff, unsigned lds) {
  asm volatile("s_mov_b32 m0, %0\n\ts_nop 0\n\tglobal_load_lds_dwordx4 %1, %2" ::"s"(lds), "v"(voff), "s"(sbase) : "memory");
}
DEV unsigned lds_addr(const void* p) { return (unsigned)(uintptr_t)(LAS const void*)p; }
#define ST(i)
#define DT(ph, i)
#define STR(i)
#define STDECL
#define STDUMP(o, ws, kid)
#define STARG
#define STSIDE(ws, kid, t0)
#define STPASS(i)
#define STDUMP2(ws, kid)
DEV void wait_lgkm0() { asm volatile("s_waitcnt lgkmcnt(0)" ::: "memory"); }
DEV void cfence() { asm volatile("" ::: "memory"); }
DEV float bfr(float x) { return (float)(__bf16)x; }
DEV unsigned pk2(float a, float b) { bf16x2 v = {(__bf16)a, (__bf16)b}; return __builtin_bit_cast(unsigned, v); }
DEV float bf_lo(unsigned u) { return __builtin_bit_cast(float, u << 16); }
DEV float bf_hi(unsigned u) { return __builtin_bit_cast(float, u & 0xffff0000u); }
DEV f32x4 mfma16(bf16x8 a, bf16x8 b, f32x4 c) { return __builtin_amdgcn_mfma_f32_16x16x32_bf16(a, b, c, 0, 0, 0); }
typedef __amdgpu_buffer_rsrc_t rsrc_t;
constexpr unsigned OOB = 0x40000000u;
template <int AUX> DEV void st16r(rsrc_t r, unsigned v, unsigned s, u32x4 d) {
  asm("" : "+v"(d));
  __builtin_amdgcn_raw_buffer_store_b128(d, r, v, s, AUX);
  asm volatile("s_nop 1" ::"v"(d) : "memory");
}
DEV bf16x8 ones8() { return __builtin_bit_cast(bf16x8, u32x4{0x3F803F80u, 0x3F803F80u, 0x3F803F80u, 0x3F803F80u}); }

DEV void ln_stats(float sum, float sq, float& mean, float& var, float& m, float& s) {
  mean = sum * (1.0f / D_);
  var = fmaxf(sq * (1.0f / D_) - mean * mean, 0.f);
  m = bfr(mean);
  s = bfr(sqrtf(bfr(bfr(var) + 1e-5f)));
}

DEV unsigned f2u(float v) { return __builtin_bit_cast(unsigned, v); }
DEV float u2f(unsigned v) { return __builtin_bit_cast(float, v); }
DEV float max_g(float v) {
  const auto r = __builtin_amdgcn_permlane16_swap(f2u(v), f2u(v), false, false);
  v = fmaxf(u2f(r[0]), u2f(r[1]));
  const auto s = __builtin_amdgcn_permlane32_swap(f2u(v), f2u(v), false, false);
  return fmaxf(u2f(s[0]), u2f(s[1]));
}

DEV unsigned wrm_voff(int l, int q, int ldw) {
  return (unsigned)((l >> 4) * ldw + ((((l & 15) >> 1) ^ ((l >> 4) | (q << 2))) * 32) + (l & 1) * 16);
}
DEV bf16x8 wrm_frag(const char* p) {
  s16x4 lo = __builtin_amdgcn_ds_read_tr16_b64_v4i16((LAS s16x4*)(p));
  s16x4 hi = __builtin_amdgcn_ds_read_tr16_b64_v4i16((LAS s16x4*)(p + 1024));
  typedef __attribute__((ext_vector_type(8))) short s16x8;
  s16x8 v = __builtin_shufflevector(lo, hi, 0, 1, 2, 3, 4, 5, 6, 7);
  return __builtin_bit_cast(bf16x8, v);
}
DEV unsigned wrm_lane(int l, int jhi, int jj) {
  const int g = l >> 4, i = l & 15;
  return (unsigned)((8 * g + (i >> 2)) * 256 + ((((jhi ^ (g & 1)) << 2) | (jj ^ (i >> 2))) * 32) + (i & 3) * 8);
}

struct WJob { const char* src; const char* code; char* ws; unsigned dst, coff, c1off, fill; int ldw, wks, r4, perm; };
DEV WJob make_job(int id, const char* wo, const char* w1, const char* w2, const char* w3, const char* w4) {
  const bool live = id < 390;
  const int b = live ? id : 389;
  const char* wsrc; unsigned wdst; int ldw = D_ * 2, wks = 20, kt, nt;
  if (b < 150) {
    const int mi = b / 50, u = b - mi * 50;
    wsrc = mi == 0 ? wo : (mi == 1 ? w1 : w2); wdst = (unsigned)WS_WO + mi * (D_ * D_ * 2);
    kt = u / 5; nt = u - kt * 5;
  } else if (b < 270) {
    const int u = b - 150; wsrc = w3; wdst = (unsigned)WS_W3; ldw = F_ * 2; kt = u / 12; nt = u - kt * 12;
  } else {
    const int u = b - 270; wsrc = w4; wdst = (unsigned)WS_W4; wks = 48; kt = u / 5; nt = u - kt * 5;
  }
  WJob j;
  j.src = wsrc + nt * 256;
  j.r4 = kt * 16; j.perm = b < 50;
  j.dst = live ? wdst + (unsigned)(nt * 8 * wks + kt * 2) * 1024 : OOB;
  j.ldw = ldw; j.wks = wks;
  return j;
}
DEV void touch4(const char* p, unsigned voff, unsigned lds) {
  asm volatile("s_mov_b32 m0, %0\n\ts_nop 0\n\tglobal_load_lds_dword %1, %2" ::"s"(lds), "v"(voff), "s"(p) : "memory");
}

#define NFILL 0
#define FT0 7
#define FDELAY 40
#define TGRP 7
#define FCUT 0
#define JCUM {0, 3, 6, 9, 12, 14, 16, 16, 16, 16, 16}
constexpr int NSPARE = (1280 - 240 * NFILL) / 16;
static_assert(NSPARE * 16 + 240 * NFILL == 1280, "fill");
template <bool SWAP>
struct GemmQ {
  f32x4 acc[4][4];
  STDECL;
  static constexpr int NG = 4, D = 3, NGRP = 10, SB = 32768, JOB = NG * SB, XCH = JOB;
  DEV bool run(char* smem, const char* Ab, const char* Wb, int k0, const WJob& job, rsrc_t wr) {
    STR(0); ST(1);
    constexpr int LDA = D_ * 2, LDW = 3 * D_ * 2;
    const int tid = threadIdx.x, l = tid & 63;
    const int wave = __builtin_amdgcn_readfirstlane(tid >> 6);
    if (wave >= 4) {
      const bool isw = wave == 5;
      unsigned vo[2];
#pragma unroll
      for (int q = 0; q < 2; ++q)
        vo[q] = isw ? wrm_voff(l, q, LDW) : (unsigned)((l >> 3) * LDA + (((l & 7) ^ (q * 4 + (l >> 4))) * 16));
      const unsigned vx[4] = {vo[0], isw ? vo[0] : vo[1], isw ? vo[1] : vo[0], vo[1]};
      const unsigned dst0 = lds_addr(smem) + (isw ? 16384 : 0);
      const char* src0 = isw ? Wb : Ab;
      const int gstr = isw ? 64 * LDW : 128, istr = isw ? 4 * LDW : 8 * LDA;
      auto issue = [&](int grp) {
        int kg = grp + k0;
        kg = kg >= NGRP ? kg - NGRP : kg;
        const char* sp = src0 + (size_t)kg * gstr;
#pragma unroll
        for (int i = 0; i < 16; ++i) glds16(sp + (size_t)i * istr, vx[i & 3], dst0 + (grp % NG) * SB + i * 1024);
      };
      static_for<0, D>([&](auto gi) { issue(gi.value); });
      touch4(job.code, job.c1off, dst0 + JOB + 2048);
      static_for<0, NGRP>([&](auto gi) {
        constexpr int grp = gi.value;
        constexpr int nj[NGRP + 1] = JCUM;
        constexpr int later = grp >= 8 ? 16 + (TGRP == 7) : 32 + (grp < 3) + nj[grp] - nj[grp < 3 ? 0 : grp - 3];
        wait_vm<later>();
        __builtin_amdgcn_s_barrier();
        cfence();
        if constexpr (grp + D < NGRP) issue(grp + D);
#pragma unroll
        for (int i = nj[grp]; i < nj[grp + 1]; ++i) {
          int r4 = job.r4 + i;
          const int kq = r4 / 40, rr = r4 - kq * 40;
          if (job.perm) r4 = rr < 32 ? (8 * kq + (rr >> 2)) * 5 + (rr & 3) : (8 * kq + rr - 32) * 5 + 4;
          glds16(job.src + (size_t)r4 * 4 * job.ldw, wrm_voff(l, (i >> 1) & 1, job.ldw), dst0 + JOB + i * 1024);
        }
        if constexpr (grp == TGRP) touch4(job.code, job.coff, dst0 + (6 % NG) * SB);
        if constexpr (grp == 8) {
          const char* tb = smem + JOB + (isw ? 16384 : 0);
#pragma unroll
          for (int kk = 0; kk < 2; ++kk)
#pragma unroll
            for (int j = 0; j < 8; ++j) {
              const bf16x8 f = wrm_frag(tb + kk * 8192 + wrm_lane(l, j >> 2, j & 3));
              st16r<16>(wr, job.dst + l * 16, (unsigned)(j * job.wks + kk) * 1024, __builtin_bit_cast(u32x4, f));
            }
        }
      });
      __builtin_amdgcn_s_barrier();
      STSIDE(job.ws, 4, st_[0]);
      return false;
    }
    const int wm = wave >> 1, wn = wave & 1, g = l >> 4;
#pragma unroll
    for (int i = 0; i < 4; ++i)
#pragma unroll
      for (int j = 0; j < 4; ++j) acc[i][j] = f32x4{0.f, 0.f, 0.f, 0.f};
    const unsigned a0 = (unsigned)((wm * 64 + (l & 15)) * 128 + ((g ^ ((l & 15) >> 1)) * 16));
    const char* al[2][2] = {{smem + a0 + wn * 4096, smem + a0 + 4096 - wn * 4096},
                            {smem + (a0 ^ 64u) + wn * 4096, smem + (a0 ^ 64u) + 4096 - wn * 4096}};
    const char* wl[4];
#pragma unroll
    for (int j = 0; j < 4; ++j) wl[j] = smem + 16384 + wrm_lane(l, wn, j ^ (2 * wm));
    bf16x8 af[2][4], wf[2][4];
    f32x4 ms[2] = {}, mq[2] = {}, mc[2] = {};
    auto ldfrag = [&](auto ti) {
      constexpr int t = ti.value, kk = t & 1, so = ((t >> 1) % NG) * SB;
#pragma unroll
      for (int i = 0; i < 4; ++i) af[kk][i] = *(const bf16x8*)(al[kk][i >> 1] + so + (i & 1) * 2048);
#pragma unroll
      for (int j = 0; j < 4; ++j) wf[kk][j] = wrm_frag(wl[j] + so + kk * 8192);
    };
    __builtin_amdgcn_s_barrier();
    cfence();
    ST(3);
    ldfrag(std::integral_constant<int, 0>{});
    static_for<0, 2 * NGRP>([&](auto ti) {
      constexpr int t = ti.value, kk = t & 1;
      if constexpr (t + 1 < 2 * NGRP) {
        if constexpr (kk == 1) {
          wait_lgkm0();
          __builtin_amdgcn_s_barrier();
          cfence();
          ST(3 + (t + 1) / 2);
        }
        ldfrag(std::integral_constant<int, t + 1>{});
      }
      if constexpr (t >= FT0 && t < FT0 + NFILL) __builtin_amdgcn_raw_buffer_store_b128(u32x4{EMPTY, EMPTY, EMPTY, EMPTY}, wr, tid * 16, job.fill + (t - FT0) * 4096, 16);
      __builtin_amdgcn_sched_barrier(0);
#pragma unroll
      for (int i = 0; i < 4; ++i)
#pragma unroll
        for (int j = 0; j < 4; ++j)
          acc[i][j] = SWAP ? mfma16(wf[kk][j], af[kk][i], acc[i][j]) : mfma16(af[kk][i], wf[kk][j], acc[i][j]);
#pragma unroll
      for (int i = 0; i < 2; ++i) {
        ms[i] = mfma16(ones8(), af[kk][i], ms[i]); mq[i] = mfma16(af[kk][i], af[kk][i], mq[i]); mc[i] = mfma16(ones8(), wf[kk][i], mc[i]);
      }
      __builtin_amdgcn_sched_barrier(0);
    });
    ST(21);
    float2* xs = (float2*)(smem + XCH);
    float* xc = (float*)(smem + XCH + 1024);
    const int li = l & 15;
#pragma unroll
    for (int i = 0; i < 2; ++i) {
      const float q01 = (li & 1) ? mq[i][1] : mq[i][0], q23 = (li & 1) ? mq[i][3] : mq[i][2];
      float mean, var, m, sd;
      ln_stats(ms[i][0], (li & 2) ? q23 : q01, mean, var, m, sd);
      const float rinv = 1.0f / sd;
      if (g == (li >> 2)) { xs[(wm * 4 + wn * 2 + i) * 16 + li] = float2{rinv, -m * rinv}; xc[(wn * 4 + wm * 2 + i) * 16 + li] = mc[i][0]; }
    }
    wait_lgkm0();
    __builtin_amdgcn_s_barrier();
    cfence();
    return true;
  }
};

template <bool ISV>
DEV void qkv_body(char* smem, const char* x, const char* wqkv, char* ws, rsrc_t wr, int rt, int ct, int k0, const WJob& job) {
  using GQ = GemmQ<!ISV>;
  GQ gm;
  const int row0 = rt * 128, n0 = ct * 128;
  if (!gm.run(smem, x + (size_t)row0 * (D_ * 2), wqkv + n0 * 2, k0, job, wr)) return;
  const int tid = threadIdx.x, l = tid & 63, wave = __builtin_amdgcn_readfirstlane(tid >> 6), wm = wave >> 1, wn = wave & 1, g = l >> 4;
  const float2* xs = (const float2*)(smem + GQ::XCH);
  const float* xc = (const float*)(smem + GQ::XCH + 1024);
  if constexpr (!ISV) {
    f32x4 cs[4];
    unsigned po[4];
#pragma unroll
    for (int j = 0; j < 4; ++j) {
      const int ja = wn * 4 + (j ^ (2 * wm));
      cs[j] = *(const f32x4*)(xc + ja * 16 + 4 * g);
      const int qd = ct * 32 + ja * 4 + g, head = qd / 5;
      po[j] = (unsigned)(head * (int)QK_HEAD + (qd - head * 5) * 128 + (l & 15) * 8);
    }
#pragma unroll
    for (int i = 0; i < 4; ++i) {
      const int ia = wm * 4 + (i ^ (2 * wn));
      const float2 st = xs[ia * 16 + (l & 15)];
#pragma unroll
      for (int j = 0; j < 4; ++j) {
        const f32x4 y = gm.acc[i][j] * st.x + cs[j] * st.y;
        __builtin_amdgcn_raw_buffer_store_b64(u32x2{pk2(y[0], y[1]), pk2(y[2], y[3])}, wr, po[j], (unsigned)WS_Q + (rt * 8 + ia) * 640, 16);
      }
    }
  } else {
    float cd[4];
    unsigned po[4];
#pragma unroll
    for (int j = 0; j < 4; ++j) {
      const int ja = wn * 4 + (j ^ (2 * wm));
      cd[j] = xc[ja * 16 + (l & 15)];
      const int c = n0 - 2 * D_ + ja * 16 + (l & 15), head = c / 20, d = c - head * 20;
      po[j] = (unsigned)(head * (int)V_HEAD + (d < 16 ? (g * 16 + d) * 16 : 1024 + (g * 4 + d - 16) * 16));
    }
#pragma unroll
    for (int i2 = 0; i2 < 2; ++i2) {
      const int ia = wm * 4 + ((2 * i2) ^ (2 * wn));
      f32x4 rr[2], nm[2];
#pragma unroll
      for (int h = 0; h < 2; ++h) {
        const f32x4 s01 = *(const f32x4*)(xs + (ia + h) * 16 + 4 * g), s23 = *(const f32x4*)(xs + (ia + h) * 16 + 4 * g + 2);
        rr[h] = f32x4{s01[0], s01[2], s23[0], s23[2]}; nm[h] = f32x4{s01[1], s01[3], s23[1], s23[3]};
      }
#pragma unroll
      for (int j = 0; j < 4; ++j) {
        const f32x4 y = gm.acc[2 * i2][j] * rr[0] + nm[0] * cd[j], z = gm.acc[2 * i2 + 1][j] * rr[1] + nm[1] * cd[j];
        st16r<16>(wr, po[j], (unsigned)WS_V + (rt * 4 + (ia >> 1)) * 1280, u32x4{pk2(y[0], y[1]), pk2(y[2], y[3]), pk2(z[0], z[1]), pk2(z[2], z[3])});
      }
    }
  }
  STDUMP(gm, ws, 1);
}

__global__ __launch_bounds__(384) void k1_qkv(const char* x, const char* wqkv, const char* wo, const char* w1, const char* w2,
                                              const char* w3, const char* w4, char* ws) {
  extern __shared__ __attribute__((aligned(16))) char smem[];
  const int b = blockIdx.x, xcd = b & 7, j = b >> 3, r = j & 3, c = j >> 2;
  const char* pc;
  unsigned c0, c1;
  asm volatile("s_getpc_b64 %0\nmx_k1_pc:\n\ts_mov_b32 %1, mx_kp_begin-mx_k1_pc\n\ts_mov_b32 %2, mx_kp_end-mx_k1_pc" : "=s"(pc), "=s"(c0), "=s"(c1));
  const rsrc_t wr = __builtin_amdgcn_make_buffer_rsrc(ws, 0, (int)WS_END, 0x00020000);
  if ((xcd & 1) && c == 7) {
    if (threadIdx.x < 256) {
      const unsigned f0 = (unsigned)WS_O + (240 * NFILL + ((xcd >> 1) * 4 + r) * NSPARE) * 4096;
#pragma unroll 1
      for (int i = 0; i < FDELAY; ++i) __builtin_amdgcn_s_sleep(2);
#pragma unroll 4
      for (int i = 0; i < NSPARE - FCUT; ++i) __builtin_amdgcn_raw_buffer_store_b128(u32x4{EMPTY, EMPTY, EMPTY, EMPTY}, wr, threadIdx.x * 16, f0 + i * 4096, 16);
      STSIDE(ws, 5, t0_);
    }
    return;
  }
  const int rt = (xcd >> 1) * 4 + r, ct = (xcd & 1) * 8 + c;
  const int wave = __builtin_amdgcn_readfirstlane(threadIdx.x >> 6);
  WJob job = make_job((rt * 15 + ct) * 2 + (wave & 1), wo, w1, w2, w3, w4);
  {
    const unsigned u = (j & 15) * 2 + (wave & 1);
    const unsigned o = c0 + u * 2560 + ((threadIdx.x & 63) % 20) * 128;
    const unsigned o1 = u * 2048 + (threadIdx.x & 15) * 128;
    job.code = pc; job.coff = o < c1 ? o : c1; job.c1off = o1 < c0 ? o1 : c0;
    job.fill = (unsigned)WS_O + (rt * 15 + ct) * (NFILL * 4096); job.ws = ws;
  }
  const int k0 = (3 * r + 4 * c) % 10;
  if (ct >= 10) qkv_body<true>(smem, x, wqkv, ws, wr, rt, ct, k0, job);
  else qkv_body<false>(smem, x, wqkv, ws, wr, rt, ct, k0, job);
}

typedef __attribute__((ext_vector_type(2))) float f32x2;
typedef unsigned long long u64;
#define SB0 __builtin_amdgcn_sched_barrier(0)
#define PR 1
#define SL 0
#define TC6 35
#define TC5 8
#define EA 2
#define EA1 2
#define XM 2
#define XS 1
#define XW 1
#define ES 0
#define ES1 0
DEV float opq(float v) { asm("" : "+v"(v)); return v; }
DEV unsigned pks(float a, float b) { unsigned u = pk2(opq(a), opq(b)); asm("" : "+v"(u)); return u; }
DEV void bfr2(float& a, float& b) { const unsigned u = pks(a, b); a = bf_lo(u); b = bf_hi(u); }
DEV float bfr1(float a) { return bf_lo(pks(a, a)); }
DEV void ln_fast(float sum, float sq, float& mean, float& var, float& m, float& s) {
  mean = opq(sum * (1.0f / D_));
  var = fmaxf(opq(sq * (1.0f / D_)) - mean * mean, 0.f);
  float v = var;
  m = mean;
  bfr2(m, v);
  s = bfr1(__builtin_amdgcn_sqrtf(bfr1(v + 1e-5f)));
}
DEV float gelu(float x) {
  const float c0 = -2.0f * 0.7978845608028654f * 1.4426950408889634f, c1 = c0 * 0.044715f;
  const float z = x * (x * x * c1 + c0);
  return x * __builtin_amdgcn_rcpf(__builtin_amdgcn_exp2f(z) + 1.0f);
}

DEV void reset1k(rsrc_t r, unsigned v, unsigned s) { __builtin_amdgcn_raw_buffer_store_b128(u32x4{EMPTY, EMPTY, EMPTY, EMPTY}, r, v, s, 16); }
DEV unsigned full(unsigned v) { return v < EMPTY ? v : EMPTY - 1; }
DEV void st4(rsrc_t r, unsigned v, unsigned s, unsigned d) { __builtin_amdgcn_raw_buffer_store_b32(full(d), r, v, s, 16); }
DEV void st8(rsrc_t r, unsigned v, unsigned s, u32x2 d) { __builtin_amdgcn_raw_buffer_store_b64(u32x2{full(d[0]), full(d[1])}, r, v, s, 16); }
DEV void st16(rsrc_t r, unsigned v, unsigned s, u32x4 d) { st16r<16>(r, v, s, u32x4{full(d[0]), full(d[1]), full(d[2]), full(d[3])}); }
DEV u32x4 ld16(rsrc_t r, unsigned v, unsigned s) { return __builtin_amdgcn_raw_buffer_load_b128(r, v, s, 16); }
DEV u32x2 ld8(rsrc_t r, unsigned v, unsigned s) { return __builtin_amdgcn_raw_buffer_load_b64(r, v, s, 16); }
DEV unsigned ld4(rsrc_t r, unsigned v, unsigned s) { return __builtin_amdgcn_raw_buffer_load_b32(r, v, s, 16); }
DEV u32x4 ld16p(rsrc_t r, unsigned v, unsigned s) { return __builtin_amdgcn_raw_buffer_load_b128(r, v, s, 0); }
DEV u32x2 ld8p(rsrc_t r, unsigned v, unsigned s) { return __builtin_amdgcn_raw_buffer_load_b64(r, v, s, 0); }
DEV unsigned ld4p(rsrc_t r, unsigned v, unsigned s) { return __builtin_amdgcn_raw_buffer_load_b32(r, v, s, 0); }
DEV void vm0() { __builtin_amdgcn_s_waitcnt(0x0f70); cfence(); }

constexpr int LDS_TOT = 163840;
constexpr int AT_Q = 0, AT_K = AT_Q + 16 * 640, AT_V = AT_K + 32 * 640;
constexpr int SLOT = 24576, SLOT7 = 40960;
constexpr int L_W = 51200;
constexpr int L_CS = 157696;
constexpr int L_SX = 159744;
constexpr int L_SY = 161792;
constexpr int C_P = L_SY, C_S = L_SY + 128, C_F = L_SY + 256, C_W2 = L_SY + 384, C_WF = L_SY + 388, C_WC = L_SY + 392, C_OK = L_SY + 396, C_V = L_SY + 400;
constexpr int L_LN = L_SY + 512;
constexpr int L_DUM = L_SY + 1024;
DEV void touch(const char* p, int l, unsigned lb) { touch4(p, l * 128, lb + L_DUM + 512); }
struct Sy {
  unsigned b, one;
  DEV unsigned operator()(int a) const { return b + (a - L_SX); }
};
DEV void lds_inc(const Sy& sy, int a) {
  asm volatile("s_mov_b64 exec, 1\n\tds_add_u32 %0, %1 offset:%2\n\ts_mov_b64 exec, -1" ::"v"(sy.b), "v"(sy.one), "i"(a - L_SX) : "memory");
}
DEV void lds_wait(unsigned a, unsigned target) {
  for (int it = 0; it < (1 << 22); ++it) {
    const unsigned v = __builtin_amdgcn_readfirstlane(*(volatile LAS unsigned*)(uintptr_t)a);
    if (v >= target) break;
    if (SL) __builtin_amdgcn_s_sleep(SL);
  }
  cfence();
}
template <class T> DEV T lds_ld(unsigned a) { return *(const volatile LAS T*)(uintptr_t)a; }
DEV unsigned max3u(unsigned a, unsigned b, unsigned c) { a = a > b ? a : b; return a > c ? a : c; }

struct KRot { unsigned o[6]; };
DEV KRot krot4(int r) { return KRot{{(r & 3) * 1024u, ((r + 1) & 3) * 1024u, ((r + 2) & 3) * 1024u, ((r + 3) & 3) * 1024u, 4096u, 0u}}; }
DEV KRot krot6(int r) {
  KRot k;
#pragma unroll
  for (int i = 0; i < 6; ++i) { const int t = i + r + (r >> 1); k.o[i] = (t < 6 ? t : t - 6) * 1024u; }
  return k;
}
template <int KW, int MODE>
DEV void m_issue(u32x4 (&a)[6], rsrc_t wr, int l, unsigned s, unsigned v, const KRot& ko) {
#pragma unroll
  for (int k = 0; k < (MODE == 1 ? KW - 1 : KW); ++k) a[k] = ld16(wr, v, s + ko.o[k]);
  if constexpr (MODE == 1) {
    const unsigned t = (l >> 4) * 256 + (l & 15) * 8 + 4096;
    const u32x2 t0 = ld8(wr, t, s), t1 = ld8(wr, t + 128, s);
    a[KW - 1] = u32x4{t0[0], t0[1], t1[0], t1[1]};
  }
  cfence();
}
template <int NT>
DEV void m_cs(const bf16x8 (&W)[30], int l, unsigned lb, int part) {
  f32x4 c[NT];
#pragma unroll
  for (int k = 0; k < 5; ++k)
#pragma unroll
    for (int j = 0; j < NT; ++j) c[j] = mfma16(W[k * NT + j], ones8(), k ? c[j] : f32x4{0.f, 0.f, 0.f, 0.f});
  if ((l & 15) == 0) {
#pragma unroll
    for (int j = 0; j < NT; ++j) *(LAS f32x4*)(uintptr_t)(lb + L_CS + part * 384 + j * 64 + l) = c[j];
  }
  cfence();
  SB0;
}
template <int KW, int MODE, int K>
DEV void m_issue1(u32x4 (&a)[6], rsrc_t wr, int l, unsigned s, unsigned v, const KRot& ko) {
  if constexpr (MODE == 1 && K >= KW - 1) {
    const u32x2 t = ld8(wr, (l >> 4) * 256 + (l & 15) * 8 + 4096 + (K - KW + 1) * 128, s);
    a[KW - 1][2 * (K - KW + 1)] = t[0]; a[KW - 1][2 * (K - KW + 1) + 1] = t[1];
  } else a[K] = ld16(wr, v, s + ko.o[K]);
}
template <int NT, int KW, int NTILE, int MODE, int NB, int PRE, class TS, class TV, class TG, class TL, class TM, class TH>
DEV void m_phase(const bf16x8 (&W)[30], u32x4 (&a)[3][6], int l, unsigned lb, const Sy& sy, rsrc_t wr, int n0, int part, const KRot& ko, TS&& so, TV&& vo, TG&& guard,
                 TL&& slot, TM&& mid, TH&& hook STARG) {
  const int g = l >> 4, li = l & 15;
  const unsigned pw = lb + part * (NT * 1024) + l * 16;
  const unsigned sx = sy.b + li * 32 + part * 8;
  const f32x4 z4 = {0.f, 0.f, 0.f, 0.f};
  constexpr int NS = MODE == 2 ? 2 * KW : 0, NL = KW + (MODE == 1), G0 = NT == 5 ? 3 : 2, T = NS + NT * KW;
  constexpr int TC = NT == 6 ? TC6 : NS + G0 * KW - KW - (MODE == 2 ? 2 : 0) + TC5;
  static_for<PRE, NB - 1>([&](auto i) { m_issue<KW, MODE>(a[i.value], wr, l, so(i), vo(i), ko); });
  if constexpr (NB == 3 && PR == 1) __builtin_amdgcn_s_setprio(2);
  if constexpr (NB == 3 && PR == 2) __builtin_amdgcn_s_setprio(0);
  unsigned mx = 0;
  u32x4 one8 = __builtin_bit_cast(u32x4, ones8());
  if constexpr (MODE == 2) asm volatile("" : "+v"(one8));
  static_for<0, NTILE>([&](auto i) {
    constexpr int B = i.value % NB, BN = (i.value + 1) % NB, BL = (i.value + NB - 1) % NB;
    constexpr bool more = i.value + NB - 1 < NTILE, early = NB == 3 && i.value + 1 < NTILE;
    const int n = n0 + i;
    auto bad = [&]() {
      unsigned m = 0;
#pragma unroll
      for (int k = 0; k < KW; ++k) m = max3u(max3u(m, a[B][k][0], a[B][k][1]), a[B][k][2], a[B][k][3]);
      return __builtin_amdgcn_ballot_w64(m == EMPTY) != 0;
    };
    const bool again = (NB == 3 && i.value > 0) ? __builtin_amdgcn_ballot_w64(mx == EMPTY) != 0 : bad();
    if (i.value >= XM ? __builtin_expect(again, 0) : again) {
      int it = 0;
#pragma unroll 1
      do { m_issue<KW, MODE>(a[B], wr, l, so(i), vo(i), ko); vm0(); } while (bad() && ++it < (1 << 16));
    }
    cfence();
    if (i == 0) { ST(STI); }
    DT(STI, 2 + 4 * i.value);
    mx = 0;
    f32x4 sa = z4, qa = z4, acc[NT];
    float q01 = 0.f, q23 = 0.f;
    const unsigned ga = guard(i), sl = slot(i);
    const unsigned gv = *(volatile LAS unsigned*)(uintptr_t)ga;
    constexpr std::integral_constant<int, more ? i.value + NB - 1 : 0> jn{};
    const unsigned sn = so(jn), vn = vo(jn);
    auto put = [&](int j) { *(LAS f32x4*)(uintptr_t)(pw + sl + j * 1024) = acc[j]; };
    SB0;
    static_for<0, T>([&](auto tt) {
      constexpr int t = tt.value, u = t - NS;
      if constexpr (t < NS) {
        const bf16x8 f = __builtin_bit_cast(bf16x8, a[B][t / 2]);
        if constexpr (t & 1) sa = mfma16(__builtin_bit_cast(bf16x8, one8), f, sa); else qa = mfma16(f, f, qa);
      } else {
        constexpr int gs = u < G0 * KW ? G0 : 2, u0 = u < G0 * KW ? 0 : G0 * KW + (u - G0 * KW) / (2 * KW) * (2 * KW);
        constexpr int j = u0 / KW + (u - u0) % gs, k = (u - u0) / gs;
        acc[j] = mfma16(W[k * NT + j], __builtin_bit_cast(bf16x8, a[B][k]), k ? acc[j] : z4);
      }
      constexpr int LS = NB == 2 ? 1 : 2;
      if constexpr (more && t % LS == LS - 1 && t / LS < NL) m_issue1<KW, MODE, t / LS>(a[BL], wr, l, sn, vn, ko);
      if constexpr (MODE == 2 && u == 2) {
        const float q0 = opq(qa[0]), q1 = opq(qa[1]), q2 = opq(qa[2]), q3 = opq(qa[3]);
        q01 = opq((li & 1) ? q1 : q0); q23 = opq((li & 1) ? q3 : q2);
      }
      if constexpr (MODE == 2 && u == 3) {
        asm volatile("s_mov_b32 exec_lo, 0x00f0000f\n\ts_mov_b32 exec_hi, 0xf0000f00\n\tds_write_b64 %0, %1 offset:%2\n\t"
                     "s_mov_b64 exec, 1\n\tds_add_u32 %3, %4 offset:%5\n\ts_mov_b64 exec, -1"
                     ::"v"(sx), "v"(f32x2{sa[0], (li & 2) ? q23 : q01}), "i"((i.value & 3) * 512), "v"(sy.b), "v"(sy.one), "i"(C_S + n * 4 - L_SX) : "memory");
        DT(STI, 3 + 4 * i.value);
      }
      if constexpr (early && t >= TC && t < TC + KW) {
        constexpr int k = t - TC;
        mx = max3u(max3u(mx, a[BN][k][0], a[BN][k][1]), a[BN][k][2], a[BN][k][3]);
        asm volatile("" : "+v"(mx));
      }
      if constexpr (u == G0 * KW + 1) {
        if (__builtin_expect(__builtin_amdgcn_readfirstlane(gv) < 4u, 0)) lds_wait(ga, 4);
        DT(STI, 4 + 4 * i.value);
      }
      if constexpr (u > G0 * KW + 1 && u < G0 * KW + 2 + 2 * G0 && (u - G0 * KW) % 2 == 0) put((u - G0 * KW) / 2 - 1);
      if constexpr (NT == 6 && (u == 4 * KW + 2 || u == 4 * KW + 4)) put((u - 4 * KW) / 2 + 1);
      if constexpr (t >= T - 7 && t != T - 6 && t != T - 1) mid(i, std::integral_constant<int, (t == T - 7 ? 0 : t - (T - 6))>{});
      SB0;
    });
    put(NT - 2); put(NT - 1);
    lds_inc(sy, C_P + n * 4);
    DT(STI, 5 + 4 * i.value);
    hook(i);
  });
  if constexpr (NB == 3 && PR == 1) __builtin_amdgcn_s_setprio(0);
  if constexpr (NB == 3 && PR == 2) __builtin_amdgcn_s_setprio(2);
  ST(STI + 1);
}

DEV unsigned lds_cnt(unsigned a) { return lds_ld<unsigned>(a); }
template <int NP> DEV float tree(float (&t)[NP]) {
#pragma unroll
  for (int s = 1; s < NP; s *= 2)
#pragma unroll
    for (int q = 0; q < NP; q += 2 * s) t[q] = opq(t[q] + t[q + s]);
  return t[0];
}
struct Nop { DEV void operator()() const {} };
template <int NP, int NT, int V = 2, class TF = Nop>
DEV void e_sum(float (&y)[4], f32x2 (&x)[NP], const Sy& sy, unsigned a, unsigned ax, int n, TF&& between = Nop{}) {
  f32x4 p[NP];
  auto rd = [&]() {
#pragma unroll
    for (int q = 0; q < NP; ++q) p[q] = lds_ld<f32x4>(a + q * (NT * 1024));
#pragma unroll
    for (int q = 0; q < NP; ++q) x[q] = lds_ld<f32x2>(ax + q * (NT * 1024));
  };
  if constexpr (V == 0) {
    between();
#pragma unroll 1
    for (int it = 0;; ++it) {
      const unsigned c = lds_cnt(sy(C_P + n * 4));
      rd();
      if (__builtin_amdgcn_readfirstlane(c) >= (unsigned)NP || it) break;
      lds_wait(sy(C_P + n * 4), NP);
    }
  } else {
    const unsigned c = lds_cnt(sy(C_P + n * 4));
    rd();
    between();
    if (__builtin_expect(__builtin_amdgcn_readfirstlane(c) < (unsigned)NP, V == 4)) { lds_wait(sy(C_P + n * 4), NP); rd(); }
  }
  lds_inc(sy, C_F + n * 4);
#pragma unroll
  for (int r = 0; r < 4; ++r) {
    float t[NP];
#pragma unroll
    for (int q = 0; q < NP; ++q) t[q] = p[q][r];
    y[r] = tree(t);
  }
}
template <int NP> DEV void e_sum2(float (&y)[2], const f32x2 (&x)[NP]) {
#pragma unroll
  for (int r = 0; r < 2; ++r) {
    float t[NP];
#pragma unroll
    for (int q = 0; q < NP; ++q) t[q] = opq(x[q][r]);
    y[r] = tree(t);
  }
}
DEV void e_cs(float (&c)[6], unsigned a, unsigned ax) {
  f32x4 p[4];
  f32x2 x[4];
#pragma unroll
  for (int q = 0; q < 4; ++q) { p[q] = lds_ld<f32x4>(a + q * 384); x[q] = lds_ld<f32x2>(ax + q * 384); }
#pragma unroll
  for (int r = 0; r < 6; ++r) {
    float t[4];
#pragma unroll
    for (int q = 0; q < 4; ++q) t[q] = opq(r < 4 ? p[q][r & 3] : x[q][r & 1]);
    c[r] = tree(t);
  }
}
DEV void pin7(float& r, float (&c)[6]) { asm volatile("" : "+v"(r), "+v"(c[0]), "+v"(c[1]), "+v"(c[2]), "+v"(c[3]), "+v"(c[4]), "+v"(c[5])); }
struct StatRd { unsigned c; f32x4 a, b; };
DEV StatRd e_stat0(const Sy& sy, int n, int li) {
  StatRd r;
  r.c = lds_cnt(sy(C_S + n * 4));
  r.a = lds_ld<f32x4>(sy.b + li * 32 + (n & 3) * 512); r.b = lds_ld<f32x4>(sy.b + li * 32 + ((n & 3) * 512 + 16));
  return r;
}
template <bool LATE>
DEV void e_stat1(StatRd r, float& sum, float& sq, const Sy& sy, int n, int li) {
  const bool again = __builtin_amdgcn_readfirstlane(r.c) < 4u;
  if (LATE ? __builtin_expect(again, 0) : again) { lds_wait(sy(C_S + n * 4), 4); r = e_stat0(sy, n, li); }
  sum = opq(opq(r.a[0] + r.b[0]) + opq(r.a[2] + r.b[2])); sq = opq(opq(r.a[1] + r.b[1]) + opq(r.a[3] + r.b[3]));
}

template <class TH>
DEV void attention(unsigned lb, int xcd, int w, int l, u32x2& om0, u32x2& ot0, u32x2& om1, u32x2& ot1, TH&& hook) {
  const int g = l >> 4, li = l & 15;
  const unsigned fo = (g < 3 ? g : 2) * 128 + li * 8;
  auto frag = [&](unsigned a) {
    const u32x2 lo = *(const LAS u32x2*)(uintptr_t)a, hi = *(const LAS u32x2*)(uintptr_t)(a + 384);
    return u32x4{lo[0], lo[1], hi[0], hi[1]};
  };
  bf16x8 qf[2];
#pragma unroll
  for (int i = 0; i < 2; ++i) {
    u32x4 v = frag(lb + AT_Q + (2 * w + i) * 640 + fo);
    if (g >= 2) { v[2] = 0u; v[3] = 0u; }
    if (g == 3) { v[0] = 0u; v[1] = 0u; }
    qf[i] = __builtin_bit_cast(bf16x8, v);
  }
  const unsigned Ks = lb + AT_K + 2 * w * 640 + fo;
  const int nneg = xcd ? -1 : 16 - 2 * w;
  const float NEG = -__builtin_inff();
  const f32x4 n4 = {NEG, NEG, NEG, NEG}, z4 = {0.f, 0.f, 0.f, 0.f};
  f32x4 cf, cl, ci;
#pragma unroll
  for (int r = 0; r < 4; ++r) {
    cf[r] = (4 * g + r <= li || nneg > 0) ? NEG : 0.f;
    cl[r] = 4 * g + r > li ? NEG : 0.f;
    ci[r] = nneg > 0 ? NEG : 0.f;
  }
  auto edge = [&](int c) { if (__builtin_expect(c == nneg, 0)) { ci = z4; asm volatile("" : "+v"(ci)); } };
  f32x4 s0[17], s1[17];
  u32x4 kf[4];
  float m0 = NEG, m1 = NEG;
  auto mx = [&](float& m, const f32x4& s) { m = fmaxf(fmaxf(m, s[0]), s[1]); m = opq(fmaxf(fmaxf(m, s[2]), s[3])); };
#pragma unroll
  for (int c = 0; c < 3; ++c) kf[c] = frag(Ks + c * 640);
  SB0;
  static_for<0, 17>([&](auto cc) {
    constexpr int c = cc.value;
    constexpr int nx = c + 3 < 17 ? c + 3 : c + 3 - 16;
    if constexpr (c >= 2 && c < 16 && c % 2 == 0) edge(c);
    s0[c] = mfma16(__builtin_bit_cast(bf16x8, kf[c % 4]), qf[0], c == 0 ? cf : c == 16 ? cl : ci);
    kf[(c + 3) % 4] = frag(Ks + nx * 640);
    if constexpr (c >= 2) mx(m0, s0[c - 2]);
    SB0;
  });
  ci = nneg > 0 ? n4 : z4;
  hook(std::integral_constant<int, 0>{});
  auto qk1 = [&](auto cc) {
    constexpr int c = cc.value;
    if constexpr (c < 17) {
      if constexpr (c < 16 && c % 2 == 1) edge(c + 1);
      s1[c] = mfma16(__builtin_bit_cast(bf16x8, kf[(c + 17) % 4]), qf[1], c == 0 ? cf : c == 16 ? cl : ci);
      if constexpr (c + 3 < 17) kf[(c + 20) % 4] = frag(Ks + (c + 4) * 640);
    }
  };
  qk1(std::integral_constant<int, 0>{});
  mx(m0, s0[15]); mx(m0, s0[16]);
  m0 = max_g(m0);
  const float C2 = 0.22360679774997896f * 1.4426950408889634f;
  const float b0 = -m0 * C2;
  SB0;
  u32x2 p0[18], p1[18];
  auto fm4 = [&](f32x4& s, float b) {
#pragma unroll
    for (int r = 0; r < 4; ++r) s[r] = opq(s[r] * C2 + b);
  };
  auto ex4 = [&](f32x4& s) {
#pragma unroll
    for (int r = 0; r < 4; ++r) s[r] = __builtin_amdgcn_exp2f(s[r]);
  };
  auto cv = [&](const f32x4& s) { return u32x2{pks(s[0], s[1]), pks(s[2], s[3])}; };
  fm4(s0[0], b0);
  SB0;
  static_for<0, 17>([&](auto cc) {
    constexpr int c = cc.value;
    qk1(std::integral_constant<int, c + 1>{});
    ex4(s0[c]);
    SB0;
    if constexpr (c < 16) fm4(s0[c + 1], b0);
    if constexpr (c >= 1) mx(m1, s1[c - 1]);
    p0[c] = cv(s0[c]);
    SB0;
    if constexpr (c % 2 == 1) hook(std::integral_constant<int, 1 + c / 2>{});
  });
  p0[17] = u32x2{0u, 0u}; p1[0] = u32x2{0u, 0u};
  mx(m1, s1[16]);
  m1 = max_g(m1);
  const float b1 = -m1 * C2;
  hook(std::integral_constant<int, 9>{});
  const unsigned Vs0 = lb + AT_V + w * 1280 + l * 16, Vs1 = lb + AT_V + w * 1280 + 1024 + (g * 4 + (li & 3)) * 16;
  u32x4 v0[2], v1[2];
  auto ldv = [&](int s) { v0[s & 1] = *(const LAS u32x4*)(uintptr_t)(Vs0 + s * 1280); v1[s & 1] = *(const LAS u32x4*)(uintptr_t)(Vs1 + s * 1280); };
  f32x4 o00 = z4, o01 = z4, o10 = z4, o11 = z4;
  auto pv = [&](int s, int t) {
    const u32x2* p = t ? p1 : p0;
    const bf16x8 pf = __builtin_bit_cast(bf16x8, u32x4{p[2 * s][0], p[2 * s][1], p[2 * s + 1][0], p[2 * s + 1][1]});
    if (t == 0) { if (li == 4) v1[s & 1] = __builtin_bit_cast(u32x4, ones8()); }
    f32x4 &oa = t ? o10 : o00, &ob = t ? o11 : o01;
    oa = mfma16(__builtin_bit_cast(bf16x8, v0[s & 1]), pf, oa);
    ob = mfma16(__builtin_bit_cast(bf16x8, v1[s & 1]), pf, ob);
  };
  ldv(0);
  fm4(s1[0], b1);
  SB0;
  static_for<0, 17>([&](auto cc) {
    constexpr int c = cc.value;
    if constexpr (c % 2 == 0) pv(c / 2, 0); else { pv(c / 2, 1); ldv(c / 2 + 1); }
    ex4(s1[c]);
    SB0;
    if constexpr (c < 16) fm4(s1[c + 1], b1);
    p1[c + 1] = cv(s1[c]);
    SB0;
    if constexpr (c % 2 == 1) hook(std::integral_constant<int, 10 + c / 2>{});
  });
  pv(8, 1);
  const float r0 = 1.0f / __shfl(o01[0], 16 + li), r1 = 1.0f / __shfl(o11[0], 16 + li);
  om0 = u32x2{pk2(o00[0] * r0, o00[1] * r0), pk2(o00[2] * r0, o00[3] * r0)};
  ot0 = u32x2{pk2(o01[0] * r0, o01[1] * r0), pk2(o01[2] * r0, o01[3] * r0)};
  om1 = u32x2{pk2(o10[0] * r1, o10[1] * r1), pk2(o10[2] * r1, o10[3] * r1)};
  ot1 = u32x2{pk2(o11[0] * r1, o11[1] * r1), pk2(o11[2] * r1, o11[3] * r1)};
}

#define CTX \
  int tid_ = threadIdx.x, b_ = blockIdx.x; \
  asm volatile("" : "+v"(tid_)); asm volatile("" : "+s"(b_)); \
  const int xcd = b_ & 7, jj = b_ >> 3, l = tid_ & 63, g = l >> 4, li = l & 15; \
  const int w = __builtin_amdgcn_readfirstlane(tid_ >> 6) & 7, q = w & 3, grp = w >> 2; \
  const int ct = jj & 7, ct16 = jj & 15; \
  const unsigned vl = l * 16; \
  const int G4 = (xcd * 4 + (jj >> 3)) * 4, dir = (jj >> 3) & 1; \
  const int B8 = (xcd * 2 + (jj >> 4)) * 8; \
  auto g4 = [&](int i) { return G4 + (dir ? 3 - i : i); }; \
  auto g8 = [&](int i) { return B8 + ((i & 1) ? 7 - (i >> 1) : (i >> 1)); }; \
  auto lane = [&](int) { return vl; }; \
  Sy sy{lb + L_SX, 1u}; \
  asm volatile("" : "+v"(sy.b), "+v"(sy.one)); \
  auto ring = [&](int n) { return sy(n >= 2 ? C_F + (n - 2) * 4 : C_OK); }; \
  auto slot2 = [&](int i) { return (unsigned)((i & 1) * SLOT); }; \
  auto slot0 = [&](int i) { return (unsigned)(10240 + (i & 1) * 20480); }; \
  const int wid = jj * 4 + q; \
  auto nohook = [](int) {}; \
  auto ureset = [&](int u) { reset1k(wr, vl, (unsigned)WS_U + (B8 + u / 3) * 49152 + ct16 * 3072 + (u % 3) * 1024); }; \
  auto nomid = [](int, int) {}; \
  auto turn = [&](int i) { return (q >> 1) == (i & 1); }; \
  const unsigned v8 = (g >> 1) * 256 + li * 16 + (g & 1) * 8, v4 = li * 16 + g * 4; \
  auto vt = [&](int i) { return turn(i) ? v4 : OOB; };     \
  const unsigned u5 = (ct * 10 + 2 * q) * 256, u5x = (ct * 10 + 8 + (q & 1)) * 256; \
  const unsigned pe = lb + q * 1024 + vl, px = lb + 4096 + (q & 1) * 512 + v8; \
  const unsigned ce = lb + L_CS + q * 64 + g * 16; \
  const unsigned xo = li * (D_ * 2) + g * 8, xox = li * (D_ * 2) + g * 4; \
  const unsigned xc = ct * 160 + q * 32, xcx = ct * 160 + 128 + (q & 1) * 16; \
  const KRot k4 = krot4(ROT * (jj >> 3)), k3 = krot4(0), k6 = krot6(ROT6 * (jj >> 3)); \
  const unsigned w4b = (unsigned)WS_W4 + ct * (5 * 48 * 1024) + w * 6144; \
  (void)0

#define HW {0, 0, 0, 1, 2, 2, 2, 2, 2, 2, 2, 2, 2, 2, 2, 2, 0, 0}
#define HV 4
#define HT 99
#define TPOS 2
#define PACE 0
#define T3 0
#define T4 0
#define XP 0
#define ROT6 0
#define ROT 0
#define HX 8
#define E2 0
#define E3 25
#define H2 {0, 0, 0, 0, 0, 0, 0, 0, 0, 0, 0, 0, 0, 0, 0, 0, 0, 0}
#define URP 4
#define UH 15
#define RP 25
#define HVA 3
#define S3 {8, 14, 20}
#define S4 {0, 0, 0, 0, 0, 5, 10, 14, 18, 22, 26, 26, 26}
constexpr int hw_[18] = HW, h2_[18] = H2, s3_[3] = S3, s4_[13] = S4;
constexpr int upto(const int (&t)[18], int h) { int s = 0; for (int i = 0; i < h; ++i) s += t[i]; return s; }
static_assert(s3_[2] <= 25 && s4_[10] == 26 && upto(hw_, 18) == 25 && upto(h2_, 18) + E2 + E3 == 25 && upto(h2_, HV + 1) == 0 && HX > HV && HX > HVA, "feed");

__global__ __launch_bounds__(512) void kp_main(const char* x, char* ws, char* out) {
  extern __shared__ __attribute__((aligned(16))) char smem[];
  STDECL;
  asm volatile("mx_kp_begin:");
  STR(0); ST(1);
  const unsigned lb = lds_addr(smem);
  const rsrc_t wr = __builtin_amdgcn_make_buffer_rsrc(ws, 0, (int)WS_END, 0x00020000);
  bf16x8 W[30];
  u32x4 a[3][6];
  int ga, gb;
  {
    CTX;
    const int ko0 = (int)WS_K + jj * (int)QK_HEAD, vo0 = (int)WS_V + jj * (int)V_HEAD;
    const int qo = (int)WS_Q + jj * (int)QK_HEAD + xcd * 16 * 640 + w * 1024;
    const int ko1 = ko0 + (xcd * 16 - 16) * 640 + (w - 10) * 1024, vo1 = vo0 + (xcd * 8 - 8) * 1280 + (w - 30) * 1024;
    static_for<0, 7>([&](auto ii) {
      constexpr int i = ii.value < 4 ? ii.value : ii.value == 4 ? 6 : ii.value - 1;
      const int k = ko1 + i * 8192, v = vo1 + i * 8192, sk = k < ko0 ? ko0 : k, sv = v < vo0 ? vo0 : v;
      int src = i < 2 ? qo + i * 8192 : i < 4 ? sk : sv;
      if constexpr (i == 1) src = w < 2 ? src : sk;
      if constexpr (i == 3) src = w < 6 ? src : sv;
      asm volatile("" : "+s"(src));
      if (i < 6 || w < 2) glds16(ws + (unsigned)src, vl, lb + w * 1024 + i * 8192);
    });
    const int wi = jj * 8 + w;
    ST(20);
    *(LAS unsigned*)(uintptr_t)(lb + L_SY + (tid_ & 127) * 4) = (tid_ & 127) == (C_OK - L_SY) / 4 ? 4u : 0u;
    const unsigned wb = (grp ? (unsigned)WS_W1 : (unsigned)WS_WO) + ct * 102400 + q * 5120;
    auto stage2 = [&](int f) { glds16(ws + (wb + 2 * D_ * D_ * 2 + (f % 5) * 20480 + k4.o[f / 5]), vl, lb + L_W + (q * 25 + f) * 1024); };
    constexpr int NTC = 0;
    if (grp == 0) {
#pragma unroll
      for (int f = 0; f < E2; ++f) stage2(f);
    }
    if (w < 2) wait_vm<3 + E2 + NTC>(); else if (w < 4) wait_vm<2 + E2 + NTC>(); else wait_vm<2 + NTC>();
    __builtin_amdgcn_s_barrier();
    cfence();
    if (grp == 0) {
#pragma unroll
      for (int f = E2; f < E2 + RP; ++f) stage2(f);
#pragma unroll
      for (int i = 0; i < 5; ++i) reset1k(wr, vl, ((q & 2) ? (unsigned)WS_X1 : (unsigned)WS_H) + (xcd * 32 + jj) * 10240 + ((q & 1) * 5 + i) * 1024);
      if constexpr (URP == 2) {
#pragma unroll
        for (int i = 0; i < 6; ++i) ureset(6 * q + i);
      }
      cfence();
#pragma unroll
      for (int f = E2 + RP; f < E2 + E3; ++f) stage2(f);
    }
    cfence();
    ST(2);
    auto feed = [&](auto hi) {
      constexpr int h = hi.value;
      SB0;
      if constexpr (h == 9) { ST(15); lds_wait(lb + C_V, 8); ST(16); }
      if constexpr (h == HT) { if (wi < 100) touch(ws + WS_W2 + (unsigned)(wi * 8192), l, lb); }
      static_for<upto(hw_, h), upto(hw_, h + 1)>([&](auto fi) {
        constexpr int f = fi.value;
        W[f] = __builtin_bit_cast(bf16x8, ld16p(wr, vl, wb + (f % 5) * 20480 + k4.o[f / 5]));
      });
      if constexpr (h2_[h] > 0) {
        if (grp == 0) {
#pragma unroll
          for (int f = E2 + E3 + upto(h2_, h); f < E2 + E3 + upto(h2_, h + 1); ++f) stage2(f);
        }
      }
      if constexpr (h == HVA) {
        if (grp == 0) { asm volatile("s_waitcnt vmcnt(%0)" ::"n"(upto(hw_, h + 1) + E3 - RP) : "memory"); lds_inc(sy, C_V); }
      }
      if constexpr (h == HV) {
        if (grp != 0) { asm volatile("s_waitcnt vmcnt(%0)" ::"n"(upto(hw_, h + 1)) : "memory"); lds_inc(sy, C_V); }
      }
      if constexpr (h == HX) {
        const char* xb = x + (unsigned)((g4(w >> 1) * 16 + (w & 1) * 8) * (D_ * 2) + ct * 160);
        const int c1 = 64 + li;
        glds16(xb, (l / 10) * (D_ * 2) + (l % 10) * 16, lb + w * 1280);
        if (l < 16) glds16(xb, (c1 / 10) * (D_ * 2) + (c1 % 10) * 16, lb + w * 1280 + 1024);
      }
      if constexpr (URP == 1 && h >= UH && h < UH + 3) ureset(3 * w + h - UH);
      if constexpr (h == 0) { ST(14); }
      cfence();
      SB0;
    };
    u32x2 om0, ot0, om1, ot1;
    attention(lb, xcd, w, l, om0, ot0, om1, ot1, feed);
    {
      const auto s0 = __builtin_amdgcn_permlane16_swap(om0[0], om1[0], false, false);
      const auto s1 = __builtin_amdgcn_permlane16_swap(om0[1], om1[1], false, false);
      const auto t0 = __builtin_amdgcn_permlane16_swap(ot0[0], ot1[0], false, false);
      const auto t1 = __builtin_amdgcn_permlane16_swap(ot0[1], ot1[1], false, false);
      const unsigned Of = (unsigned)WS_O + (xcd * 16 + 2 * w) * 20480 + (jj >> 3) * 5120;
      st16(wr, (g & 1) * 20480 + (g >> 1) * 256 + li * 16, Of + (jj & 7) * 512, u32x4{s0[0], s1[0], s0[1], s1[1]});
      st8(wr, g < 2 ? g * 20480 + li * 8 : OOB, Of + 4096 + (jj & 7) * 128, u32x2{t0[0], t1[0]});
    }
    ST(3);
    ga = grp; gb = grp;
    asm volatile("" : "+s"(ga), "+s"(gb));
  }
  if (ga == 0) {
    CTX;
    auto soo = [&](int i) { return (unsigned)WS_O + g4(i) * 20480 + q * 5120; };
    __builtin_amdgcn_s_barrier();
    cfence();
    m_issue<5, 1>(a[0], wr, l, soo(0), vl, k4); m_issue<5, 1>(a[1], wr, l, soo(1), vl, k4);
    ST(17);
    m_phase<5, 5, 4, 1, 3, 2>(W, a, l, lb, sy, wr, 0, q, k4, soo, lane, ring, slot0, nomid,
                              [&](int i) { if (i == 0) lds_inc(sy, C_W2); } STPASS(4));
    DT(7, 2);
    lds_wait(lb + C_F + 3 * 4, 4);
    DT(7, 3);
#pragma unroll
    for (int f = 0; f < 25; ++f) W[f] = *(const LAS bf16x8*)(uintptr_t)(lb + L_W + (q * 25 + f) * 1024 + vl);
    wait_lgkm0();
    DT(7, 4);
    const unsigned w3a = (unsigned)WS_W3 + ct16 * (6 * 20 * 1024) + q * 5120;
    auto stage3 = [&](int f) { glds16(ws + (w3a + (f % 6) * (20 * 1024) + (f / 6) * 1024), vl, lb + L_W + (f < 25 ? q * 25 + f : 100 + q) * 1024); };
#pragma unroll
    for (int f = 0; f < s3_[0]; ++f) stage3(f);
    DT(7, 5);
    const int sr = li - ((q & 2) ? ((q & 1) ? -2 : 2) : ((q & 1) ? -1 : 1));
    const unsigned vg = (unsigned)(((sr >> 4) + 1) * 20480 + g * 256 + (sr & 15) * 16);
    auto sog = [&](int i) { return (unsigned)WS_G + (g4(i) - 1) * 20480 + q * 5120; };
    auto vog = [&](int i) { return (unsigned)(g4(i) * 16 + sr) < (unsigned)S_ ? vg : OOB; };
    float cs[6];
    {
      StatRd sd = e_stat0(sy, 4, li);
      static_for<0, 4>([&](auto i) {
        constexpr int n = 4 + i.value;
        float rr, c[6];
        auto norm = [&]() {
        float sum, sq, mean, var, m1, s1;
        e_stat1<(i.value >= XS)>(sd, sum, sq, sy, n, li);
        if constexpr (i.value == 0) e_cs(cs, ce, lb + L_CS + 256 + (q & 1) * 32 + g * 8);
        DT(6, 2 + 5 * i.value);
        DT(7, 6 + 2 * i.value);
        ln_fast(sum, sq, mean, var, m1, s1);
        const float r1 = __builtin_amdgcn_rcpf(s1);
        float m2 = (mean - m1) * r1, v2 = var * r1 * r1;
        bfr2(m2, v2);
        const float s2 = bfr1(__builtin_amdgcn_sqrtf(bfr1(v2 + 1e-5f)));
        rr = r1 * __builtin_amdgcn_rcpf(s2);
        const float nm = -(m1 + m2 * s1) * rr;
        if (XW || w == 0) *(LAS f32x2*)(uintptr_t)(lb + L_LN + (i * 16 + li) * 8) = f32x2{m1, r1};
#pragma unroll
        for (int r = 0; r < 6; ++r) c[r] = cs[r] * nm;
        pin7(rr, c);
        };
        DT(6, 3 + 5 * i.value);
        float y[4], z[2];
        f32x2 e[4];
        e_sum<4, 5, i.value ? EA1 : 0>(y, e, sy, pe + (n & 1) * SLOT, px + (n & 1) * SLOT, n, norm);
        if constexpr (i.value < 3) sd = e_stat0(sy, n + 1, li);
        if constexpr (i.value >= 2) {
          asm volatile("s_waitcnt vmcnt(%0)" ::"n"(i.value == 2 ? 2 + s3_[2] - s3_[1] : 7) : "memory");
          lds_inc(sy, C_WF);
        }
        if (i == 0) { ST(6); }
        DT(6, 4 + 5 * i.value);
        DT(7, 7 + 2 * i.value);
#pragma unroll
        for (int r = 0; r < 4; ++r) y[r] = y[r] * rr + c[r];
        bfr2(y[0], y[1]); bfr2(y[2], y[3]);
        const unsigned go = (unsigned)WS_G + g4(i) * 20480;
        st8(wr, v8, go + u5, u32x2{pks(gelu(y[0]), gelu(y[1])), pks(gelu(y[2]), gelu(y[3]))});
        unsigned d = 0;
        if (turn(i)) {
          e_sum2(z, e);
          z[0] = z[0] * rr + c[4]; z[1] = z[1] * rr + c[5];
          bfr2(z[0], z[1]);
          d = pks(gelu(z[0]), gelu(z[1]));
        }
        st4(wr, vt(i), go + u5x, d);
        DT(6, 5 + 5 * i.value);
        if constexpr (i.value < 2 && T4) { const int u = i.value * 128 + wid; touch(ws + WS_W4 + (unsigned)((u < 240 ? u : 239) * 8192), l, lb); }
        if constexpr (i.value < 2) {
#pragma unroll
          for (int f = s3_[i.value]; f < s3_[i.value + 1]; ++f) stage3(f);
        }
        if constexpr (i.value == 2) m_issue<5, 0>(a[0], wr, l, sog(0), vog(0), k4);
        DT(6, 6 + 5 * i.value);
      });
    }
    ST(7);
    m_phase<5, 5, 4, 0, 3, 1>(W, a, l, lb, sy, wr, 8, q, k4, sog, vog, [&](int i) { return ring(8 + i); }, slot2,
                              [&](auto i, auto u) {
                                if constexpr (URP == 4 && (i.value == 1 || i.value == 2) && u.value < 3) ureset(6 * q + 3 * (i.value - 1) + u.value);
                                if constexpr (URP == 4 && i.value == 3 && u.value == 4) vm0();
                              }, nohook STPASS(8));
    {
      const unsigned px6 = px + (q >> 1) * 1024, u6 = (ct16 * 12 + 2 * q) * 256, u6x = (ct16 * 12 + 8 + q) * 256;
      StatRd sd = e_stat0(sy, 12, li);
      static_for<0, 8>([&](auto i) {
        constexpr int n = 12 + i.value;
        if constexpr (i.value < 6 && !(XP & 1)) {
#pragma unroll
          for (int j = 0; j < 5; ++j) W[i.value * 5 + j] = __builtin_bit_cast(bf16x8, ld16p(wr, vl, w4b + j * (48 * 1024) + k6.o[i.value]));
          cfence();
        }
        if constexpr (i.value == 7 && URP != 4) m_issue<6, 0>(a[0], wr, l, (unsigned)WS_U + g4(0) * 49152 + w * 6144, vl, k6);
        float rr, c[6];
        auto norm = [&]() {
        float sum, sq, mean, var, m, s;
        e_stat1<(i.value >= XS)>(sd, sum, sq, sy, n, li);
        if constexpr (i.value == 0) e_cs(cs, ce, lb + L_CS + 256 + q * 32 + g * 8);
        DT(10, 2 + 5 * i.value);
        ln_fast(sum, sq, mean, var, m, s);
        rr = __builtin_amdgcn_rcpf(s);
        const float nm = -m * rr;
#pragma unroll
        for (int r = 0; r < 6; ++r) c[r] = cs[r] * nm;
        pin7(rr, c);
        };
        DT(10, 3 + 5 * i.value);
        float y[4], z[2];
        f32x2 e[4];
        e_sum<4, 6, i.value ? EA : 0>(y, e, sy, pe + (n & 1) * SLOT, px6 + (n & 1) * SLOT, n, norm);
        if constexpr (i.value < 7) sd = e_stat0(sy, n + 1, li);
        if constexpr (i.value == 7 && URP == 4) m_issue<6, 0>(a[0], wr, l, (unsigned)WS_U + g4(0) * 49152 + w * 6144, vl, k6);
        if (i == 0) { ST(10); }
        DT(10, 4 + 5 * i.value);
        e_sum2(z, e);
#pragma unroll
        for (int r = 0; r < 4; ++r) y[r] = y[r] * rr + c[r];
        z[0] = z[0] * rr + c[4]; z[1] = z[1] * rr + c[5];
        bfr2(y[0], y[1]); bfr2(y[2], y[3]); bfr2(z[0], z[1]);
        const unsigned uo = (unsigned)WS_U + g8(i) * 49152;
        st8(wr, v8, uo + u6, u32x2{pks(gelu(y[0]), gelu(y[1])), pks(gelu(y[2]), gelu(y[3]))});
        st4(wr, v4, uo + u6x, pks(gelu(z[0]), gelu(z[1])));
        DT(10, 5 + 5 * i.value);
      });
      ST(11);
    }
  }
  if (gb != 0) {
    CTX;
    auto soh = [&](int i) { return (unsigned)WS_H + g4(i) * 20480 + q * 5120; };
    auto sox = [&](int i) { return (unsigned)WS_X1 + g8(i) * 20480 + q * 5120; };
    __builtin_amdgcn_s_barrier();
    wait_vm<2>();
    lds_inc(sy, C_W2);
    ST(17);
    if constexpr (URP == 0) {
#pragma unroll
      for (int i = 0; i < 6; ++i) ureset(6 * q + i);
      cfence();
    }
    m_cs<5>(W, l, lb, q);
    auto add2 = [&](unsigned u, float y0, float y1) { bfr2(y0, y1); return pks(bf_lo(u) + y0, bf_hi(u) + y1); };
    u32x2 xv[4];
    unsigned xx[4];
#pragma unroll
    for (int i = 0; i < 4; ++i) {
      float y[4], z[2];
      f32x2 e[4];
      e_sum<4, 5>(y, e, sy, pe + slot0(i), px + slot0(i), i);
      DT(4, 2 + 4 * i);
      if (i == 0) {
        ST(4);
        lds_wait(lb + C_W2, 8);
#pragma unroll
        for (int t = 0; t < 4; ++t) {
          xv[t] = lds_ld<u32x2>(lb + (t * 16 + li) * 160 + q * 32 + g * 8);
          xx[t] = lds_ld<unsigned>(lb + (t * 16 + li) * 160 + 128 + (q & 1) * 16 + g * 4);
        }
      }
      const unsigned ho = (unsigned)WS_H + g4(i) * 20480;
      xv[i] = u32x2{add2(xv[i][0], y[0], y[1]), add2(xv[i][1], y[2], y[3])};
      DT(4, 3 + 4 * i);
      st8(wr, v8, ho + u5, xv[i]);
      if (turn(i)) {
        e_sum2(z, e);
        xx[i] = add2(xx[i], z[0], z[1]);
      }
      st4(wr, vt(i), ho + u5x, xx[i]);
      DT(4, 4 + 4 * i);
      if (i < 2 && T3) { const int u = i * 128 + wid; touch(ws + WS_W3 + (unsigned)((u < 240 ? u : 239) * 8192), l, lb); }
      if (i == 2) m_issue<5, 2>(a[0], wr, l, soh(0), vl, k4);
      DT(4, 5 + 4 * i);
    }
    ST(5);
    m_phase<5, 5, 4, 2, 3, 1>(W, a, l, lb, sy, wr, 4, q, k4, soh, lane, [&](int i) { return ring(4 + i); }, slot2,
                              [&](auto i, auto u) { if constexpr (URP == 3 && i.value >= 2 && u.value < 3) ureset(6 * q + 3 * (i.value - 2) + u.value); }, nohook STPASS(6));
    auto stage4 = [&](int f) { glds16(ws + (w4b + (f % 5) * (48 * 1024) + k6.o[f / 5]), vl, lb + L_W + (f < 25 ? q * 25 + f : 100 + q) * 1024); };
    {
      const unsigned w3b = (unsigned)WS_W3 + ct16 * (6 * 20 * 1024) + q * 5120;
#pragma unroll
      for (int f = s3_[2]; f < 30; ++f) W[f] = __builtin_bit_cast(bf16x8, ld16p(wr, vl, w3b + (f % 6) * (20 * 1024) + (f / 6) * 1024));
      cfence();
      lds_wait(lb + C_WF, 4);
#pragma unroll
      for (int f = 0; f < s3_[1]; ++f) W[f] = *(const LAS bf16x8*)(uintptr_t)(lb + L_W + (q * 25 + f) * 1024 + vl);
      cfence();
      lds_wait(lb + C_WF, 8);
      ST(19);
      DT(8, 2);
#pragma unroll
      for (int f = s3_[1]; f < s3_[2]; ++f) W[f] = *(const LAS bf16x8*)(uintptr_t)(lb + L_W + (q * 25 + f) * 1024 + vl);
      wait_lgkm0();
#pragma unroll
      for (int f = 0; f < s4_[0]; ++f) stage4(f);
      vm0();
      m_cs<6>(W, l, lb, q);
      ST(18);
      DT(8, 3);
#pragma unroll
      for (int i = 0; i < 4; ++i) {
        const int n = 8 + i;
        float y[4], z[2];
        f32x2 e[4];
        const f32x2 mr = lds_ld<f32x2>(sy(L_LN) + (i * 16 + li) * 8);
        float hq[6];
        auto hb = [&](unsigned u, int o) {
          float p = bf_lo(u) - mr[0], r = bf_hi(u) - mr[0];
          bfr2(p, r);
          p *= mr[1]; r *= mr[1];
          bfr2(p, r);
          hq[o] = p; hq[o + 1] = r;
        };
        float dm = 0.f;
        e_sum<4, 5, 2>(y, e, sy, pe + (n & 1) * SLOT, px + (n & 1) * SLOT, n, [&]() { hb(xv[i][0], 0); hb(xv[i][1], 2); hb(xx[i], 4); pin7(dm, hq); });
        if (i == 0) { ST(8); }
        DT(8, 4 + 3 * i);
        const unsigned xo1 = (unsigned)WS_X1 + g4(i) * 20480;
        bfr2(y[0], y[1]); bfr2(y[2], y[3]);
        st8(wr, v8, xo1 + u5, u32x2{pks(hq[0] + y[0], hq[1] + y[1]), pks(hq[2] + y[2], hq[3] + y[3])});
        if (turn(i)) {
          e_sum2(z, e);
          bfr2(z[0], z[1]);
          xx[i] = pks(hq[4] + z[0], hq[5] + z[1]);
        }
        st4(wr, vt(i), xo1 + u5x, xx[i]);
        DT(8, 5 + 3 * i);
        if (i == 2) { m_issue<5, 2>(a[0], wr, l, sox(0), vl, k3); m_issue<5, 2>(a[1], wr, l, sox(1), vl, k3); }
#pragma unroll
        for (int f = s4_[i]; f < s4_[i + 1]; ++f) stage4(f);
        cfence();
      }
      ST(9);
    }
    m_phase<6, 5, 8, 2, 3, 2>(W, a, l, lb, sy, wr, 12, q, k3, sox, lane, [&](int i) { return ring(12 + i); }, slot2,
                              [&](auto i, auto u) { if constexpr (s4_[4 + i.value] + u.value < s4_[5 + i.value] && !(XP & 2)) stage4(s4_[4 + i.value] + u.value); },
                              nohook STPASS(10));
    m_issue<6, 0>(a[0], wr, l, (unsigned)WS_U + g4(0) * 49152 + w * 6144, vl, k6);
#pragma unroll
    for (int j = 1; j < 5; ++j) W[25 + j] = __builtin_bit_cast(bf16x8, ld16p(wr, vl, w4b + j * (48 * 1024) + k6.o[5]));
#pragma unroll
    for (int f = 0; f < 26; ++f) W[f] = *(const LAS bf16x8*)(uintptr_t)(lb + L_W + (f < 25 ? q * 25 + f : 100 + q) * 1024 + vl);
    cfence();
    lds_inc(sy, C_WC);
  }
  {
    CTX;
    const rsrc_t orr = __builtin_amdgcn_make_buffer_rsrc(out, 0, S_ * D_ * 2, 0x00020000);
    u32x2 hv[2], yv[2];
    unsigned hx, yx;
#pragma unroll
    for (int i = 0; i < 2; ++i) {
      const unsigned to = g4(2 * i + grp) * 20480 + u5;
      hv[i] = ld8(wr, v8, (unsigned)WS_H + to); yv[i] = ld8(wr, v8, (unsigned)WS_X1 + to);
    }
    {
      const unsigned to = g4(2 * (q >> 1) + grp) * 20480 + u5x;
      hx = ld4(wr, v4, (unsigned)WS_H + to); yx = ld4(wr, v4, (unsigned)WS_X1 + to);
    }
    auto fin = [&](int p, auto si) {
      constexpr int s = si.value;
      float y[4], z[2];
      f32x2 e[8];
      float hf[6], uf[6], dm = 0.f;
      auto add3 = [&](int o, float y0, float y1) {
        bfr2(y0, y1);
        y0 += uf[o]; y1 += uf[o + 1];
        bfr2(y0, y1);
        return pks(hf[o] + y0, hf[o + 1] + y1);
      };
      e_sum<8, 5, 2>(y, e, Sy{sy.b + p * 4, sy.one}, pe + (p % 3) * SLOT7, px + (p % 3) * SLOT7, 20, [&]() {
        const unsigned hh[3] = {hv[s][0], hv[s][1], hx}, uu[3] = {yv[s][0], yv[s][1], yx};
#pragma unroll
        for (int r = 0; r < 3; ++r) { hf[2 * r] = bf_lo(hh[r]); hf[2 * r + 1] = bf_hi(hh[r]); uf[2 * r] = bf_lo(uu[r]); uf[2 * r + 1] = bf_hi(uu[r]); }
        pin7(dm, hf); pin7(dm, uf);
      });
      const unsigned oo = g4(p) * (16 * D_ * 2);
      __builtin_amdgcn_raw_buffer_store_b64(u32x2{add3(0, y[0], y[1]), add3(2, y[2], y[3])}, orr, xo, oo + xc, 0);
      if (turn(s)) {
        e_sum2(z, e);
        __builtin_amdgcn_raw_buffer_store_b32(add3(4, z[0], z[1]), orr, xox, oo + xcx, 0);
      }
    };
    m_phase<5, 6, 4, 0, 2, 1>(W, a, l, lb, sy, wr, 20, w, k6, [&](int i) { return (unsigned)WS_U + g4(i) * 49152 + w * 6144; }, lane,
        [&](auto i) { return sy(i.value == 0 ? C_F + 19 * 4 : i.value == 1 ? C_WC : i.value == 2 ? C_OK : C_F + 20 * 4); },
        [&](auto i) { return (unsigned)((i.value % 3) * SLOT7); }, nomid,
        [&](auto i) {
          if constexpr (i.value == 1) { if (grp == 0) fin(0, std::integral_constant<int, 0>{}); }
          if constexpr (i.value == 2) { if (grp == 1) fin(1, std::integral_constant<int, 0>{}); }
          if constexpr (i.value == 3) fin(2 + grp, std::integral_constant<int, 1>{});
        } STPASS(12));
    STDUMP2(ws, 2 + grp);
  }
}
__device__ __attribute__((used, noinline)) void mx_tail() { asm volatile("mx_kp_end:"); }

extern "C" void kernel_launch(void* const* d_in, const int* in_sizes, int n_in, void* d_out, int out_size,
                              void* d_ws, size_t ws_size, hipStream_t stream) {
  const char* x = (const char*)d_in[0];
  const char* wqkv = (const char*)d_in[1];
  const char* wo = (const char*)d_in[2];
  const char* w1 = (const char*)d_in[4];
  const char* w2 = (const char*)d_in[5];
  const char* w3 = (const char*)d_in[6];
  const char* w4 = (const char*)d_in[7];
  char* ws = (char*)d_ws;
  if (ws_size < WS_END) return;
  (void)hipFuncSetAttribute((const void*)k1_qkv, hipFuncAttributeMaxDynamicSharedMemorySize, LDS_TOT);
  (void)hipFuncSetAttribute((const void*)kp_main, hipFuncAttributeMaxDynamicSharedMemorySize, LDS_TOT);
  k1_qkv<<<256, 384, LDS_TOT, stream>>>(x, wqkv, wo, w1, w2, w3, w4, ws);
  kp_main<<<256, 512, LDS_TOT, stream>>>(x, ws, (char*)d_out);
}
```
